```python
import torch

def _softmax(x, axis=-1):
    return torch.softmax(x, dim=axis)
def _gelu(x):
    return torch.nn.functional.gelu(x, approximate="tanh")
def _silu(x):
    return torch.nn.functional.silu(x)
def _sigmoid(x):
    return torch.sigmoid(x)
def _layernorm(x, axis=-1, eps=1e-5):
    m = x.mean(dim=axis, keepdim=True)
    v = x.var(dim=axis, keepdim=True, unbiased=False)
    return (x - m) / torch.sqrt(v + eps)
def _scatter_add(tgt, idx, src, axis=0):
    return tgt.clone().index_add_(axis, idx, src)
def _segment_sum(src, seg, n):
    out = torch.zeros((n,) + src.shape[1:], dtype=src.dtype)
    return out.index_add_(0, seg, src)
def _pad(x, pw):
    flat = tuple(v for pair in reversed(pw) for v in pair)
    return torch.nn.functional.pad(x, flat)

kernel_name = 'recurrent_scan__mha_block_cached__moe_arch_mod__decode'

def setup_inputs():

    _w = (lambda a: (lambda t: t.to(torch.bfloat16) if t.is_floating_point() else t)(torch.as_tensor(a)))
    def _A_setup():
        B, Sq, D = 2, 1, 640
        H, Sk, Dh = 32, 8192, 192
        return {
            "x": _w((lambda *s: torch.randn(*s, dtype=torch.bfloat16) * 0.05)(B, Sq, D)),
            "k_cache": _w((lambda *s: torch.randn(*s, dtype=torch.bfloat16) * 1.6)(B, H, Sk, Dh)),
            "v_cache": _w((lambda *s: torch.randn(*s, dtype=torch.bfloat16) * 0.2)(B, H, Sk, Dh)),
            "wq": _w((lambda *s: torch.randn(*s, dtype=torch.bfloat16) * 1.6)(D, H * Dh) * (D ** -0.5)),
            "wo": _w((lambda *s: torch.randn(*s, dtype=torch.bfloat16) * 0.2)(H * Dh, D) * ((H * Dh) ** -0.5)),
        }
    _A_in = _A_setup()

    def _B_setup():
        B, S, D, F, C = 2, 1, 640, 1536, 1
        return {
            "x": _w((lambda *s: torch.randn(*s, dtype=torch.bfloat16) * 0.05)(B, S, D)),
            "w_r": _w((lambda *s: torch.randn(*s, dtype=torch.bfloat16) * 0.1)(D)),
            "w_gate": _w((lambda *s: torch.randn(*s, dtype=torch.bfloat16) * 0.1)(D, F)),
            "w_up": _w((lambda *s: torch.randn(*s, dtype=torch.bfloat16) * 0.1)(D, F)),
            "w_down": _w((lambda *s: torch.randn(*s, dtype=torch.bfloat16) * 0.1)(F, D)),
        }
    _B_in = _B_setup()

    return {
        "A_x": _A_in["x"],
        "A_k_cache": _A_in["k_cache"],
        "A_v_cache": _A_in["v_cache"],
        "A_wq": _A_in["wq"],
        "A_wo": _A_in["wo"],
        "B_x": _B_in["x"],
        "B_w_r": _B_in["w_r"],
        "B_w_gate": _B_in["w_gate"],
        "B_w_up": _B_in["w_up"],
        "B_w_down": _B_in["w_down"],
    }

def reference(A_x, A_k_cache, A_v_cache, A_wq, A_wo, B_x, B_w_r, B_w_gate, B_w_up, B_w_down):
    def _A_ref(x, k_cache, v_cache, wq, wo):
        B, Sq, D = x.shape
        H, Dh = 32, 192
        q = (lambda x, *d: x.permute(*d))((x @ wq).reshape(B, Sq, H, Dh), 0, 2, 1, 3)
        scores = torch.einsum("bhqd,bhkd->bhqk", q, k_cache) * (Dh ** -0.5)
        p = _softmax(scores, axis=-1)
        o = torch.einsum("bhqk,bhkd->bhqd", p, v_cache)
        return (lambda x, *d: x.permute(*d))(o, 0, 2, 1, 3).reshape(B, Sq, H * Dh) @ wo

    def _B_ref(x, w_r, w_gate, w_up, w_down):
        B, S, D = x.shape
        C = 1
        r = x @ w_r
        idx = torch.argsort(-r, axis=-1)[:, :C]
        idx_e = (lambda x, n, axis: torch.repeat_interleave(x, n, dim=axis))(idx.reshape(B, C, 1), D, axis=2)
        x_sel = (lambda x, i, axis: torch.take_along_dim(x, i, dim=axis))(x, idx_e, axis=1)
        g = _silu(x_sel @ w_gate)
        u = x_sel @ w_up
        y = (g * u) @ w_down
        r_sel = _sigmoid((lambda x, i, axis: torch.take_along_dim(x, i, dim=axis))(r, idx, axis=1))
        y = y * r_sel.reshape(B, C, 1)
        b_off = (torch.arange(B) * S).reshape(B, 1)
        flat_idx = (idx + b_off).reshape(B * C)
        zero = (x * 0.0).reshape(B * S, D)
        upd = _scatter_add(zero, flat_idx, y.reshape(B * C, D), axis=0)
        return x + upd.reshape(B, S, D)

    x = A_x
    xn = _layernorm(x, axis=-1)
    s = _A_ref(xn, A_k_cache, A_v_cache, A_wq, A_wo)
    g = _silu(_B_ref(xn, B_w_r, B_w_gate, B_w_up, B_w_down))
    return x + s * g
```

```cpp
#include <hip/hip_runtime.h>
#include <hip/hip_bf16.h>

namespace {

#define GAS __attribute__((address_space(1)))
#define LAS __attribute__((address_space(3)))
#define RLX_AGENT __ATOMIC_RELAXED, __HIP_MEMORY_SCOPE_AGENT
#define RLX_WG __ATOMIC_RELAXED, __HIP_MEMORY_SCOPE_WORKGROUP
typedef unsigned v4u __attribute__((ext_vector_type(4)));
typedef unsigned v2u __attribute__((ext_vector_type(2)));
typedef float v4f __attribute__((ext_vector_type(4)));
typedef float v2f __attribute__((ext_vector_type(2)));
typedef short v4s __attribute__((ext_vector_type(4)));
typedef __bf16 bf16x2 __attribute__((ext_vector_type(2)));
typedef __bf16 bf16x8 __attribute__((ext_vector_type(8)));
typedef unsigned long long u64;

#define K_INF 21
#define K_NSLOT 22
#define K_WQ_AUX 2

constexpr int DM = 640, NH = 32, DH = 192, SK = 8192, FF = 1536;
constexpr int NWG = 256, NC = 6, NTHR = (NC + 2) * 64;
constexpr int INF = K_INF;
constexpr int NSLOT = K_NSLOT;
constexpr int SLOTB = 6144, NKV = 256, NWO = 5;
constexpr unsigned KV_BYTES = 2u * NH * SK * DH * 2u;
constexpr int SLAB = SK * DH * 2;
constexpr int NMLP = FF / 64;
constexpr int NFIN = 2 * DM / 8;
constexpr int WO_POS = NKV % NSLOT;
static_assert(WO_POS + NWO <= NSLOT, "the wo image must not wrap around the ring");
static_assert(INF >= 7 && INF <= 60, "vmcnt is 6 bits");

constexpr int L_XP = NSLOT * SLOTB;
constexpr int L_QP = L_XP + 2816;
constexpr int L_QB = L_QP + NC * 384 * 4;
constexpr int L_REC = L_QB + 768;
constexpr int L_OA = L_REC + 2 * NC * 200 * 4;
constexpr int L_HM = L_OA + 768;
constexpr int L_CTL = L_HM + 256;
constexpr int L_END = L_CTL + 128;
static_assert(L_END <= 160 * 1024 && L_END > 80 * 1024, "one workgroup per CU");
enum { C_LANDED = 0, C_Q1 = 1, C_Q2 = 2, C_B0 = 3, C_B1 = 4, C_O = 5, C_T3 = 6, C_TAG = 7, C_TAGOK = 8, C_NEXT = 16 };

constexpr int W_EPOCH = 0;
constexpr int W_ML = 1024;
constexpr int W_OP = W_ML + 2 * NH * 8 * 16;
constexpr int W_SP = W_OP + 2 * NH * 8 * DH * 8;
constexpr int W_YP = W_SP + NFIN * NH * 64;
constexpr int W_END = W_YP + NFIN * NMLP * 64;

constexpr unsigned LDS_SPIN = 1u << 20, GLB_SPIN = 1u << 16;
constexpr float LOG2E = 1.4426950408889634f;

struct Params {
  const unsigned short *x, *k, *v, *wq, *wo, *wr, *wg, *wu, *wd;
  unsigned short* out;
  unsigned char* ws;
};

__device__ __forceinline__ int rfl(int v) { return __builtin_amdgcn_readfirstlane(v); }
__device__ __forceinline__ float bfr(float x) { return (float)(__bf16)x; }
__device__ __forceinline__ unsigned bfbits(float x) { return (unsigned)__builtin_bit_cast(unsigned short, (__bf16)x); }
__device__ __forceinline__ float lo16(unsigned u) { return __builtin_bit_cast(float, u << 16); }
__device__ __forceinline__ float hi16(unsigned u) { return __builtin_bit_cast(float, u & 0xffff0000u); }
__device__ __forceinline__ float asf(unsigned u) { return __builtin_bit_cast(float, u); }
__device__ __forceinline__ unsigned asu(float f) { return __builtin_bit_cast(unsigned, f); }
__device__ __forceinline__ float dot2(unsigned a, unsigned b, float c) {
  return __builtin_amdgcn_fdot2_f32_bf16(__builtin_bit_cast(bf16x2, a), __builtin_bit_cast(bf16x2, b), c, false);
}
template <int CTRL> __device__ __forceinline__ float dpp(float x) {
  return __builtin_bit_cast(float, __builtin_amdgcn_mov_dpp(__builtin_bit_cast(int, x), CTRL, 0xf, 0xf, true));
}
template <int CTRL> __device__ __forceinline__ int dppi(int x) { return __builtin_amdgcn_mov_dpp(x, CTRL, 0xf, 0xf, true); }
constexpr int XOR1 = 0xB1, XOR2 = 0x4E, MIR8 = 0x141, ROR4 = 0x124, ROR8 = 0x128;
__device__ __forceinline__ float fold32(float a, float b) {
  auto s = __builtin_amdgcn_permlane32_swap(asu(a), asu(b), false, false);
  return asf(s[0]) + asf(s[1]);
}
__device__ __forceinline__ float fold16(float a, float b) {
  auto s = __builtin_amdgcn_permlane16_swap(asu(a), asu(b), false, false);
  return asf(s[0]) + asf(s[1]);
}
__device__ __forceinline__ float max_rows(float x) {
  auto s = __builtin_amdgcn_permlane16_swap(asu(x), asu(x), false, false);
  x = fmaxf(asf(s[0]), asf(s[1]));
  auto t = __builtin_amdgcn_permlane32_swap(asu(x), asu(x), false, false);
  return fmaxf(asf(t[0]), asf(t[1]));
}
__device__ __forceinline__ float sum_rows(float x) { x = fold16(x, x); return fold32(x, x); }
__device__ __forceinline__ float wave_sum(float x) {
  x += dpp<XOR1>(x); x += dpp<XOR2>(x); x += dpp<ROR4>(x); x += dpp<ROR8>(x);
  return sum_rows(x);
}
__device__ __forceinline__ float exp2f_(float x) { return __builtin_amdgcn_exp2f(x); }
__device__ __forceinline__ float siluf(float x) { return x / (1.0f + expf(-x)); }
#define CBAR() asm volatile("" ::: "memory")

__device__ __forceinline__ __amdgpu_buffer_rsrc_t rsrc(const void* p, unsigned bytes) {
  return __builtin_amdgcn_make_buffer_rsrc((void*)p, 0, bytes, 0x00020000);
}
__device__ __forceinline__ void lds_arrive(LAS unsigned* w, int lane) {
  CBAR();
  if (lane == 0) __hip_atomic_fetch_add(w, 1u, RLX_WG);
  CBAR();
}
template <int SLEEP> __device__ __forceinline__ void lds_wait(LAS unsigned* w, unsigned want) {
#pragma nounroll
  for (unsigned n = 0; n < LDS_SPIN; ++n) {
    if ((unsigned)rfl((int)__hip_atomic_load(w, RLX_WG)) >= want) break;
    __builtin_amdgcn_s_sleep(SLEEP);
  }
  CBAR();
}
__device__ __forceinline__ void st_gran(unsigned char* ws, int off, float v, unsigned tag) {
  __hip_atomic_store((GAS u64*)(ws + off), ((u64)tag << 32) | asu(v), RLX_AGENT);
}
__device__ __forceinline__ u64 ld_gran(const unsigned char* ws, int off) { return __hip_atomic_load((GAS u64*)(ws + off), RLX_AGENT); }

#define STAMP(i) do { } while (0)

__device__ __forceinline__ void load_x(const Params& p, unsigned (&xr)[2][5], int lane) {
  CBAR();
#pragma unroll
  for (int b = 0; b < 2; ++b)
#pragma unroll
    for (int i = 0; i < 5; ++i) xr[b][i] = ((const GAS unsigned*)p.x)[b * 320 + lane + 64 * i];
}
__device__ __forceinline__ void layer_norm(unsigned (&xr)[2][5], LAS unsigned char* L, int lane) {
  asm volatile("" : "+v"(xr[0][0]), "+v"(xr[0][1]), "+v"(xr[0][2]), "+v"(xr[0][3]), "+v"(xr[0][4]),
                    "+v"(xr[1][0]), "+v"(xr[1][1]), "+v"(xr[1][2]), "+v"(xr[1][3]), "+v"(xr[1][4]));
  unsigned xn[2][5][2];
#pragma unroll
  for (int b = 0; b < 2; ++b) {
    float s = 0.f;
#pragma unroll
    for (int i = 0; i < 5; ++i) s += lo16(xr[b][i]) + hi16(xr[b][i]);
    const float mean = wave_sum(s) * (1.0f / DM);
    float q = 0.f;
#pragma unroll
    for (int i = 0; i < 5; ++i) { const float a = lo16(xr[b][i]) - mean, c = hi16(xr[b][i]) - mean; q += a * a + c * c; }
    const float var = bfr(wave_sum(q) * (1.0f / DM)), mb = bfr(mean);
    const float den = bfr(sqrtf(bfr(var + 1e-5f)));
    const float rcp = 1.0f / den;
    auto quot = [&](float a) { const float q0 = a * rcp; return fmaf(fmaf(-q0, den, a), rcp, q0); };
#pragma unroll
    for (int i = 0; i < 5; ++i) {
      xn[b][i][0] = bfbits(quot(bfr(lo16(xr[b][i]) - mb)));
      xn[b][i][1] = bfbits(quot(bfr(hi16(xr[b][i]) - mb)));
    }
  }
#pragma unroll
  for (int i = 0; i < 5; ++i)
    *(LAS v2u*)(L + L_XP + (lane + 64 * i) * 8) = v2u{xn[0][i][0] | (xn[1][i][0] << 16), xn[0][i][1] | (xn[1][i][1] << 16)};
  asm volatile("s_waitcnt lgkmcnt(0)" ::: "memory");
}

template <int ND, int SLEEP>
__device__ __forceinline__ void combine(const Params& p, LAS unsigned char* L, int b, int h, int m, int d0, bool lead, unsigned tag, int lane) {
  const LAS float* rec = (const LAS float*)(L + L_REC) + b * NC * 200;
  float M = rec[192];
#pragma unroll
  for (int c = 1; c < NC; ++c) M = fmaxf(M, rec[c * 200 + 192]);
  float Ls = 0.f, o[ND];
#pragma unroll
  for (int i = 0; i < ND; ++i) o[i] = 0.f;
#pragma unroll
  for (int c = 0; c < NC; ++c) {
    const float w = exp2f_(rec[c * 200 + 192] - M);
    Ls = fmaf(w, rec[c * 200 + 193], Ls);
#pragma unroll
    for (int i = 0; i < ND; ++i) o[i] = fmaf(w, rec[c * 200 + d0 + 64 * i], o[i]);
  }
  const int bh = b * NH + h;
  const __amdgpu_buffer_rsrc_t rw = rsrc(p.ws, W_END);
#pragma unroll
  for (int i = 0; i < ND; ++i) st_gran(p.ws, W_OP + ((bh * 8 + m) * DH + d0 + 64 * i) * 8, o[i], tag);
  if (lead && lane == 0) __builtin_amdgcn_raw_buffer_store_b128(v4u{asu(M), tag, asu(Ls), tag}, rw, W_ML + (bh * 8 + m) * 16, 0, 16);
  u64 g[ND][8];
  v4u ml;
#pragma nounroll
  for (unsigned n = 0; n < GLB_SPIN; ++n) {
    CBAR();
    ml = __builtin_amdgcn_raw_buffer_load_b128(rw, W_ML + (bh * 8 + (lane & 7)) * 16, 0, 16);
#pragma unroll
    for (int i = 0; i < ND; ++i)
#pragma unroll
      for (int j = 0; j < 8; ++j) g[i][j] = ld_gran(p.ws, W_OP + ((bh * 8 + j) * DH + d0 + 64 * i) * 8);
    bool ok = ml.y == tag && ml.w == tag;
#pragma unroll
    for (int i = 0; i < ND; ++i)
#pragma unroll
      for (int j = 0; j < 8; ++j) ok &= (unsigned)(g[i][j] >> 32) == tag;
    if (__all(ok)) break;
    if (SLEEP) __builtin_amdgcn_s_sleep(SLEEP);
  }
  float Mg = asf(ml.x);
  Mg = fmaxf(Mg, dpp<XOR1>(Mg)); Mg = fmaxf(Mg, dpp<XOR2>(Mg)); Mg = fmaxf(Mg, dpp<MIR8>(Mg));
  const float w = exp2f_(asf(ml.x) - Mg);
  float Lg = w * asf(ml.z);
  Lg += dpp<XOR1>(Lg); Lg += dpp<XOR2>(Lg); Lg += dpp<MIR8>(Lg);
  const float wn = w / Lg;
#pragma unroll
  for (int i = 0; i < ND; ++i) {
    float a = 0.f;
#pragma unroll
    for (int j = 0; j < 8; ++j) a = fmaf(asf((unsigned)__builtin_amdgcn_readlane((int)asu(wn), j)), asf((unsigned)g[i][j]), a);
    *(LAS unsigned short*)(L + L_OA + (b * DH + d0 + 64 * i) * 2) = (unsigned short)bfbits(a);
  }
}

template <int NP, int SLEEP>
__device__ __forceinline__ v2f gather8(const Params& p, int base, unsigned tag, int lane) {
  static_assert(NP > 16 && NP <= 32, "two 16-byte pieces per lane");
  const __amdgpu_buffer_rsrc_t rw = rsrc(p.ws, W_END);
  const bool two = 64 + lane < NP * 4;
  v4u a, c;
#pragma nounroll
  for (unsigned n = 0; n < GLB_SPIN; ++n) {
    CBAR();
    a = __builtin_amdgcn_raw_buffer_load_b128(rw, base + lane * 16, 0, 16);
    c = __builtin_amdgcn_raw_buffer_load_b128(rw, base + (two ? 64 + lane : lane) * 16, 0, 16);
    if (__all(a.y == tag && a.w == tag && c.y == tag && c.w == tag)) break;
    if (SLEEP) __builtin_amdgcn_s_sleep(SLEEP);
  }
  float s0 = asf(a.x) + (two ? asf(c.x) : 0.f), s1 = asf(a.z) + (two ? asf(c.z) : 0.f);
  s0 += dpp<ROR4>(s0); s1 += dpp<ROR4>(s1);
  s0 += dpp<ROR8>(s0); s1 += dpp<ROR8>(s1);
  return v2f{sum_rows(s0), sum_rows(s1)};
}

template <int N> __device__ __forceinline__ void dma3(int voff, v4u srd, int soff, unsigned lds) {
  asm volatile("s_mov_b32 m0, %3\n\t"
               "s_waitcnt vmcnt(%4)\n\t"
               "buffer_load_dwordx4 %0, %1, %2 offen nt lds\n\t"
               "s_waitcnt vmcnt(%4)\n\t"
               "buffer_load_dwordx4 %0, %1, %2 offen offset:1024 nt lds\n\t"
               "s_waitcnt vmcnt(%4)\n\t"
               "buffer_load_dwordx4 %0, %1, %2 offen offset:2048 nt lds"
               :: "v"(voff), "s"(srd), "s"(soff), "s"(lds), "i"(N) : "memory");
}
template <int N> __device__ __forceinline__ void dma1(int voff, v4u srd, unsigned lds) {
  asm volatile("s_mov_b32 m0, %2\n\t"
               "s_waitcnt vmcnt(%3)\n\t"
               "buffer_load_dwordx4 %0, %1, 0 offen nt lds"
               :: "v"(voff), "s"(srd), "s"(lds), "i"(N) : "memory");
}
__device__ __forceinline__ v4u srd_of(const void* ptr, unsigned bytes) {
  const u64 a = (u64)ptr;
  return v4u{(unsigned)a, (unsigned)(a >> 32) & 0xffffu, bytes, 0x00020000u};
}
constexpr int fdiv6(int a) { return a >= 0 ? a / 6 : -((5 - a) / 6); }

__global__ void __launch_bounds__(NTHR, 2) fused_decode(Params p) {
  extern __shared__ __attribute__((aligned(16))) unsigned char smem[];
  LAS unsigned char* L = (LAS unsigned char*)smem;
  LAS unsigned* ctl = (LAS unsigned*)(L + L_CTL);
  const int tid = threadIdx.x, lane = tid & 63, wave = rfl(tid >> 6);
  const int bid = blockIdx.x, h = bid & 31, m = bid >> 5;
  const int r = lane >> 3, j = lane & 7;

  if (wave == 0) {
    __builtin_amdgcn_s_setprio(3);
    STAMP(0);
    const v4u rk = srd_of(p.k, KV_BYTES), rv = srd_of(p.v, KV_BYTES), ro = srd_of(p.wo, NH * DH * DM * 2);
    const unsigned lbase = (unsigned)(size_t)L;
    const int voff = lane * 16;
    int pos = 0, limit = NSLOT;
    auto credit = [&](int s) {
      if (s >= limit) {
#pragma nounroll
        for (unsigned n = 0; n < LDS_SPIN; ++n) {
          int v = (int)__hip_atomic_load(ctl + C_NEXT + (j < NC ? j : 0), RLX_WG);
          v = min(v, dppi<XOR1>(v)); v = min(v, dppi<XOR2>(v)); v = min(v, dppi<MIR8>(v));
          limit = rfl(v) + NSLOT;
          if (s < limit) break;
          __builtin_amdgcn_s_sleep(1);
        }
        CBAR();
      }
    };
    auto publish = [&](int n) { if (n > 0) __hip_atomic_store(ctl + C_LANDED, (unsigned)n, RLX_WG); };
    auto kv_off = [&](int s) { const int i = s & 127; return ((s >> 7) * NH + h) * SLAB + ((i >> 2) * 32 + m * 4 + (i & 3)) * 3072; };
#pragma unroll
    for (int s = 0; s < 3; ++s) {
      dma3<INF - 1>(voff, rk, kv_off(s), lbase + s * SLOTB);
      dma3<INF - 1>(voff, rv, kv_off(s), lbase + s * SLOTB + 3072);
    }
    pos = 3;
    __builtin_amdgcn_s_barrier();
    CBAR();
#pragma nounroll
    for (int s = 3; s < NKV; ++s) {
      credit(s);
      const int so = kv_off(s);
      const unsigned dst = lbase + pos * SLOTB;
      dma3<INF - 1>(voff, rk, so, dst);
      publish(s + fdiv6(3 - INF));
      dma3<INF - 1>(voff, rv, so, dst + 3072);
      if (fdiv6(6 - INF) != fdiv6(3 - INF)) publish(s + fdiv6(6 - INF));
      pos = pos + 1 == NSLOT ? 0 : pos + 1;
    }
    STAMP(1);
    int row = lane / 10, ch = lane - row * 10;
#pragma nounroll
    for (int s = NKV; s < NKV + NWO; ++s) {
      credit(s);
#pragma unroll
      for (int q = 0; q < 6; ++q) {
        dma1<INF - 1>((h * DH + row) * (DM * 2) + m * 160 + ch * 16, ro, lbase + (WO_POS + s - NKV) * SLOTB + q * 1024);
        if (q == 2) publish(s + fdiv6(3 - INF));
        if (q == 5 && fdiv6(6 - INF) != fdiv6(3 - INF)) publish(s + fdiv6(6 - INF));
        row += 6; ch += 4;
        if (ch >= 10) { ch -= 10; row += 1; }
      }
    }
    asm volatile("s_waitcnt vmcnt(0)" ::: "memory");
    publish(NKV + NWO);
    STAMP(2);
    return;
  }

  if (wave == NC + 1) {
    unsigned xr[2][5];
    load_x(p, xr, lane);
    const unsigned e0 = __hip_atomic_load((GAS unsigned*)(p.ws + W_EPOCH), RLX_AGENT);
    if (lane < 32) ctl[lane] = lane >= C_NEXT ? lane - C_NEXT : 0u;
    ((LAS unsigned*)(L + L_XP))[640 + lane] = 0u;
    asm volatile("s_waitcnt lgkmcnt(0)" ::: "memory");
    __builtin_amdgcn_s_barrier();
    CBAR();
    const unsigned tag = (unsigned)rfl((int)e0) + 1u;
    if (lane == 0) { ctl[C_TAG] = tag; CBAR(); __hip_atomic_store(ctl + C_TAGOK, 1u, RLX_WG); }
    layer_norm(xr, L, lane);
    const LAS unsigned* xp = (const LAS unsigned*)(L + L_XP);
    const __amdgpu_buffer_rsrc_t rw = rsrc(p.ws, W_END);
    if (bid < NMLP) {
      const __amdgpu_buffer_rsrc_t rg = rsrc(p.wg, DM * FF * 2), ru = rsrc(p.wu, DM * FF * 2), rd = rsrc(p.wd, FF * DM * 2);
      float a[32];
#pragma unroll
      for (int i = 0; i < 32; ++i) a[i] = 0.f;
      const int vo = r * (FF * 2) + j * 16;
#pragma nounroll
      for (int t0 = 0; t0 < 80; t0 += 8) {
        v4u wgv[8], wuv[8];
#pragma unroll
        for (int t = 0; t < 8; ++t) {
          const int so = (t0 + t) * (8 * FF * 2) + bid * 128;
          wgv[t] = __builtin_amdgcn_raw_buffer_load_b128(rg, vo, so, 2);
          wuv[t] = __builtin_amdgcn_raw_buffer_load_b128(ru, vo, so, 2);
        }
        __builtin_amdgcn_sched_barrier(0);
#pragma unroll
        for (int t = 0; t < 8; ++t) {
          const unsigned xx = xp[(t0 + t) * 8 + r];
          const unsigned x0l = xx & 0xffffu, x0h = xx << 16, x1l = xx >> 16, x1h = xx & 0xffff0000u;
#pragma unroll
          for (int e = 0; e < 4; ++e) {
            a[2 * e] = dot2(wgv[t][e], x0l, a[2 * e]);           a[2 * e + 1] = dot2(wgv[t][e], x0h, a[2 * e + 1]);
            a[8 + 2 * e] = dot2(wgv[t][e], x1l, a[8 + 2 * e]);   a[9 + 2 * e] = dot2(wgv[t][e], x1h, a[9 + 2 * e]);
            a[16 + 2 * e] = dot2(wuv[t][e], x0l, a[16 + 2 * e]); a[17 + 2 * e] = dot2(wuv[t][e], x0h, a[17 + 2 * e]);
            a[24 + 2 * e] = dot2(wuv[t][e], x1l, a[24 + 2 * e]); a[25 + 2 * e] = dot2(wuv[t][e], x1h, a[25 + 2 * e]);
          }
        }
      }
#pragma unroll
      for (int i = 0; i < 16; ++i) a[i] = fold32(a[i], a[i + 16]);
#pragma unroll
      for (int i = 0; i < 8; ++i) { a[i] = fold16(a[i], a[i + 8]); a[i] += dpp<ROR8>(a[i]); }
#pragma unroll
      for (int i = 0; i < 8; ++i) {
        auto s = __builtin_amdgcn_permlane32_swap(asu(a[i]), asu(a[i]), false, false);
        const float hm = bfr(siluf(bfr(asf(s[0])))) * bfr(asf(s[1]));
        if (lane < 32 && (r & 1) == 0) *(LAS unsigned short*)(L + L_HM + (8 * j + i) * 4 + (lane >> 4) * 2) = (unsigned short)bfbits(hm);
      }
      asm volatile("s_waitcnt lgkmcnt(0)" ::: "memory");
      const LAS unsigned* hp = (const LAS unsigned*)(L + L_HM);
      const unsigned hv = hp[lane];
      float ym[16], ye[16];
#pragma unroll
      for (int i = 0; i < 16; ++i) { ym[i] = 0.f; ye[i] = 0.f; }
      const int ve = (lane >> 4) * (DM * 2) + 1024 + (lane & 15) * 16;
#pragma nounroll
      for (int f0 = 0; f0 < 64; f0 += 16) {
        v4u wm[16], we[4];
#pragma unroll
        for (int f = 0; f < 16; ++f) wm[f] = __builtin_amdgcn_raw_buffer_load_b128(rd, lane * 16, (bid * 64 + f0 + f) * (DM * 2), 2);
#pragma unroll
        for (int t = 0; t < 4; ++t) we[t] = __builtin_amdgcn_raw_buffer_load_b128(rd, ve, (bid * 64 + f0 + 4 * t) * (DM * 2), 2);
        __builtin_amdgcn_sched_barrier(0);
#pragma unroll
        for (int f = 0; f < 16; ++f) {
          const unsigned xx = (unsigned)__builtin_amdgcn_readlane((int)hv, f0 + f);
          const unsigned x0l = xx & 0xffffu, x0h = xx << 16, x1l = xx >> 16, x1h = xx & 0xffff0000u;
#pragma unroll
          for (int e = 0; e < 4; ++e) {
            ym[2 * e] = dot2(wm[f][e], x0l, ym[2 * e]);         ym[2 * e + 1] = dot2(wm[f][e], x0h, ym[2 * e + 1]);
            ym[8 + 2 * e] = dot2(wm[f][e], x1l, ym[8 + 2 * e]); ym[9 + 2 * e] = dot2(wm[f][e], x1h, ym[9 + 2 * e]);
          }
          __builtin_amdgcn_sched_barrier(0);
        }
#pragma unroll
        for (int t = 0; t < 4; ++t) {
          const unsigned xx = hp[f0 + 4 * t + (lane >> 4)];
          const unsigned x0l = xx & 0xffffu, x0h = xx << 16, x1l = xx >> 16, x1h = xx & 0xffff0000u;
#pragma unroll
          for (int e = 0; e < 4; ++e) {
            ye[2 * e] = dot2(we[t][e], x0l, ye[2 * e]);         ye[2 * e + 1] = dot2(we[t][e], x0h, ye[2 * e + 1]);
            ye[8 + 2 * e] = dot2(we[t][e], x1l, ye[8 + 2 * e]); ye[9 + 2 * e] = dot2(we[t][e], x1h, ye[9 + 2 * e]);
          }
        }
      }
#pragma unroll
      for (int i = 0; i < 16; ++i) ye[i] = sum_rows(ye[i]);
#pragma unroll
      for (int b = 0; b < 2; ++b)
#pragma unroll
        for (int e = 0; e < 4; ++e) {
          __builtin_amdgcn_raw_buffer_store_b128(v4u{asu(ym[8 * b + 2 * e]), tag, asu(ym[8 * b + 2 * e + 1]), tag}, rw,
                                                 W_YP + ((b * 80 + lane) * NMLP + bid) * 64 + e * 16, 0, 16);
          if (lane < 16)
            __builtin_amdgcn_raw_buffer_store_b128(v4u{asu(ye[8 * b + 2 * e]), tag, asu(ye[8 * b + 2 * e + 1]), tag}, rw,
                                                   W_YP + ((b * 80 + 64 + lane) * NMLP + bid) * 64 + e * 16, 0, 16);
        }
    }
    STAMP(3);
    lds_wait<8>(ctl + C_B0, NC);
    combine<3, 8>(p, L, 0, h, m, lane, true, tag, lane);
    lds_arrive(ctl + C_O, lane);
    STAMP(4);
    if (bid >= NFIN) return;
    const int fb = bid / 80, col = (bid - fb * 80) * 8 + 2 * (lane & 3);
    float rs = 0.f;
#pragma unroll
    for (int i = 0; i < 5; ++i) {
      const unsigned w = ((const GAS unsigned*)p.wr)[lane + 64 * i];
      const v2u xx = *(const LAS v2u*)(L + L_XP + (lane + 64 * i) * 8);
      rs = fmaf(fb ? hi16(xx.x) : lo16(xx.x), lo16(w), rs);
      rs = fmaf(fb ? hi16(xx.y) : lo16(xx.y), hi16(w), rs);
    }
    const float sg = bfr(1.0f / (1.0f + expf(-bfr(wave_sum(rs)))));
    const unsigned xo = ((const GAS unsigned*)p.x)[(fb * DM + col) >> 1];
    const v2f y = gather8<NMLP, 8>(p, W_YP + bid * NMLP * 64, tag, lane);
    const v2u xx = *(const LAS v2u*)(L + L_XP + col * 4);
    const float g0 = bfr(siluf(bfr((fb ? hi16(xx.x) : lo16(xx.x)) + bfr(bfr(y.x) * sg))));
    const float g1 = bfr(siluf(bfr((fb ? hi16(xx.y) : lo16(xx.y)) + bfr(bfr(y.y) * sg))));
    STAMP(5);
    lds_wait<2>(ctl + C_T3, 5);
    const v2f s = gather8<NH, 0>(p, W_SP + bid * NH * 64, tag, lane);
    const unsigned o = bfbits(lo16(xo) + bfr(bfr(s.x) * g0)) | (bfbits(hi16(xo) + bfr(bfr(s.y) * g1)) << 16);
    if (lane < 4) ((GAS unsigned*)p.out)[(fb * DM + col) >> 1] = o;
    if (bid == 0 && lane == 0) __hip_atomic_store((GAS unsigned*)(p.ws + W_EPOCH), tag, RLX_AGENT);
    STAMP(6);
    return;
  }

  const int c = wave - 1;
  {
    const __amdgpu_buffer_rsrc_t rq = rsrc(p.wq, DM * NH * DH * 2), rq13 = rsrc(p.wq, c < 2 ? DM * NH * DH * 2 : 0);
    const int vo = r * (NH * DH * 2) + j * 16;
    unsigned xr[2][5];
    load_x(p, xr, lane);
    v4u wv[7][3];
    auto issue = [&](int i) {
      const int so = min(c + 6 * i, 79) * (8 * NH * DH * 2) + h * (DH * 2);
#pragma unroll
      for (int u = 0; u < 3; ++u) wv[i % 7][u] = __builtin_amdgcn_raw_buffer_load_b128(i == 13 ? rq13 : rq, vo + u * 128, so, K_WQ_AUX);
    };
#pragma unroll
    for (int i = 0; i < 7; ++i) issue(i);
    __builtin_amdgcn_sched_barrier(0);
    __builtin_amdgcn_s_barrier();
    CBAR();
    layer_norm(xr, L, lane);
    const LAS unsigned* xp = (const LAS unsigned*)(L + L_XP);
    float a[48];
#pragma unroll
    for (int i = 0; i < 48; ++i) a[i] = 0.f;
#pragma unroll
    for (int i = 0; i < 14; ++i) {
      const unsigned xx = xp[(c + 6 * i) * 8 + r];
      const unsigned x0l = xx & 0xffffu, x0h = xx << 16, x1l = xx >> 16, x1h = xx & 0xffff0000u;
#pragma unroll
      for (int u = 0; u < 3; ++u)
#pragma unroll
        for (int e = 0; e < 4; ++e) {
          const unsigned w = wv[i % 7][u][e];
          const int k = 8 * u + 2 * e;
          a[k] = dot2(w, x0l, a[k]);           a[k + 1] = dot2(w, x0h, a[k + 1]);
          a[24 + k] = dot2(w, x1l, a[24 + k]); a[25 + k] = dot2(w, x1h, a[25 + k]);
        }
      __builtin_amdgcn_sched_barrier(0);
      if (i + 7 < 14) issue(i + 7);
      __builtin_amdgcn_sched_barrier(0);
    }
#pragma unroll
    for (int i = 0; i < 24; ++i) a[i] = fold32(a[i], a[i + 24]);
#pragma unroll
    for (int i = 0; i < 12; ++i) { a[i] = fold16(a[i], a[i + 12]); a[i] += dpp<ROR8>(a[i]); }
    if ((r & 1) == 0) {
#pragma unroll
      for (int u = 0; u < 3; ++u) {
        const int v = 12 * ((lane >> 4) & 1) + 4 * u;
        *(LAS v4f*)(L + L_QP + (c * 384 + (lane >> 5) * DH + (j + 8 * (v >> 3)) * 8 + (v & 7)) * 4) = v4f{a[4 * u], a[4 * u + 1], a[4 * u + 2], a[4 * u + 3]};
      }
    }
    lds_arrive(ctl + C_Q1, lane);
    lds_wait<0>(ctl + C_Q1, NC);
    float q = 0.f;
#pragma unroll
    for (int w = 0; w < NC; ++w) q += ((const LAS float*)(L + L_QP))[w * 384 + c * 64 + lane];
    *(LAS unsigned short*)(L + L_QB + (c * 64 + lane) * 2) = (unsigned short)bfbits(q);
    lds_arrive(ctl + C_Q2, lane);
    lds_wait<0>(ctl + C_Q2, NC);
  }
  if (c == 0) STAMP(8);

  int s = c, pos = c;
  const int koff = r * 384 + j * 16;
#pragma nounroll
  for (int b = 0; b < 2; ++b) {
    v4u q[3];
#pragma unroll
    for (int u = 0; u < 3; ++u) q[u] = *(const LAS v4u*)(L + L_QB + (b * DH + (j + 8 * u) * 8) * 2);
    float mx = -3.0e38f, ls = 0.f, acc[24];
#pragma unroll
    for (int i = 0; i < 24; ++i) acc[i] = 0.f;
#pragma nounroll
    for (; s < 128 * (b + 1); s += NC) {
      lds_wait<1>(ctl + C_LANDED, s + 1);
      const LAS unsigned char* sp = L + pos * SLOTB + koff;
      v4u kk[3], vv[3];
#pragma unroll
      for (int u = 0; u < 3; ++u) kk[u] = *(const LAS v4u*)(sp + u * 128);
#pragma unroll
      for (int u = 0; u < 3; ++u) vv[u] = *(const LAS v4u*)(sp + 3072 + u * 128);
      CBAR();
      __hip_atomic_store(ctl + C_NEXT + c, (unsigned)(s + NC), RLX_WG);
      CBAR();
      float d0 = 0.f, d1 = 0.f, d2 = 0.f;
#pragma unroll
      for (int e = 0; e < 4; ++e) { d0 = dot2(kk[0][e], q[0][e], d0); d1 = dot2(kk[1][e], q[1][e], d1); d2 = dot2(kk[2][e], q[2][e], d2); }
      float d = d0 + d1 + d2;
      d += dpp<XOR1>(d); d += dpp<XOR2>(d); d += dpp<MIR8>(d);
      const float t = bfr(bfr(d) * 0.07216878364870322f) * LOG2E;
      const float mn = fmaxf(mx, t);
      const float al = exp2f_(mx - mn), pr = exp2f_(t - mn);
      mx = mn;
      ls = fmaf(ls, al, pr);
#pragma unroll
      for (int u = 0; u < 3; ++u)
#pragma unroll
        for (int e = 0; e < 4; ++e) {
          const int k = 8 * u + 2 * e;
          acc[k] = fmaf(pr, lo16(vv[u][e]), acc[k] * al);
          acc[k + 1] = fmaf(pr, hi16(vv[u][e]), acc[k + 1] * al);
        }
      pos += NC;
      if (pos >= NSLOT) pos -= NSLOT;
    }
    if (b == 1 && c == 0) STAMP(9);
    const float M = max_rows(fmaxf(mx, dpp<ROR8>(mx)));
    const float sc = exp2f_(mx - M);
    ls *= sc;
    ls += dpp<ROR8>(ls);
    ls = sum_rows(ls);
#pragma unroll
    for (int i = 0; i < 24; ++i) acc[i] *= sc;
#pragma unroll
    for (int i = 0; i < 12; ++i) acc[i] = fold32(acc[i], acc[i + 12]);
#pragma unroll
    for (int i = 0; i < 6; ++i) { acc[i] = fold16(acc[i], acc[i + 6]); acc[i] += dpp<ROR8>(acc[i]); }
    LAS float* rec = (LAS float*)(L + L_REC) + (b * NC + c) * 200;
    if ((r & 1) == 0) {
#pragma unroll
      for (int u = 0; u < 3; ++u) {
        const int v = 6 * (lane >> 4) + 2 * u;
        *(LAS v2f*)(rec + (j + 8 * (v >> 3)) * 8 + (v & 7)) = v2f{acc[2 * u], acc[2 * u + 1]};
      }
    }
    if (lane == 0) *(LAS v2f*)(rec + 192) = v2f{M, ls};
    lds_arrive(ctl + C_B0 + b, lane);
  }
  if (c == 5) return;
  lds_wait<0>(ctl + C_TAGOK, 1);
  const unsigned tag = (unsigned)rfl((int)ctl[C_TAG]);
  if (c < 3) {
    lds_wait<0>(ctl + C_B1, NC);
    if (c == 0) STAMP(10);
    combine<1, 0>(p, L, 1, h, m, c * 64 + lane, c == 0, tag, lane);
    lds_arrive(ctl + C_O, lane);
    if (c == 0) STAMP(11);
  }
  lds_wait<0>(ctl + C_LANDED, NKV + NWO);
  {
    const LAS unsigned char* wo = L + WO_POS * SLOTB + ((lane >> 4) * 8 + ((lane & 15) >> 2)) * 160 + c * 32 + (lane & 3) * 8;
    v4s bl[6], bh[6];
#pragma unroll
    for (int ks = 0; ks < 6; ++ks) {
      bl[ks] = __builtin_amdgcn_ds_read_tr16_b64_v4i16((LAS v4s*)(wo + ks * 32 * 160));
      bh[ks] = __builtin_amdgcn_ds_read_tr16_b64_v4i16((LAS v4s*)(wo + ks * 32 * 160 + 4 * 160));
    }
    lds_wait<0>(ctl + C_O, 4);
    v4f cc = {0.f, 0.f, 0.f, 0.f};
#pragma unroll
    for (int ks = 0; ks < 6; ++ks) {
      v4u av = *(const LAS v4u*)(L + L_OA + ((lane & 1) * DH + ks * 32 + (lane >> 4) * 8) * 2);
      if ((lane & 15) >= 2) av = v4u{0u, 0u, 0u, 0u};
      const v2u b0 = __builtin_bit_cast(v2u, bl[ks]), b1 = __builtin_bit_cast(v2u, bh[ks]);
      cc = __builtin_amdgcn_mfma_f32_16x16x32_bf16(__builtin_bit_cast(bf16x8, av), __builtin_bit_cast(bf16x8, (v4u{b0.x, b0.y, b1.x, b1.y})), cc, 0, 0, 0);
    }
    if (lane < 16) {
      const int col = m * 80 + c * 16 + lane;
      st_gran(p.ws, W_SP + (((col >> 3)) * NH + h) * 64 + (col & 7) * 8, cc[0], tag);
      st_gran(p.ws, W_SP + ((80 + (col >> 3)) * NH + h) * 64 + (col & 7) * 8, cc[1], tag);
    }
    lds_arrive(ctl + C_T3, lane);
  }
  if (c == 0) STAMP(12);
}

}

extern "C" void kernel_launch(void* const* d_in, const int* in_sizes, int n_in, void* d_out, int out_size, void* d_ws,
                              size_t ws_size, hipStream_t stream) {
  Params p;
  p.x = (const unsigned short*)d_in[0];
  p.k = (const unsigned short*)d_in[1];
  p.v = (const unsigned short*)d_in[2];
  p.wq = (const unsigned short*)d_in[3];
  p.wo = (const unsigned short*)d_in[4];
  p.wr = (const unsigned short*)d_in[6];
  p.wg = (const unsigned short*)d_in[7];
  p.wu = (const unsigned short*)d_in[8];
  p.wd = (const unsigned short*)d_in[9];
  p.out = (unsigned short*)d_out;
  p.ws = (unsigned char*)d_ws;
  (void)hipFuncSetAttribute((const void*)fused_decode, hipFuncAttributeMaxDynamicSharedMemorySize, L_END);
  fused_decode<<<NWG, NTHR, L_END, stream>>>(p);
}
```

```cpp
#include <hip/hip_runtime.h>
#include <hip/hip_bf16.h>

namespace {

#define GAS __attribute__((address_space(1)))
#define LAS __attribute__((address_space(3)))
#define RLX_AGENT __ATOMIC_RELAXED, __HIP_MEMORY_SCOPE_AGENT
#define RLX_WG __ATOMIC_RELAXED, __HIP_MEMORY_SCOPE_WORKGROUP
typedef unsigned v4u __attribute__((ext_vector_type(4)));
typedef unsigned v2u __attribute__((ext_vector_type(2)));
typedef float v4f __attribute__((ext_vector_type(4)));
typedef float v2f __attribute__((ext_vector_type(2)));
typedef short v4s __attribute__((ext_vector_type(4)));
typedef __bf16 bf16x2 __attribute__((ext_vector_type(2)));
typedef __bf16 bf16x8 __attribute__((ext_vector_type(8)));
typedef unsigned long long u64;

#define K_INF 21
#define K_NSLOT 22
#define K_ORDER 0
#define K_PK0 0
#define K_PK1 0
#define K_PV0 0
#define K_PV1 0
#define K_PW 0

constexpr int DM = 640, NH = 32, DH = 192, SK = 8192, FF = 1536;
constexpr int NWG = 256, NC = 6, NTHR = (NC + 2) * 64;
constexpr int INF = K_INF;
constexpr int NSLOT = K_NSLOT;
constexpr int ORDER = K_ORDER;
constexpr int SLOTB = 6144, NWO = 5, NS_WQ = 7, NS_GU = 27, NS_DN = 16;
constexpr unsigned KV_BYTES = 2u * NH * SK * DH * 2u;
constexpr int SLAB = SK * DH * 2;
constexpr int NMLP = FF / 64;
constexpr int NFIN = 2 * DM / 8;
static_assert(INF >= 7 && INF <= 60, "vmcnt is 6 bits");

constexpr int L_XP = NSLOT * SLOTB;
constexpr int L_SCR = L_XP + 2560;
constexpr int L_QB = L_SCR + 16384;
constexpr int L_OA = L_QB + 768;
constexpr int L_HM = L_OA + 768;
constexpr int L_CTL = L_HM + 256;
constexpr int L_END = L_CTL + 320;
static_assert(L_END <= 160 * 1024 && L_END > 80 * 1024, "one workgroup per CU");
enum { C_NEXT = 0, C_P1 = 8, C_HM = 16, C_Y = 24, C_R0 = 32, C_R1 = 40, C_O = 48, C_T3 = 56, C_LANDED = 64, C_XOK, C_TAG, C_Q0, C_Q1 };

constexpr int W_EPOCH = 0;
constexpr int W_ML = 1024;
constexpr int W_OP = W_ML + 2 * NH * 8 * 16;
constexpr int W_SP = W_OP + 2 * NH * 8 * DH * 8;
constexpr int W_YP = W_SP + NFIN * NH * 64;
constexpr int W_QP = W_YP + NFIN * NMLP * 64;
constexpr int W_END = W_QP + NH * 2 * 384 * 8;

constexpr unsigned LDS_SPIN = 1u << 20, GLB_SPIN = 1u << 16;
constexpr float LOG2E = 1.4426950408889634f;

struct Params {
  const unsigned short *x, *k, *v, *wq, *wo, *wr, *wg, *wu, *wd;
  unsigned short* out;
  unsigned char* ws;
};

__device__ __forceinline__ int rfl(int v) { return __builtin_amdgcn_readfirstlane(v); }
__device__ __forceinline__ float bfr(float x) { return (float)(__bf16)x; }
__device__ __forceinline__ unsigned bfbits(float x) { return (unsigned)__builtin_bit_cast(unsigned short, (__bf16)x); }
__device__ __forceinline__ float lo16(unsigned u) { return __builtin_bit_cast(float, u << 16); }
__device__ __forceinline__ float hi16(unsigned u) { return __builtin_bit_cast(float, u & 0xffff0000u); }
__device__ __forceinline__ float asf(unsigned u) { return __builtin_bit_cast(float, u); }
__device__ __forceinline__ unsigned asu(float f) { return __builtin_bit_cast(unsigned, f); }
__device__ __forceinline__ float dot2(unsigned a, unsigned b, float c) {
  return __builtin_amdgcn_fdot2_f32_bf16(__builtin_bit_cast(bf16x2, a), __builtin_bit_cast(bf16x2, b), c, false);
}
template <int CTRL> __device__ __forceinline__ float dpp(float x) {
  return __builtin_bit_cast(float, __builtin_amdgcn_mov_dpp(__builtin_bit_cast(int, x), CTRL, 0xf, 0xf, true));
}
template <int CTRL> __device__ __forceinline__ int dppi(int x) { return __builtin_amdgcn_mov_dpp(x, CTRL, 0xf, 0xf, true); }
constexpr int XOR1 = 0xB1, XOR2 = 0x4E, MIR8 = 0x141, ROR4 = 0x124, ROR8 = 0x128;
__device__ __forceinline__ float fold32(float a, float b) {
  auto s = __builtin_amdgcn_permlane32_swap(asu(a), asu(b), false, false);
  return asf(s[0]) + asf(s[1]);
}
__device__ __forceinline__ float fold16(float a, float b) {
  auto s = __builtin_amdgcn_permlane16_swap(asu(a), asu(b), false, false);
  return asf(s[0]) + asf(s[1]);
}
__device__ __forceinline__ float max_rows(float x) {
  auto s = __builtin_amdgcn_permlane16_swap(asu(x), asu(x), false, false);
  x = fmaxf(asf(s[0]), asf(s[1]));
  auto t = __builtin_amdgcn_permlane32_swap(asu(x), asu(x), false, false);
  return fmaxf(asf(t[0]), asf(t[1]));
}
__device__ __forceinline__ float sum_rows(float x) { x = fold16(x, x); return fold32(x, x); }
__device__ __forceinline__ float wave_sum(float x) {
  x += dpp<XOR1>(x); x += dpp<XOR2>(x); x += dpp<ROR4>(x); x += dpp<ROR8>(x);
  return sum_rows(x);
}
__device__ __forceinline__ float exp2f_(float x) { return __builtin_amdgcn_exp2f(x); }
__device__ __forceinline__ float siluf(float x) { return x / (1.0f + expf(-x)); }
#define CBAR() asm volatile("" ::: "memory")

__device__ __forceinline__ __amdgpu_buffer_rsrc_t rsrc(const void* p, unsigned bytes) {
  return __builtin_amdgcn_make_buffer_rsrc((void*)p, 0, bytes, 0x00020000);
}
__device__ __forceinline__ void lds_set(LAS unsigned* w, unsigned v) {
  CBAR();
  __hip_atomic_store(w, v, RLX_WG);
  CBAR();
}
template <int SLEEP> __device__ __forceinline__ void lds_wait(LAS unsigned* w, unsigned want) {
#pragma nounroll
  for (unsigned n = 0; n < LDS_SPIN; ++n) {
    if ((unsigned)rfl((int)__hip_atomic_load(w, RLX_WG)) >= want) break;
    if (SLEEP) __builtin_amdgcn_s_sleep(SLEEP);
  }
  CBAR();
}
template <int N, int SLEEP> __device__ __forceinline__ void lds_wait_all(LAS unsigned* w, int j) {
  LAS unsigned* a = w + (j < N ? j : 0);
#pragma nounroll
  for (unsigned n = 0; n < LDS_SPIN; ++n) {
    if (__all(__hip_atomic_load(a, RLX_WG) != 0u)) break;
    if (SLEEP) __builtin_amdgcn_s_sleep(SLEEP);
  }
  CBAR();
}
__device__ __forceinline__ void st_gran(unsigned char* ws, int off, float v, unsigned tag) {
  __hip_atomic_store((GAS u64*)(ws + off), ((u64)tag << 32) | asu(v), RLX_AGENT);
}
__device__ __forceinline__ u64 ld_gran(const unsigned char* ws, int off) { return __hip_atomic_load((GAS u64*)(ws + off), RLX_AGENT); }

#define STAMP(i) do { } while (0)

__device__ __forceinline__ void layer_norm(v2u (&xr)[5], LAS unsigned char* L, int lane) {
  asm volatile("" : "+v"(xr[0]), "+v"(xr[1]), "+v"(xr[2]), "+v"(xr[3]), "+v"(xr[4]));
  v2f x[10];
#pragma unroll
  for (int i = 0; i < 5; ++i) { x[2 * i] = v2f{lo16(xr[i].x), hi16(xr[i].x)}; x[2 * i + 1] = v2f{lo16(xr[i].y), hi16(xr[i].y)}; }
  const float p0 = asf((unsigned)__builtin_amdgcn_readlane((int)asu(x[0].x), 0)), p1 = asf((unsigned)__builtin_amdgcn_readlane((int)asu(x[0].x), 32));
  const float piv = lane < 32 ? p0 : p1;
  v2f s1 = {0.f, 0.f}, s2 = {0.f, 0.f};
#pragma unroll
  for (int i = 0; i < 10; ++i) { const v2f d = x[i] - piv; s1 += d; s2 += d * d; }
  float a = s1.x + s1.y, q = s2.x + s2.y;
  a += dpp<XOR1>(a); q += dpp<XOR1>(q); a += dpp<XOR2>(a); q += dpp<XOR2>(q);
  a += dpp<ROR4>(a); q += dpp<ROR4>(q); a += dpp<ROR8>(a); q += dpp<ROR8>(q);
  a = fold16(a, a) * (1.0f / DM); q = fold16(q, q) * (1.0f / DM);
  const float mb = bfr(piv + a), var = bfr(fmaf(-a, a, q));
  const float den = bfr(__builtin_amdgcn_sqrtf(bfr(var + 1e-5f)));
  const float rcp = __builtin_amdgcn_rcpf(den);
#pragma unroll
  for (int i = 0; i < 5; ++i) {
    unsigned o[2];
#pragma unroll
    for (int e = 0; e < 2; ++e) {
      const v2f t = x[2 * i + e] - mb;
      const v2f d = {bfr(t.x), bfr(t.y)}, q0 = d * rcp;
      const v2f qq = (d - q0 * den) * rcp + q0;
      o[e] = bfbits(qq.x) | (bfbits(qq.y) << 16);
    }
    *(LAS v2u*)(L + L_XP + (lane >> 5) * 1280 + ((lane & 31) + 32 * i) * 8) = v2u{o[0], o[1]};
  }
}

__device__ __forceinline__ void gather_q(const Params& p, LAS unsigned char* L, int h, int b, unsigned tag, int lane) {
  const __amdgpu_buffer_rsrc_t rw = rsrc(p.ws, W_END);
  const int l = min(lane, 47), off = W_QP + (h * 768 + b * DH + 4 * l) * 8;
  v4u g[4];
#pragma nounroll
  for (unsigned n = 0; n < GLB_SPIN; ++n) {
    CBAR();
#pragma unroll
    for (int i = 0; i < 4; ++i) g[i] = __builtin_amdgcn_raw_buffer_load_b128(rw, off + (i & 1) * 16 + (i >> 1) * 3072, 0, 16);
    bool ok = true;
#pragma unroll
    for (int i = 0; i < 4; ++i) ok &= g[i].y == tag && g[i].w == tag;
    if (__all(ok)) break;
  }
  *(LAS v2u*)(L + L_QB + (b * DH + 4 * l) * 2) = v2u{bfbits(asf(g[0].x) + asf(g[2].x)) | (bfbits(asf(g[0].z) + asf(g[2].z)) << 16),
                                                      bfbits(asf(g[1].x) + asf(g[3].x)) | (bfbits(asf(g[1].z) + asf(g[3].z)) << 16)};
}

template <int NK, int KSTEP, int HALF>
__device__ __forceinline__ void load_bt(const LAS unsigned char* b, v4u (&f)[NK]) {
#pragma unroll
  for (int ks = 0; ks < NK; ++ks) {
    const v2u lo = __builtin_bit_cast(v2u, __builtin_amdgcn_ds_read_tr16_b64_v4i16((LAS v4s*)(b + ks * KSTEP)));
    const v2u hi = __builtin_bit_cast(v2u, __builtin_amdgcn_ds_read_tr16_b64_v4i16((LAS v4s*)(b + ks * KSTEP + HALF)));
    f[ks] = v4u{lo.x, lo.y, hi.x, hi.y};
  }
}
template <int NK> __device__ __forceinline__ float mm2(const LAS unsigned char* a, const v4u (&f)[NK], int lane) {
  v4u av[NK];
#pragma unroll
  for (int ks = 0; ks < NK; ++ks) av[ks] = *(const LAS v4u*)(a + ks * 64);
  v4f c = {0.f, 0.f, 0.f, 0.f};
#pragma unroll
  for (int ks = 0; ks < NK; ++ks) c = __builtin_amdgcn_mfma_f32_16x16x32_bf16(__builtin_bit_cast(bf16x8, av[ks]), __builtin_bit_cast(bf16x8, f[ks]), c, 0, 0, 0);
  return lane & 16 ? c[1] : c[0];
}

template <int ND, int SLEEP>
__device__ __forceinline__ void combine(const Params& p, LAS unsigned char* L, int ph, int h, int m, int d0, bool lead, unsigned tag, int lane) {
  const int b = ph ^ ORDER;
  const LAS float* rec = (const LAS float*)(L + L_SCR) + ph * NC * 200;
  float M = rec[192];
#pragma unroll
  for (int c = 1; c < NC; ++c) M = fmaxf(M, rec[c * 200 + 192]);
  float Ls = 0.f, o[ND];
#pragma unroll
  for (int i = 0; i < ND; ++i) o[i] = 0.f;
#pragma unroll
  for (int c = 0; c < NC; ++c) {
    const float w = exp2f_(rec[c * 200 + 192] - M);
    Ls = fmaf(w, rec[c * 200 + 193], Ls);
#pragma unroll
    for (int i = 0; i < ND; ++i) o[i] = fmaf(w, rec[c * 200 + d0 + 64 * i], o[i]);
  }
  const int bh = b * NH + h;
  const __amdgpu_buffer_rsrc_t rw = rsrc(p.ws, W_END);
#pragma unroll
  for (int i = 0; i < ND; ++i) st_gran(p.ws, W_OP + ((bh * 8 + m) * DH + d0 + 64 * i) * 8, o[i], tag);
  if (lead && lane == 0) __builtin_amdgcn_raw_buffer_store_b128(v4u{asu(M), tag, asu(Ls), tag}, rw, W_ML + (bh * 8 + m) * 16, 0, 16);
  u64 g[ND][8];
  v4u ml;
#pragma nounroll
  for (unsigned n = 0; n < GLB_SPIN; ++n) {
    CBAR();
    ml = __builtin_amdgcn_raw_buffer_load_b128(rw, W_ML + (bh * 8 + (lane & 7)) * 16, 0, 16);
#pragma unroll
    for (int i = 0; i < ND; ++i)
#pragma unroll
      for (int j = 0; j < 8; ++j) g[i][j] = ld_gran(p.ws, W_OP + ((bh * 8 + j) * DH + d0 + 64 * i) * 8);
    bool ok = ml.y == tag && ml.w == tag;
#pragma unroll
    for (int i = 0; i < ND; ++i)
#pragma unroll
      for (int j = 0; j < 8; ++j) ok &= (unsigned)(g[i][j] >> 32) == tag;
    if (__all(ok)) break;
    if (SLEEP) __builtin_amdgcn_s_sleep(SLEEP);
  }
  float Mg = asf(ml.x);
  Mg = fmaxf(Mg, dpp<XOR1>(Mg)); Mg = fmaxf(Mg, dpp<XOR2>(Mg)); Mg = fmaxf(Mg, dpp<MIR8>(Mg));
  const float w = exp2f_(asf(ml.x) - Mg);
  float Lg = w * asf(ml.z);
  Lg += dpp<XOR1>(Lg); Lg += dpp<XOR2>(Lg); Lg += dpp<MIR8>(Lg);
  const float wn = w / Lg;
#pragma unroll
  for (int i = 0; i < ND; ++i) {
    float a = 0.f;
#pragma unroll
    for (int j = 0; j < 8; ++j) a = fmaf(asf((unsigned)__builtin_amdgcn_readlane((int)asu(wn), j)), asf((unsigned)g[i][j]), a);
    *(LAS unsigned short*)(L + L_OA + (b * DH + d0 + 64 * i) * 2) = (unsigned short)bfbits(a);
  }
}

template <int NP, int SLEEP>
__device__ __forceinline__ v2f gather8(const Params& p, int base, unsigned tag, int lane) {
  static_assert(NP > 16 && NP <= 32, "two 16-byte pieces per lane");
  const __amdgpu_buffer_rsrc_t rw = rsrc(p.ws, W_END);
  const bool two = 64 + lane < NP * 4;
  v4u a, c;
#pragma nounroll
  for (unsigned n = 0; n < GLB_SPIN; ++n) {
    CBAR();
    a = __builtin_amdgcn_raw_buffer_load_b128(rw, base + lane * 16, 0, 16);
    c = __builtin_amdgcn_raw_buffer_load_b128(rw, base + (two ? 64 + lane : lane) * 16, 0, 16);
    if (__all(a.y == tag && a.w == tag && c.y == tag && c.w == tag)) break;
    if (SLEEP) __builtin_amdgcn_s_sleep(SLEEP);
  }
  float s0 = asf(a.x) + (two ? asf(c.x) : 0.f), s1 = asf(a.z) + (two ? asf(c.z) : 0.f);
  s0 += dpp<ROR4>(s0); s1 += dpp<ROR4>(s1);
  s0 += dpp<ROR8>(s0); s1 += dpp<ROR8>(s1);
  return v2f{sum_rows(s0), sum_rows(s1)};
}

#define DMA3(POL)                                                                    \
  asm volatile("s_mov_b32 m0, %3\n\t"                                                \
               "s_waitcnt vmcnt(%4)\n\t"                                             \
               "buffer_load_dwordx4 %0, %1, %2 offen " POL "lds\n\t"                 \
               "s_waitcnt vmcnt(%4)\n\t"                                             \
               "buffer_load_dwordx4 %0, %1, %2 offen offset:1024 " POL "lds\n\t"     \
               "s_waitcnt vmcnt(%4)\n\t"                                             \
               "buffer_load_dwordx4 %0, %1, %2 offen offset:2048 " POL "lds"         \
               :: "v"(voff), "s"(srd), "s"(soff), "s"(lds), "i"(N) : "memory")
#define DMA1(POL)                                                                    \
  asm volatile("s_mov_b32 m0, %3\n\t"                                                \
               "s_waitcnt vmcnt(%4)\n\t"                                             \
               "buffer_load_dwordx4 %0, %1, %2 offen " POL "lds"                     \
               :: "v"(voff), "s"(srd), "s"(soff), "s"(lds), "i"(N) : "memory")
template <int N, int POL> __device__ __forceinline__ void dma3(int voff, v4u srd, int soff, unsigned lds) {
  if constexpr (POL == 0) DMA3("nt "); else DMA3("");
}
template <int N, int POL> __device__ __forceinline__ void dma1(int voff, v4u srd, int soff, unsigned lds) {
  if constexpr (POL == 0) DMA1("nt "); else DMA1("");
}
__device__ __forceinline__ v4u srd_of(const void* ptr, unsigned bytes) {
  const u64 a = (u64)ptr;
  return v4u{(unsigned)a, (unsigned)(a >> 32) & 0xffffu, bytes, 0x00020000u};
}
constexpr int fdiv6(int a) { return a >= 0 ? a / 6 : -((5 - a) / 6); }
constexpr int LAG3 = fdiv6(3 - INF), LAG6 = fdiv6(6 - INF);

struct Loader {
  LAS unsigned* ctl;
  unsigned lbase;
  int s, pos, limit, j;
  __device__ __forceinline__ void credit(int last) {
    if (__builtin_expect(last >= limit, 0)) {
#pragma nounroll
      for (unsigned n = 0; n < LDS_SPIN; ++n) {
        int v = (int)__hip_atomic_load(ctl + C_NEXT + (j < NC ? j : 0), RLX_WG);
        v = min(v, dppi<XOR1>(v)); v = min(v, dppi<XOR2>(v)); v = min(v, dppi<MIR8>(v));
        limit = rfl(v) + NSLOT;
        if (last < limit) break;
        __builtin_amdgcn_s_sleep(1);
      }
      CBAR();
    }
  }
  __device__ __forceinline__ void publish(int n) { __hip_atomic_store(ctl + C_LANDED, (unsigned)max(n, 0), RLX_WG); }
  __device__ __forceinline__ unsigned dst() const { return lbase + pos * SLOTB; }
  __device__ __forceinline__ void advance() { ++s; pos = pos + 1 == NSLOT ? 0 : pos + 1; }
  template <int PK, int PV> __device__ __forceinline__ void kv(int i0, int i1, int n1, int st, int m, int slab, int voff, v4u rk, v4u rv) {
#pragma nounroll
    for (int i = i0; i < i1; ++i) {
      credit(s + 3);
      const int piece = i < n1 ? min(st * i + m, 63) : 64 + 8 * (i - n1) + m;
      const int so = slab + (piece & 255) * 12288;
#pragma unroll
      for (int u = 0; u < 4; ++u) {
        dma3<INF - 1, PK>(voff, rk, so + u * 3072, dst());
        if (LAG3 == LAG6) publish(s + LAG3);
        dma3<INF - 1, PV>(voff, rv, so + u * 3072, dst() + 3072);
        if (LAG3 != LAG6) publish(s + LAG6);
        advance();
      }
    }
  }
};

__device__ __forceinline__ void dot16(float (&a)[16], v4u w, unsigned x0l, unsigned x1l) {
  const unsigned x0h = x0l << 16, x1h = x1l << 16;
#pragma unroll
  for (int e = 0; e < 4; ++e) {
    a[2 * e] = dot2(w[e], x0l, a[2 * e]);         a[2 * e + 1] = dot2(w[e], x0h, a[2 * e + 1]);
    a[8 + 2 * e] = dot2(w[e], x1l, a[8 + 2 * e]); a[9 + 2 * e] = dot2(w[e], x1h, a[9 + 2 * e]);
  }
}

__global__ void __launch_bounds__(NTHR, 2) fused_decode(Params p) {
  extern __shared__ __attribute__((aligned(16))) unsigned char smem[];
  LAS unsigned char* L = (LAS unsigned char*)smem;
  LAS unsigned* ctl = (LAS unsigned*)(L + L_CTL);
  const int tid = threadIdx.x, lane = tid & 63, wave = rfl(tid >> 6);
  const int bid = blockIdx.x, h = bid & 31, m = bid >> 5;
  const int r = lane >> 3, j = lane & 7;
  const bool has_wq = m < 6, has_mlp = m == 7 && h < NMLP;
  const int npro = has_wq ? NS_WQ : has_mlp ? NS_GU + NS_DN : 0;
  const int st = h < NMLP ? 7 : 8, n1 = h < NMLP ? (m < 6 ? 9 : m == 6 ? 10 : 0) : 8;
  const int ntot = npro + 4 * (n1 + 56);
  const int wo_skip = ntot % NSLOT + NWO > NSLOT ? NSLOT - ntot % NSLOT : 0, wo_pos = (ntot + wo_skip) % NSLOT;

  if (wave == 0) {
    __builtin_amdgcn_s_setprio(3);
    STAMP(0);
    Loader ld{ctl, (unsigned)(size_t)L, 0, 0, NSLOT, j};
    const v4u rk = srd_of(p.k, KV_BYTES), rv = srd_of(p.v, KV_BYTES);
    const int voff = lane * 16;
    const int slab0 = (ORDER * NH + h) * SLAB, slab1 = ((ORDER ^ 1) * NH + h) * SLAB;
    int i0 = 0;
    if (has_wq) {
      const v4u rq = srd_of(p.wq, DM * NH * DH * 2);
      const int vo = r * (NH * DH * 2) + (j ^ (r & 2)) * 16, so = (m / 3) * 320 * (NH * DH * 2) + h * (DH * 2) + (m % 3) * 128;
#pragma unroll 6
      for (int g = 0; g < 6 * NS_WQ; ++g) dma1<63, K_PW>(g & 1 ? vo ^ 64 : vo, rq, so + min(g, 39) * (8 * NH * DH * 2), ld.lbase + g * 1024);
      ld.s = ld.pos = NS_WQ;
      __builtin_amdgcn_s_barrier();
    } else if (has_mlp) {
      __builtin_amdgcn_s_barrier();
      const v4u rg = srd_of(p.wg, DM * FF * 2), ru = srd_of(p.wu, DM * FF * 2), rd = srd_of(p.wd, FF * DM * 2);
      const int vo = r * (FF * 2) + j * 16, ve = (lane >> 4) * (DM * 2) + 1024 + (lane & 15) * 16;
#pragma nounroll
      for (int t = 0; t < NS_GU; ++t) {
        ld.credit(ld.s);
#pragma unroll
        for (int q = 0; q < 6; ++q) dma1<INF - 1, K_PW>(vo, q & 1 ? ru : rg, min(3 * t + (q >> 1), 79) * (8 * FF * 2) + h * 128, ld.dst() + q * 1024);
        ld.publish(ld.s + LAG6);
        ld.advance();
      }
#pragma nounroll
      for (int t = 0; t < NS_DN; ++t) {
        ld.credit(ld.s);
#pragma unroll
        for (int q = 0; q < 6; ++q) dma1<INF - 1, K_PW>(q < 4 ? voff : ve, rd, (64 * h + 4 * t + (q < 4 ? q : 0)) * (DM * 2), ld.dst() + q * 1024);
        ld.publish(ld.s + LAG6);
        ld.advance();
      }
    } else {
      const int so = slab0 + (n1 ? m : 64 + m) * 12288;
#pragma unroll
      for (int u = 0; u < 4; ++u) {
        dma3<63, ORDER ? K_PK1 : K_PK0>(voff, rk, so + u * 3072, ld.dst());
        dma3<63, ORDER ? K_PV1 : K_PV0>(voff, rv, so + u * 3072, ld.dst() + 3072);
        ld.advance();
      }
      i0 = 1;
      __builtin_amdgcn_s_barrier();
    }
    CBAR();
    ld.kv<ORDER ? K_PK1 : K_PK0, ORDER ? K_PV1 : K_PV0>(i0, n1 + 24, n1, st, m, slab0, voff, rk, rv);
    ld.kv<ORDER ? K_PK0 : K_PK1, ORDER ? K_PV0 : K_PV1>(n1 + 24, n1 + 56, n1, st, m, slab1, voff, rk, rv);
    STAMP(1);
    const v4u ro = srd_of(p.wo, NH * DH * DM * 2);
    int row = lane / 10, ch = lane - row * 10;
    ld.credit(ld.s + wo_skip + NWO - 1);
    ld.pos = wo_pos;
#pragma nounroll
    for (int t = 0; t < NWO; ++t) {
#pragma unroll
      for (int q = 0; q < 6; ++q) {
        dma1<INF - 1, K_PW>(row * (DM * 2) + ch * 16, ro, h * DH * (DM * 2) + m * 160, ld.dst() + q * 1024);
        if (q == 2 && LAG3 == LAG6) ld.publish(ld.s + LAG3);
        if (q == 5 && LAG3 != LAG6) ld.publish(ld.s + LAG6);
        row += 6; ch += 4;
        if (ch >= 10) { ch -= 10; row += 1; }
      }
      ld.advance();
    }
    if (INF > 30) { asm volatile("s_waitcnt vmcnt(30)" ::: "memory"); ld.publish(ld.s - NWO); }
    asm volatile("s_waitcnt vmcnt(0)" ::: "memory");
    ld.publish(ld.s);
    STAMP(2);
    return;
  }

  if (wave == NC + 1) {
    v2u xr[5];
#pragma unroll
    for (int i = 0; i < 5; ++i) xr[i] = ((const GAS v2u*)p.x)[(lane >> 5) * 160 + (lane & 31) + 32 * i];
    const unsigned e0 = __hip_atomic_load((GAS unsigned*)(p.ws + W_EPOCH), RLX_AGENT);
    ctl[lane] = lane < NC && (npro == 0 || (has_wq && lane >= 4)) ? npro + lane : 0;
    if (lane < 16) ctl[64 + lane] = 0u;
    asm volatile("s_waitcnt lgkmcnt(0)" ::: "memory");
    __builtin_amdgcn_s_barrier();
    CBAR();
    layer_norm(xr, L, lane);
    const unsigned tag = (unsigned)rfl((int)e0) + 1u;
    ctl[C_TAG] = tag;
    lds_set(ctl + C_XOK, 1u);
    STAMP(3);
    lds_wait<8>(ctl + C_Q0, 1);
    gather_q(p, L, h, ORDER ^ 1, tag, lane);
    lds_set(ctl + C_Q1, 1u);
    lds_wait_all<NC, 16>(ctl + C_R0, j);
    combine<3, 8>(p, L, 0, h, m, lane, true, tag, lane);
    lds_set(ctl + C_O + 3, 1u);
    STAMP(4);
    if (bid >= NFIN) return;
    const int fb = bid / 80, col = (bid - fb * 80) * 8 + 2 * (lane & 3);
    float rs = 0.f;
#pragma unroll
    for (int i = 0; i < 5; ++i) {
      const unsigned w = ((const GAS unsigned*)p.wr)[lane + 64 * i];
      rs = dot2(w, *(const LAS unsigned*)(L + L_XP + fb * 1280 + (lane + 64 * i) * 4), rs);
    }
    const float sg = bfr(1.0f / (1.0f + expf(-bfr(wave_sum(rs)))));
    const unsigned xo = ((const GAS unsigned*)p.x)[(fb * DM + col) >> 1];
    const v2f y = gather8<NMLP, 8>(p, W_YP + bid * NMLP * 64, tag, lane);
    const unsigned xx = *(const LAS unsigned*)(L + L_XP + fb * 1280 + col * 2);
    const float g0 = bfr(siluf(bfr(lo16(xx) + bfr(bfr(y.x) * sg))));
    const float g1 = bfr(siluf(bfr(hi16(xx) + bfr(bfr(y.y) * sg))));
    STAMP(5);
    lds_wait_all<5, 2>(ctl + C_T3, j);
    const v2f s = gather8<NH, 0>(p, W_SP + bid * NH * 64, tag, lane);
    const unsigned o = bfbits(lo16(xo) + bfr(bfr(s.x) * g0)) | (bfbits(hi16(xo) + bfr(bfr(s.y) * g1)) << 16);
    if (lane < 4) ((GAS unsigned*)p.out)[(fb * DM + col) >> 1] = o;
    if (bid == 0 && lane == 0) __hip_atomic_store((GAS unsigned*)(p.ws + W_EPOCH), tag, RLX_AGENT);
    STAMP(6);
    return;
  }

  const int c = wave - 1;
  __builtin_amdgcn_s_barrier();
  CBAR();
  lds_wait<1>(ctl + C_XOK, 1);
  const unsigned tag = (unsigned)rfl((int)ctl[C_TAG]);
  const __amdgpu_buffer_rsrc_t rw = rsrc(p.ws, W_END);
  if (c == 0) STAMP(13);
  if (has_wq) {
    if (c < 4) {
      const int sw = ((lane >> 3) & 1) | ((lane >> 3) & 2);
      lds_wait<0>(ctl + C_LANDED, NS_WQ);
      v4u bf[10];
      load_bt<10, 4096, 512>(L + (lane >> 4) * 1024 + ((lane & 15) >> 2) * 128 + ((2 * c + ((lane & 3) >> 1)) ^ (2 * sw)) * 16 + (lane & 1) * 8, bf);
      lds_set(ctl + C_NEXT + c, NS_WQ + c);
      const float qv = mm2<10>(L + L_XP + (lane & 1) * 1280 + (m / 3) * 640 + (lane >> 4) * 16, bf, lane);
      if (lane < 32) st_gran(p.ws, W_QP + ((h * 2 + m / 3) * 384 + (lane >> 4) * DH + (m % 3) * 64 + 16 * c + (lane & 15)) * 8, qv, tag);
    }
  } else if (has_mlp) {
    const LAS unsigned short* xp = (const LAS unsigned short*)(L + L_XP);
    int pos = 0;
    float a[16];
#pragma unroll
    for (int i = 0; i < 16; ++i) a[i] = 0.f;
#pragma nounroll
    for (int t = 0; t < NS_GU; ++t) {
      const int g = 3 * t + (c >> 1);
      lds_wait<1>(ctl + C_LANDED, t + 1);
      const v4u w = *(const LAS v4u*)(L + pos * SLOTB + c * 1024 + lane * 16);
      const unsigned x0 = xp[min(g, 79) * 8 + r], x1 = xp[DM + min(g, 79) * 8 + r];
      lds_set(ctl + C_NEXT + c, t + 1 == NS_GU && c == 5 ? NS_GU + NS_DN + c : t + 1);
      if (g < 80) dot16(a, w, x0, x1);
      pos = pos + 1 == NSLOT ? 0 : pos + 1;
    }
#pragma unroll
    for (int i = 0; i < 8; ++i) a[i] = fold32(a[i], a[i + 8]);
#pragma unroll
    for (int i = 0; i < 4; ++i) { a[i] = fold16(a[i], a[i + 4]); a[i] += dpp<ROR8>(a[i]); }
    if ((r & 1) == 0) *(LAS v4f*)(L + L_SCR + (c * 128 + (lane >> 5) * 64 + 8 * j + 4 * ((lane >> 4) & 1)) * 4) = v4f{a[0], a[1], a[2], a[3]};
    lds_set(ctl + C_P1 + c, 1u);
    if (c < 2) {
      lds_wait_all<NC, 0>(ctl + C_P1, j);
      const LAS float* pp = (const LAS float*)(L + L_SCR) + c * 64 + lane;
      const float s0 = pp[0] + pp[256] + pp[512], s1 = pp[128] + pp[384] + pp[640];
      *(LAS unsigned short*)(L + L_HM + lane * 4 + c * 2) = (unsigned short)bfbits(bfr(siluf(bfr(s0))) * bfr(s1));
      lds_set(ctl + C_HM + c, 1u);
    }
    if (c < 5) {
      lds_wait_all<2, 1>(ctl + C_HM, j);
#pragma unroll
      for (int i = 0; i < 16; ++i) a[i] = 0.f;
#pragma nounroll
      for (int t = 0; t < NS_DN; ++t) {
        lds_wait<1>(ctl + C_LANDED, NS_GU + t + 1);
        const v4u w = *(const LAS v4u*)(L + pos * SLOTB + c * 1024 + lane * 16);
        const unsigned xx = ((const LAS unsigned*)(L + L_HM))[4 * t + (c < 4 ? c : lane >> 4)];
        lds_set(ctl + C_NEXT + c, t + 1 == NS_DN ? NS_GU + NS_DN + c : NS_GU + t + 1);
        dot16(a, w, xx & 0xffffu, xx >> 16);
        pos = pos + 1 == NSLOT ? 0 : pos + 1;
      }
      if (c < 4) {
#pragma unroll
        for (int u = 0; u < 4; ++u) *(LAS v4f*)(L + L_SCR + c * 4096 + u * 1024 + lane * 16) = v4f{a[4 * u], a[4 * u + 1], a[4 * u + 2], a[4 * u + 3]};
        lds_set(ctl + C_Y + c, 1u);
        lds_wait_all<4, 0>(ctl + C_Y, j);
        v4f y = {0.f, 0.f, 0.f, 0.f};
#pragma unroll
        for (int w = 0; w < 4; ++w) y += *(const LAS v4f*)(L + L_SCR + w * 4096 + c * 1024 + lane * 16);
        lds_set(ctl + C_Y + 4 + c, 1u);
        const int off = W_YP + (((c >> 1) * 80 + lane) * NMLP + h) * 64 + (c & 1) * 32;
        __builtin_amdgcn_raw_buffer_store_b128(v4u{asu(y.x), tag, asu(y.y), tag}, rw, off, 0, 16);
        __builtin_amdgcn_raw_buffer_store_b128(v4u{asu(y.z), tag, asu(y.w), tag}, rw, off + 16, 0, 16);
      } else {
#pragma unroll
        for (int i = 0; i < 16; ++i) a[i] = sum_rows(a[i]);
        if (lane < 16) {
#pragma unroll
          for (int u = 0; u < 8; ++u)
            __builtin_amdgcn_raw_buffer_store_b128(v4u{asu(a[2 * u]), tag, asu(a[2 * u + 1]), tag}, rw,
                                                   W_YP + (((u >> 2) * 80 + 64 + lane) * NMLP + h) * 64 + (u & 3) * 16, 0, 16);
        }
      }
    }
  }
  if (c == 0) STAMP(15);
  if (c == 5) {
    gather_q(p, L, h, ORDER, tag, lane);
    lds_set(ctl + C_Q0, 1u);
  }
  if (has_mlp) lds_wait_all<8, 1>(ctl + C_Y, j);

  int s = npro + c, pos = s % NSLOT;
  const int koff = r * 384 + j * 16;
#pragma nounroll
  for (int ph = 0; ph < 2; ++ph) {
    lds_wait<0>(ctl + C_Q0 + ph, 1);
    if (c == 0 && ph == 0) STAMP(8);
    v4u q[3];
#pragma unroll
    for (int u = 0; u < 3; ++u) q[u] = *(const LAS v4u*)(L + L_QB + ((ph ^ ORDER) * DH + (j + 8 * u) * 8) * 2);
    float mx = -3.0e38f, ls = 0.f, acc[24];
#pragma unroll
    for (int i = 0; i < 24; ++i) acc[i] = 0.f;
    const int send = ph ? ntot : ntot - 128;
#pragma nounroll
    for (; s < send; s += NC) {
      lds_wait<1>(ctl + C_LANDED, s + 1);
      const LAS unsigned char* sp = L + pos * SLOTB + koff;
      v4u kk[3], vv[3];
#pragma unroll
      for (int u = 0; u < 3; ++u) kk[u] = *(const LAS v4u*)(sp + u * 128);
#pragma unroll
      for (int u = 0; u < 3; ++u) vv[u] = *(const LAS v4u*)(sp + 3072 + u * 128);
      lds_set(ctl + C_NEXT + c, s + NC);
      float d0 = 0.f, d1 = 0.f, d2 = 0.f;
#pragma unroll
      for (int e = 0; e < 4; ++e) { d0 = dot2(kk[0][e], q[0][e], d0); d1 = dot2(kk[1][e], q[1][e], d1); d2 = dot2(kk[2][e], q[2][e], d2); }
      float d = d0 + d1 + d2;
      d += dpp<XOR1>(d); d += dpp<XOR2>(d); d += dpp<MIR8>(d);
      const float t = bfr(bfr(d) * 0.07216878364870322f) * LOG2E;
      const float mn = fmaxf(mx, t);
      const float al = exp2f_(mx - mn), pr = exp2f_(t - mn);
      mx = mn;
      ls = fmaf(ls, al, pr);
#pragma unroll
      for (int u = 0; u < 3; ++u)
#pragma unroll
        for (int e = 0; e < 4; ++e) {
          const int k = 8 * u + 2 * e;
          acc[k] = fmaf(pr, lo16(vv[u][e]), acc[k] * al);
          acc[k + 1] = fmaf(pr, hi16(vv[u][e]), acc[k + 1] * al);
        }
      pos += NC;
      if (pos >= NSLOT) pos -= NSLOT;
    }
    if (ph == 1 && c == 0) STAMP(9);
    const float M = max_rows(fmaxf(mx, dpp<ROR8>(mx)));
    const float sc = exp2f_(mx - M);
    ls *= sc;
    ls += dpp<ROR8>(ls);
    ls = sum_rows(ls);
#pragma unroll
    for (int i = 0; i < 24; ++i) acc[i] *= sc;
#pragma unroll
    for (int i = 0; i < 12; ++i) acc[i] = fold32(acc[i], acc[i + 12]);
#pragma unroll
    for (int i = 0; i < 6; ++i) { acc[i] = fold16(acc[i], acc[i + 6]); acc[i] += dpp<ROR8>(acc[i]); }
    LAS float* rec = (LAS float*)(L + L_SCR) + (ph * NC + c) * 200;
    if ((r & 1) == 0) {
#pragma unroll
      for (int u = 0; u < 3; ++u) {
        const int v = 6 * (lane >> 4) + 2 * u;
        *(LAS v2f*)(rec + (j + 8 * (v >> 3)) * 8 + (v & 7)) = v2f{acc[2 * u], acc[2 * u + 1]};
      }
    }
    if (lane == 0) *(LAS v2f*)(rec + 192) = v2f{M, ls};
    lds_set(ctl + C_R0 + 8 * ph + c, 1u);
  }
  if (c == 5) return;
  if (c < 3) {
    lds_wait_all<NC, 0>(ctl + C_R1, j);
    if (c == 0) STAMP(10);
    combine<1, 0>(p, L, 1, h, m, c * 64 + lane, c == 0, tag, lane);
    lds_set(ctl + C_O + c, 1u);
    if (c == 0) STAMP(11);
  }
  lds_wait<0>(ctl + C_LANDED, ntot + NWO);
  v4u bf[6];
  load_bt<6, 32 * 160, 4 * 160>(L + wo_pos * SLOTB + ((lane >> 4) * 8 + ((lane & 15) >> 2)) * 160 + c * 32 + (lane & 3) * 8, bf);
  lds_wait_all<4, 0>(ctl + C_O, j);
  const float sv = mm2<6>(L + L_OA + (lane & 1) * (DH * 2) + (lane >> 4) * 16, bf, lane);
  const int col = m * 80 + c * 16 + (lane & 15);
  if (lane < 32) st_gran(p.ws, W_SP + (((lane >> 4) * 80 + (col >> 3)) * NH + h) * 64 + (col & 7) * 8, sv, tag);
  lds_set(ctl + C_T3 + c, 1u);
  if (c == 0) STAMP(12);
}

}

#define PROBE 2

namespace {
__device__ __forceinline__ bool resident(const unsigned char* base, size_t sz) {
  unsigned acc = 0;
  { const unsigned* p = (const unsigned*)(base + 128); unsigned v; asm volatile("global_load_dword %0, %1, off nt\n\ts_waitcnt vmcnt(0)" : "=v"(v) : "v"(p) : "memory"); acc += v;
    p = (const unsigned*)(base + sz - 128); asm volatile("global_load_dword %0, %1, off nt\n\ts_waitcnt vmcnt(0)" : "=v"(v) : "v"(p) : "memory"); acc += v;
    p = (const unsigned*)(base + sz / 2 / 128 * 128 + 128); asm volatile("global_load_dword %0, %1, off nt\n\ts_waitcnt vmcnt(0)" : "=v"(v) : "v"(p) : "memory"); acc += v; }
  const unsigned long long t0 = __builtin_amdgcn_s_memrealtime();
  for (int i = 0; i < 8; ++i) {
    const unsigned* p = (const unsigned*)(base + (sz / 8 / 256 * 256) * i + 512);
    unsigned v; asm volatile("global_load_dword %0, %1, off nt\n\ts_waitcnt vmcnt(0)" : "=v"(v) : "v"(p) : "memory"); acc += v;
  }
  const unsigned long long t1 = __builtin_amdgcn_s_memrealtime();
  return (t1 - t0) + (acc == 0x13572468u) < 255;
}
__global__ void k_probe(Params p) {
  const unsigned long long t0 = __builtin_amdgcn_s_memrealtime();
  int n = 0;
  const unsigned char* K = (const unsigned char*)p.k; const unsigned char* V = (const unsigned char*)p.v;
  if (PROBE == 0) { for (int s = 0; s < 64; ++s) n += resident(V + (size_t)s * SLAB, SLAB); }
  if (PROBE == 1) { for (int s = 0; s < 64; ++s) n += resident(K + (size_t)s * SLAB, SLAB); }
  if (PROBE == 2) { n = resident((const unsigned char*)p.wq, 7864320) + 2 * resident((const unsigned char*)p.wo, 7864320) + 4 * resident((const unsigned char*)p.wg, 1966080) + 8 * resident((const unsigned char*)p.wu, 1966080) + 16 * resident((const unsigned char*)p.wd, 1966080); }
  if (PROBE == 3) { n = 64; int run = 0; for (int s = 63; s >= 0; --s) { if (resident(V + (size_t)s * SLAB, SLAB)) n = s; } }
  if (PROBE == 4) { n = 64; for (int s = 63; s >= 0; --s) { if (resident(K + (size_t)s * SLAB, SLAB)) n = s; } }
  if (PROBE == 5) { for (int s = 0; s < 64; ++s) n += resident(p.ws + (size_t)s * 12582912 + (8 << 20), SLAB); }
  while (__builtin_amdgcn_s_memrealtime() - t0 < 40000ull + 1000ull * n) __builtin_amdgcn_s_sleep(8);
}
}
extern "C" void kernel_launch(void* const* d_in, const int* in_sizes, int n_in, void* d_out, int out_size, void* d_ws,
                              size_t ws_size, hipStream_t stream) {
  Params p;
  p.x = (const unsigned short*)d_in[0];
  p.k = (const unsigned short*)d_in[1];
  p.v = (const unsigned short*)d_in[2];
  p.wq = (const unsigned short*)d_in[3];
  p.wo = (const unsigned short*)d_in[4];
  p.wr = (const unsigned short*)d_in[6];
  p.wg = (const unsigned short*)d_in[7];
  p.wu = (const unsigned short*)d_in[8];
  p.wd = (const unsigned short*)d_in[9];
  p.out = (unsigned short*)d_out;
  p.ws = (unsigned char*)d_ws;
  (void)hipFuncSetAttribute((const void*)fused_decode, hipFuncAttributeMaxDynamicSharedMemorySize, L_END);
  k_probe<<<1, 64, 0, stream>>>(p);
  fused_decode<<<NWG, NTHR, L_END, stream>>>(p);
}
```

```cpp
#define K_STAMPS
#define IDX 3
#define MODE 1
#define SKIPMLP 0
#include <hip/hip_runtime.h>
#include <hip/hip_bf16.h>

namespace {

#define GAS __attribute__((address_space(1)))
#define LAS __attribute__((address_space(3)))
#define RLX_AGENT __ATOMIC_RELAXED, __HIP_MEMORY_SCOPE_AGENT
#define RLX_WG __ATOMIC_RELAXED, __HIP_MEMORY_SCOPE_WORKGROUP
typedef unsigned v4u __attribute__((ext_vector_type(4)));
typedef unsigned v2u __attribute__((ext_vector_type(2)));
typedef float v4f __attribute__((ext_vector_type(4)));
typedef float v2f __attribute__((ext_vector_type(2)));
typedef short v4s __attribute__((ext_vector_type(4)));
typedef __bf16 bf16x2 __attribute__((ext_vector_type(2)));
typedef __bf16 bf16x8 __attribute__((ext_vector_type(8)));
typedef unsigned long long u64;

#define K_INF 21
#define K_NSLOT 22
#define K_ORDER 0
#define K_PK0 0
#define K_PK1 0
#define K_PV0 0
#define K_PV1 0
#define K_PW 0

constexpr int DM = 640, NH = 32, DH = 192, SK = 8192, FF = 1536;
constexpr int NWG = 256, NC = 6, NTHR = (NC + 2) * 64;
constexpr int INF = K_INF;
constexpr int NSLOT = K_NSLOT;
constexpr int ORDER = K_ORDER;
constexpr int SLOTB = 6144, NWO = 5, NS_WQ = 7, NS_GU = 27, NS_DN = 16;
constexpr unsigned KV_BYTES = 2u * NH * SK * DH * 2u;
constexpr int SLAB = SK * DH * 2;
constexpr int NMLP = FF / 64;
constexpr int NFIN = 2 * DM / 8;
static_assert(INF >= 7 && INF <= 60, "vmcnt is 6 bits");

constexpr int L_XP = NSLOT * SLOTB;
constexpr int L_SCR = L_XP + 2560;
constexpr int L_QB = L_SCR + 16384;
constexpr int L_OA = L_QB + 768;
constexpr int L_HM = L_OA + 768;
constexpr int L_CTL = L_HM + 256;
constexpr int L_END = L_CTL + 320;
static_assert(L_END <= 160 * 1024 && L_END > 80 * 1024, "one workgroup per CU");
enum { C_NEXT = 0, C_P1 = 8, C_HM = 16, C_Y = 24, C_R0 = 32, C_R1 = 40, C_O = 48, C_T3 = 56, C_LANDED = 64, C_XOK, C_TAG, C_Q0, C_Q1 };

constexpr int W_EPOCH = 0;
constexpr int W_ML = 1024;
constexpr int W_OP = W_ML + 2 * NH * 8 * 16;
constexpr int W_SP = W_OP + 2 * NH * 8 * DH * 8;
constexpr int W_YP = W_SP + NFIN * NH * 64;
constexpr int W_QP = W_YP + NFIN * NMLP * 64;
constexpr int W_END = W_QP + NH * 2 * 384 * 8;
constexpr int W_ST = 4 << 20;

constexpr unsigned LDS_SPIN = 1u << 20, GLB_SPIN = 1u << 16;
constexpr float LOG2E = 1.4426950408889634f;

struct Params {
  const unsigned short *x, *k, *v, *wq, *wo, *wr, *wg, *wu, *wd;
  unsigned short* out;
  unsigned char* ws;
};

__device__ __forceinline__ int rfl(int v) { return __builtin_amdgcn_readfirstlane(v); }
__device__ __forceinline__ float bfr(float x) { return (float)(__bf16)x; }
__device__ __forceinline__ unsigned bfbits(float x) { return (unsigned)__builtin_bit_cast(unsigned short, (__bf16)x); }
__device__ __forceinline__ float lo16(unsigned u) { return __builtin_bit_cast(float, u << 16); }
__device__ __forceinline__ float hi16(unsigned u) { return __builtin_bit_cast(float, u & 0xffff0000u); }
__device__ __forceinline__ float asf(unsigned u) { return __builtin_bit_cast(float, u); }
__device__ __forceinline__ unsigned asu(float f) { return __builtin_bit_cast(unsigned, f); }
__device__ __forceinline__ float dot2(unsigned a, unsigned b, float c) {
  return __builtin_amdgcn_fdot2_f32_bf16(__builtin_bit_cast(bf16x2, a), __builtin_bit_cast(bf16x2, b), c, false);
}
template <int CTRL> __device__ __forceinline__ float dpp(float x) {
  return __builtin_bit_cast(float, __builtin_amdgcn_mov_dpp(__builtin_bit_cast(int, x), CTRL, 0xf, 0xf, true));
}
template <int CTRL> __device__ __forceinline__ int dppi(int x) { return __builtin_amdgcn_mov_dpp(x, CTRL, 0xf, 0xf, true); }
constexpr int XOR1 = 0xB1, XOR2 = 0x4E, MIR8 = 0x141, ROR4 = 0x124, ROR8 = 0x128;
__device__ __forceinline__ float fold32(float a, float b) {
  auto s = __builtin_amdgcn_permlane32_swap(asu(a), asu(b), false, false);
  return asf(s[0]) + asf(s[1]);
}
__device__ __forceinline__ float fold16(float a, float b) {
  auto s = __builtin_amdgcn_permlane16_swap(asu(a), asu(b), false, false);
  return asf(s[0]) + asf(s[1]);
}
__device__ __forceinline__ float max_rows(float x) {
  auto s = __builtin_amdgcn_permlane16_swap(asu(x), asu(x), false, false);
  x = fmaxf(asf(s[0]), asf(s[1]));
  auto t = __builtin_amdgcn_permlane32_swap(asu(x), asu(x), false, false);
  return fmaxf(asf(t[0]), asf(t[1]));
}
__device__ __forceinline__ float sum_rows(float x) { x = fold16(x, x); return fold32(x, x); }
__device__ __forceinline__ float wave_sum(float x) {
  x += dpp<XOR1>(x); x += dpp<XOR2>(x); x += dpp<ROR4>(x); x += dpp<ROR8>(x);
  return sum_rows(x);
}
__device__ __forceinline__ float exp2f_(float x) { return __builtin_amdgcn_exp2f(x); }
__device__ __forceinline__ float siluf(float x) { return x / (1.0f + expf(-x)); }
#define CBAR() asm volatile("" ::: "memory")

__device__ __forceinline__ __amdgpu_buffer_rsrc_t rsrc(const void* p, unsigned bytes) {
  return __builtin_amdgcn_make_buffer_rsrc((void*)p, 0, bytes, 0x00020000);
}
__device__ __forceinline__ void lds_set(LAS unsigned* w, unsigned v) {
  CBAR();
  __hip_atomic_store(w, v, RLX_WG);
  CBAR();
}
template <int SLEEP> __device__ __forceinline__ void lds_wait(LAS unsigned* w, unsigned want) {
#pragma nounroll
  for (unsigned n = 0; n < LDS_SPIN; ++n) {
    if ((unsigned)rfl((int)__hip_atomic_load(w, RLX_WG)) >= want) break;
    if (SLEEP) __builtin_amdgcn_s_sleep(SLEEP);
  }
  CBAR();
}
template <int N, int SLEEP> __device__ __forceinline__ void lds_wait_all(LAS unsigned* w, int j) {
  LAS unsigned* a = w + (j < N ? j : 0);
#pragma nounroll
  for (unsigned n = 0; n < LDS_SPIN; ++n) {
    if (__all(__hip_atomic_load(a, RLX_WG) != 0u)) break;
    if (SLEEP) __builtin_amdgcn_s_sleep(SLEEP);
  }
  CBAR();
}
__device__ __forceinline__ void st_gran(unsigned char* ws, int off, float v, unsigned tag) {
  __hip_atomic_store((GAS u64*)(ws + off), ((u64)tag << 32) | asu(v), RLX_AGENT);
}
__device__ __forceinline__ u64 ld_gran(const unsigned char* ws, int off) { return __hip_atomic_load((GAS u64*)(ws + off), RLX_AGENT); }

#define STAMP(i) do { if (lane == 0) ((GAS u64*)(p.ws + W_ST))[blockIdx.x * 32 + (i)] = __builtin_amdgcn_s_memrealtime(); } while (0)

__device__ __forceinline__ void layer_norm(v2u (&xr)[5], LAS unsigned char* L, int lane) {
  asm volatile("" : "+v"(xr[0]), "+v"(xr[1]), "+v"(xr[2]), "+v"(xr[3]), "+v"(xr[4]));
  v2f x[10];
#pragma unroll
  for (int i = 0; i < 5; ++i) { x[2 * i] = v2f{lo16(xr[i].x), hi16(xr[i].x)}; x[2 * i + 1] = v2f{lo16(xr[i].y), hi16(xr[i].y)}; }
  const float p0 = asf((unsigned)__builtin_amdgcn_readlane((int)asu(x[0].x), 0)), p1 = asf((unsigned)__builtin_amdgcn_readlane((int)asu(x[0].x), 32));
  const float piv = lane < 32 ? p0 : p1;
  v2f s1 = {0.f, 0.f}, s2 = {0.f, 0.f};
#pragma unroll
  for (int i = 0; i < 10; ++i) { const v2f d = x[i] - piv; s1 += d; s2 += d * d; }
  float a = s1.x + s1.y, q = s2.x + s2.y;
  a += dpp<XOR1>(a); q += dpp<XOR1>(q); a += dpp<XOR2>(a); q += dpp<XOR2>(q);
  a += dpp<ROR4>(a); q += dpp<ROR4>(q); a += dpp<ROR8>(a); q += dpp<ROR8>(q);
  a = fold16(a, a) * (1.0f / DM); q = fold16(q, q) * (1.0f / DM);
  const float mb = bfr(piv + a), var = bfr(fmaf(-a, a, q));
  const float den = bfr(__builtin_amdgcn_sqrtf(bfr(var + 1e-5f)));
  const float rcp = __builtin_amdgcn_rcpf(den);
#pragma unroll
  for (int i = 0; i < 5; ++i) {
    unsigned o[2];
#pragma unroll
    for (int e = 0; e < 2; ++e) {
      const v2f t = x[2 * i + e] - mb;
      const v2f d = {bfr(t.x), bfr(t.y)}, q0 = d * rcp;
      const v2f qq = (d - q0 * den) * rcp + q0;
      o[e] = bfbits(qq.x) | (bfbits(qq.y) << 16);
    }
    *(LAS v2u*)(L + L_XP + (lane >> 5) * 1280 + ((lane & 31) + 32 * i) * 8) = v2u{o[0], o[1]};
  }
}

__device__ __forceinline__ void gather_q(const Params& p, LAS unsigned char* L, int h, int b, unsigned tag, int lane) {
  const __amdgpu_buffer_rsrc_t rw = rsrc(p.ws, W_END);
  const int l = min(lane, 47), off = W_QP + (h * 768 + b * DH + 4 * l) * 8;
  v4u g[4];
#pragma nounroll
  for (unsigned n = 0; n < GLB_SPIN; ++n) {
    CBAR();
#pragma unroll
    for (int i = 0; i < 4; ++i) g[i] = __builtin_amdgcn_raw_buffer_load_b128(rw, off + (i & 1) * 16 + (i >> 1) * 3072, 0, 16);
    bool ok = true;
#pragma unroll
    for (int i = 0; i < 4; ++i) ok &= g[i].y == tag && g[i].w == tag;
    if (__all(ok)) break;
  }
  *(LAS v2u*)(L + L_QB + (b * DH + 4 * l) * 2) = v2u{bfbits(asf(g[0].x) + asf(g[2].x)) | (bfbits(asf(g[0].z) + asf(g[2].z)) << 16),
                                                      bfbits(asf(g[1].x) + asf(g[3].x)) | (bfbits(asf(g[1].z) + asf(g[3].z)) << 16)};
}

template <int NK, int KSTEP, int HALF>
__device__ __forceinline__ void load_bt(const LAS unsigned char* b, v4u (&f)[NK]) {
#pragma unroll
  for (int ks = 0; ks < NK; ++ks) {
    const v2u lo = __builtin_bit_cast(v2u, __builtin_amdgcn_ds_read_tr16_b64_v4i16((LAS v4s*)(b + ks * KSTEP)));
    const v2u hi = __builtin_bit_cast(v2u, __builtin_amdgcn_ds_read_tr16_b64_v4i16((LAS v4s*)(b + ks * KSTEP + HALF)));
    f[ks] = v4u{lo.x, lo.y, hi.x, hi.y};
  }
}
template <int NK> __device__ __forceinline__ float mm2(const LAS unsigned char* a, const v4u (&f)[NK], int lane) {
  v4u av[NK];
#pragma unroll
  for (int ks = 0; ks < NK; ++ks) av[ks] = *(const LAS v4u*)(a + ks * 64);
  v4f c = {0.f, 0.f, 0.f, 0.f};
#pragma unroll
  for (int ks = 0; ks < NK; ++ks) c = __builtin_amdgcn_mfma_f32_16x16x32_bf16(__builtin_bit_cast(bf16x8, av[ks]), __builtin_bit_cast(bf16x8, f[ks]), c, 0, 0, 0);
  return lane & 16 ? c[1] : c[0];
}

template <int ND, int SLEEP>
__device__ __forceinline__ void combine(const Params& p, LAS unsigned char* L, int ph, int h, int m, int d0, bool lead, unsigned tag, int lane) {
  const int b = ph ^ ORDER;
  const LAS float* rec = (const LAS float*)(L + L_SCR) + ph * NC * 200;
  float M = rec[192];
#pragma unroll
  for (int c = 1; c < NC; ++c) M = fmaxf(M, rec[c * 200 + 192]);
  float Ls = 0.f, o[ND];
#pragma unroll
  for (int i = 0; i < ND; ++i) o[i] = 0.f;
#pragma unroll
  for (int c = 0; c < NC; ++c) {
    const float w = exp2f_(rec[c * 200 + 192] - M);
    Ls = fmaf(w, rec[c * 200 + 193], Ls);
#pragma unroll
    for (int i = 0; i < ND; ++i) o[i] = fmaf(w, rec[c * 200 + d0 + 64 * i], o[i]);
  }
  const int bh = b * NH + h;
  const __amdgpu_buffer_rsrc_t rw = rsrc(p.ws, W_END);
#pragma unroll
  for (int i = 0; i < ND; ++i) st_gran(p.ws, W_OP + ((bh * 8 + m) * DH + d0 + 64 * i) * 8, o[i], tag);
  if (lead && lane == 0) __builtin_amdgcn_raw_buffer_store_b128(v4u{asu(M), tag, asu(Ls), tag}, rw, W_ML + (bh * 8 + m) * 16, 0, 16);
  u64 g[ND][8];
  v4u ml;
#pragma nounroll
  for (unsigned n = 0; n < GLB_SPIN; ++n) {
    CBAR();
    ml = __builtin_amdgcn_raw_buffer_load_b128(rw, W_ML + (bh * 8 + (lane & 7)) * 16, 0, 16);
#pragma unroll
    for (int i = 0; i < ND; ++i)
#pragma unroll
      for (int j = 0; j < 8; ++j) g[i][j] = ld_gran(p.ws, W_OP + ((bh * 8 + j) * DH + d0 + 64 * i) * 8);
    bool ok = ml.y == tag && ml.w == tag;
#pragma unroll
    for (int i = 0; i < ND; ++i)
#pragma unroll
      for (int j = 0; j < 8; ++j) ok &= (unsigned)(g[i][j] >> 32) == tag;
    if (__all(ok)) break;
    if (SLEEP) __builtin_amdgcn_s_sleep(SLEEP);
  }
  float Mg = asf(ml.x);
  Mg = fmaxf(Mg, dpp<XOR1>(Mg)); Mg = fmaxf(Mg, dpp<XOR2>(Mg)); Mg = fmaxf(Mg, dpp<MIR8>(Mg));
  const float w = exp2f_(asf(ml.x) - Mg);
  float Lg = w * asf(ml.z);
  Lg += dpp<XOR1>(Lg); Lg += dpp<XOR2>(Lg); Lg += dpp<MIR8>(Lg);
  const float wn = w / Lg;
#pragma unroll
  for (int i = 0; i < ND; ++i) {
    float a = 0.f;
#pragma unroll
    for (int j = 0; j < 8; ++j) a = fmaf(asf((unsigned)__builtin_amdgcn_readlane((int)asu(wn), j)), asf((unsigned)g[i][j]), a);
    *(LAS unsigned short*)(L + L_OA + (b * DH + d0 + 64 * i) * 2) = (unsigned short)bfbits(a);
  }
}

template <int NP, int SLEEP>
__device__ __forceinline__ v2f gather8(const Params& p, int base, unsigned tag, int lane) {
  static_assert(NP > 16 && NP <= 32, "two 16-byte pieces per lane");
  const __amdgpu_buffer_rsrc_t rw = rsrc(p.ws, W_END);
  const bool two = 64 + lane < NP * 4;
  v4u a, c;
#pragma nounroll
  for (unsigned n = 0; n < GLB_SPIN; ++n) {
    CBAR();
    a = __builtin_amdgcn_raw_buffer_load_b128(rw, base + lane * 16, 0, 16);
    c = __builtin_amdgcn_raw_buffer_load_b128(rw, base + (two ? 64 + lane : lane) * 16, 0, 16);
    if (__all(a.y == tag && a.w == tag && c.y == tag && c.w == tag)) break;
    if (SLEEP) __builtin_amdgcn_s_sleep(SLEEP);
  }
  float s0 = asf(a.x) + (two ? asf(c.x) : 0.f), s1 = asf(a.z) + (two ? asf(c.z) : 0.f);
  s0 += dpp<ROR4>(s0); s1 += dpp<ROR4>(s1);
  s0 += dpp<ROR8>(s0); s1 += dpp<ROR8>(s1);
  return v2f{sum_rows(s0), sum_rows(s1)};
}

#define DMA3(POL)                                                                    \
  asm volatile("s_mov_b32 m0, %3\n\t"                                                \
               "s_waitcnt vmcnt(%4)\n\t"                                             \
               "buffer_load_dwordx4 %0, %1, %2 offen " POL "lds\n\t"                 \
               "s_waitcnt vmcnt(%4)\n\t"                                             \
               "buffer_load_dwordx4 %0, %1, %2 offen offset:1024 " POL "lds\n\t"     \
               "s_waitcnt vmcnt(%4)\n\t"                                             \
               "buffer_load_dwordx4 %0, %1, %2 offen offset:2048 " POL "lds"         \
               :: "v"(voff), "s"(srd), "s"(soff), "s"(lds), "i"(N) : "memory")
#define DMA1(POL)                                                                    \
  asm volatile("s_mov_b32 m0, %3\n\t"                                                \
               "s_waitcnt vmcnt(%4)\n\t"                                             \
               "buffer_load_dwordx4 %0, %1, %2 offen " POL "lds"                     \
               :: "v"(voff), "s"(srd), "s"(soff), "s"(lds), "i"(N) : "memory")
template <int N, int POL> __device__ __forceinline__ void dma3(int voff, v4u srd, int soff, unsigned lds) {
  if constexpr (POL == 0) DMA3("nt "); else DMA3("");
}
template <int N, int POL> __device__ __forceinline__ void dma1(int voff, v4u srd, int soff, unsigned lds) {
  if constexpr (POL == 0) DMA1("nt "); else DMA1("");
}
__device__ __forceinline__ v4u srd_of(const void* ptr, unsigned bytes) {
  const u64 a = (u64)ptr;
  return v4u{(unsigned)a, (unsigned)(a >> 32) & 0xffffu, bytes, 0x00020000u};
}
constexpr int fdiv6(int a) { return a >= 0 ? a / 6 : -((5 - a) / 6); }
constexpr int LAG3 = fdiv6(3 - INF), LAG6 = fdiv6(6 - INF);

struct Loader {
  LAS unsigned* ctl;
  unsigned lbase;
  int s, pos, limit, j;
  __device__ __forceinline__ void credit(int last) {
    if (__builtin_expect(last >= limit, 0)) {
#pragma nounroll
      for (unsigned n = 0; n < LDS_SPIN; ++n) {
        int v = (int)__hip_atomic_load(ctl + C_NEXT + (j < NC ? j : 0), RLX_WG);
        v = min(v, dppi<XOR1>(v)); v = min(v, dppi<XOR2>(v)); v = min(v, dppi<MIR8>(v));
        limit = rfl(v) + NSLOT;
        if (last < limit) break;
        __builtin_amdgcn_s_sleep(1);
      }
      CBAR();
    }
  }
  __device__ __forceinline__ void publish(int n) { __hip_atomic_store(ctl + C_LANDED, (unsigned)max(n, 0), RLX_WG); }
  __device__ __forceinline__ unsigned dst() const { return lbase + pos * SLOTB; }
  __device__ __forceinline__ void advance() { ++s; pos = pos + 1 == NSLOT ? 0 : pos + 1; }
  template <int PK, int PV> __device__ __forceinline__ void kv(int i0, int i1, int n1, int st, int m, int slab, int voff, v4u rk, v4u rv) {
#pragma nounroll
    for (int i = i0; i < i1; ++i) {
      credit(s + 3);
      const int piece = i < n1 ? min(st * i + m, 63) : 64 + 8 * (i - n1) + m;
      const int so = slab + (piece & 255) * 12288;
#pragma unroll
      for (int u = 0; u < 4; ++u) {
        dma3<INF - 1, PK>(voff, rk, so + u * 3072, dst());
        if (LAG3 == LAG6) publish(s + LAG3);
        dma3<INF - 1, PV>(voff, rv, so + u * 3072, dst() + 3072);
        if (LAG3 != LAG6) publish(s + LAG6);
        advance();
      }
    }
  }
};

__device__ __forceinline__ void dot16(float (&a)[16], v4u w, unsigned x0l, unsigned x1l) {
  const unsigned x0h = x0l << 16, x1h = x1l << 16;
#pragma unroll
  for (int e = 0; e < 4; ++e) {
    a[2 * e] = dot2(w[e], x0l, a[2 * e]);         a[2 * e + 1] = dot2(w[e], x0h, a[2 * e + 1]);
    a[8 + 2 * e] = dot2(w[e], x1l, a[8 + 2 * e]); a[9 + 2 * e] = dot2(w[e], x1h, a[9 + 2 * e]);
  }
}

__global__ void __launch_bounds__(NTHR, 2) fused_decode(Params p) {
  extern __shared__ __attribute__((aligned(16))) unsigned char smem[];
  LAS unsigned char* L = (LAS unsigned char*)smem;
  LAS unsigned* ctl = (LAS unsigned*)(L + L_CTL);
  const int tid = threadIdx.x, lane = tid & 63, wave = rfl(tid >> 6);
  const int bid = blockIdx.x, h = bid & 31, m = bid >> 5;
  const int r = lane >> 3, j = lane & 7;
  const bool has_wq = m < 6, has_mlp = m == 7 && h < NMLP;
  const int npro = has_wq ? NS_WQ : has_mlp ? NS_GU + NS_DN : 0;
  const int st = h < NMLP ? 7 : 8, n1 = h < NMLP ? (m < 6 ? 9 : m == 6 ? 10 : 0) : 8;
  const int ntot = npro + 4 * (n1 + 56);
  const int wo_skip = ntot % NSLOT + NWO > NSLOT ? NSLOT - ntot % NSLOT : 0, wo_pos = (ntot + wo_skip) % NSLOT;

  if (wave == 0) {
    __builtin_amdgcn_s_setprio(3);
    STAMP(0);
    Loader ld{ctl, (unsigned)(size_t)L, 0, 0, NSLOT, j};
    const v4u rk = srd_of(p.k, KV_BYTES), rv = srd_of(p.v, KV_BYTES);
    const int voff = lane * 16;
    const int slab0 = (ORDER * NH + h) * SLAB, slab1 = ((ORDER ^ 1) * NH + h) * SLAB;
    int i0 = 0;
    if (has_wq) {
      const v4u rq = srd_of(p.wq, DM * NH * DH * 2);
      const int vo = r * (NH * DH * 2) + (j ^ (r & 2)) * 16, so = (m / 3) * 320 * (NH * DH * 2) + h * (DH * 2) + (m % 3) * 128;
#pragma unroll 6
      for (int g = 0; g < 6 * NS_WQ; ++g) dma1<63, K_PW>(g & 1 ? vo ^ 64 : vo, rq, so + min(g, 39) * (8 * NH * DH * 2), ld.lbase + g * 1024);
      ld.s = ld.pos = NS_WQ;
      __builtin_amdgcn_s_barrier();
    } else if (has_mlp) {
      __builtin_amdgcn_s_barrier();
      const v4u rg = srd_of(p.wg, DM * FF * 2), ru = srd_of(p.wu, DM * FF * 2), rd = srd_of(p.wd, FF * DM * 2);
      const int vo = r * (FF * 2) + j * 16, ve = (lane >> 4) * (DM * 2) + 1024 + (lane & 15) * 16;
#pragma nounroll
      for (int t = 0; t < NS_GU; ++t) {
        ld.credit(ld.s);
#pragma unroll
        for (int q = 0; q < 6; ++q) dma1<INF - 1, K_PW>(vo, q & 1 ? ru : rg, min(3 * t + (q >> 1), 79) * (8 * FF * 2) + h * 128, ld.dst() + q * 1024);
        ld.publish(ld.s + LAG6);
        ld.advance();
      }
#pragma nounroll
      for (int t = 0; t < NS_DN; ++t) {
        ld.credit(ld.s);
#pragma unroll
        for (int q = 0; q < 6; ++q) dma1<INF - 1, K_PW>(q < 4 ? voff : ve, rd, (64 * h + 4 * t + (q < 4 ? q : 0)) * (DM * 2), ld.dst() + q * 1024);
        ld.publish(ld.s + LAG6);
        ld.advance();
      }
    } else {
      const int so = slab0 + (n1 ? m : 64 + m) * 12288;
#pragma unroll
      for (int u = 0; u < 4; ++u) {
        dma3<63, ORDER ? K_PK1 : K_PK0>(voff, rk, so + u * 3072, ld.dst());
        dma3<63, ORDER ? K_PV1 : K_PV0>(voff, rv, so + u * 3072, ld.dst() + 3072);
        ld.advance();
      }
      i0 = 1;
      __builtin_amdgcn_s_barrier();
    }
    CBAR();
    ld.kv<ORDER ? K_PK1 : K_PK0, ORDER ? K_PV1 : K_PV0>(i0, n1 + 24, n1, st, m, slab0, voff, rk, rv);
    const u64 tmid = __builtin_amdgcn_s_memrealtime();
    ld.kv<ORDER ? K_PK0 : K_PK1, ORDER ? K_PV0 : K_PV1>(n1 + 24, n1 + 56, n1, st, m, slab1, voff, rk, rv);
    STAMP(1);
    ((GAS u64*)(p.ws + W_ST))[blockIdx.x * 32 + 7] = tmid;
    const v4u ro = srd_of(p.wo, NH * DH * DM * 2);
    int row = lane / 10, ch = lane - row * 10;
    ld.credit(ld.s + wo_skip + NWO - 1);
    ld.pos = wo_pos;
#pragma nounroll
    for (int t = 0; t < NWO; ++t) {
#pragma unroll
      for (int q = 0; q < 6; ++q) {
        dma1<INF - 1, K_PW>(row * (DM * 2) + ch * 16, ro, h * DH * (DM * 2) + m * 160, ld.dst() + q * 1024);
        if (q == 2 && LAG3 == LAG6) ld.publish(ld.s + LAG3);
        if (q == 5 && LAG3 != LAG6) ld.publish(ld.s + LAG6);
        row += 6; ch += 4;
        if (ch >= 10) { ch -= 10; row += 1; }
      }
      ld.advance();
    }
    if (INF > 30) { asm volatile("s_waitcnt vmcnt(30)" ::: "memory"); ld.publish(ld.s - NWO); }
    asm volatile("s_waitcnt vmcnt(0)" ::: "memory");
    ld.publish(ld.s);
    STAMP(2);
    return;
  }

  if (wave == NC + 1) {
    v2u xr[5];
#pragma unroll
    for (int i = 0; i < 5; ++i) xr[i] = ((const GAS v2u*)p.x)[(lane >> 5) * 160 + (lane & 31) + 32 * i];
    const unsigned e0 = __hip_atomic_load((GAS unsigned*)(p.ws + W_EPOCH), RLX_AGENT);
    ctl[lane] = lane < NC && (npro == 0 || (has_wq && lane >= 4)) ? npro + lane : 0;
    if (lane < 16) ctl[64 + lane] = 0u;
    asm volatile("s_waitcnt lgkmcnt(0)" ::: "memory");
    __builtin_amdgcn_s_barrier();
    CBAR();
    layer_norm(xr, L, lane);
    const unsigned tag = (unsigned)rfl((int)e0) + 1u;
    ctl[C_TAG] = tag;
    lds_set(ctl + C_XOK, 1u);
    STAMP(3);
    lds_wait<8>(ctl + C_Q0, 1);
    gather_q(p, L, h, ORDER ^ 1, tag, lane);
    lds_set(ctl + C_Q1, 1u);
    lds_wait_all<NC, 16>(ctl + C_R0, j);
    combine<3, 8>(p, L, 0, h, m, lane, true, tag, lane);
    lds_set(ctl + C_O + 3, 1u);
    STAMP(4);
    if (bid >= NFIN) return;
    const int fb = bid / 80, col = (bid - fb * 80) * 8 + 2 * (lane & 3);
    float rs = 0.f;
#pragma unroll
    for (int i = 0; i < 5; ++i) {
      const unsigned w = ((const GAS unsigned*)p.wr)[lane + 64 * i];
      rs = dot2(w, *(const LAS unsigned*)(L + L_XP + fb * 1280 + (lane + 64 * i) * 4), rs);
    }
    const float sg = bfr(1.0f / (1.0f + expf(-bfr(wave_sum(rs)))));
    const unsigned xo = ((const GAS unsigned*)p.x)[(fb * DM + col) >> 1];
    const v2f y = gather8<NMLP, 8>(p, W_YP + bid * NMLP * 64, tag, lane);
    const unsigned xx = *(const LAS unsigned*)(L + L_XP + fb * 1280 + col * 2);
    const float g0 = bfr(siluf(bfr(lo16(xx) + bfr(bfr(y.x) * sg))));
    const float g1 = bfr(siluf(bfr(hi16(xx) + bfr(bfr(y.y) * sg))));
    STAMP(5);
    lds_wait_all<5, 2>(ctl + C_T3, j);
    const v2f s = gather8<NH, 0>(p, W_SP + bid * NH * 64, tag, lane);
    const unsigned o = bfbits(lo16(xo) + bfr(bfr(s.x) * g0)) | (bfbits(hi16(xo) + bfr(bfr(s.y) * g1)) << 16);
    if (lane < 4) ((GAS unsigned*)p.out)[(fb * DM + col) >> 1] = o;
    if (bid == 0 && lane == 0) __hip_atomic_store((GAS unsigned*)(p.ws + W_EPOCH), tag, RLX_AGENT);
    STAMP(6);
    return;
  }

  const int c = wave - 1;
  __builtin_amdgcn_s_barrier();
  CBAR();
  lds_wait<1>(ctl + C_XOK, 1);
  const unsigned tag = (unsigned)rfl((int)ctl[C_TAG]);
  const __amdgpu_buffer_rsrc_t rw = rsrc(p.ws, W_END);
  if (c == 0) STAMP(13);
  if (has_wq) {
    if (c < 4) {
      const int sw = ((lane >> 3) & 1) | ((lane >> 3) & 2);
      lds_wait<0>(ctl + C_LANDED, NS_WQ);
      v4u bf[10];
      load_bt<10, 4096, 512>(L + (lane >> 4) * 1024 + ((lane & 15) >> 2) * 128 + ((2 * c + ((lane & 3) >> 1)) ^ (2 * sw)) * 16 + (lane & 1) * 8, bf);
      lds_set(ctl + C_NEXT + c, NS_WQ + c);
      const float qv = mm2<10>(L + L_XP + (lane & 1) * 1280 + (m / 3) * 640 + (lane >> 4) * 16, bf, lane);
      if (lane < 32) st_gran(p.ws, W_QP + ((h * 2 + m / 3) * 384 + (lane >> 4) * DH + (m % 3) * 64 + 16 * c + (lane & 15)) * 8, qv, tag);
    }
  } else if (has_mlp) {
    const LAS unsigned short* xp = (const LAS unsigned short*)(L + L_XP);
    int pos = 0;
    float a[16];
#pragma unroll
    for (int i = 0; i < 16; ++i) a[i] = 0.f;
#pragma nounroll
    for (int t = 0; t < NS_GU; ++t) {
      const int g = 3 * t + (c >> 1);
      lds_wait<1>(ctl + C_LANDED, t + 1);
      const v4u w = *(const LAS v4u*)(L + pos * SLOTB + c * 1024 + lane * 16);
      const unsigned x0 = xp[min(g, 79) * 8 + r], x1 = xp[DM + min(g, 79) * 8 + r];
      lds_set(ctl + C_NEXT + c, t + 1 == NS_GU && c == 5 ? NS_GU + NS_DN + c : t + 1);
      if (g < 80) dot16(a, w, x0, x1);
      pos = pos + 1 == NSLOT ? 0 : pos + 1;
    }
#pragma unroll
    for (int i = 0; i < 8; ++i) a[i] = fold32(a[i], a[i + 8]);
#pragma unroll
    for (int i = 0; i < 4; ++i) { a[i] = fold16(a[i], a[i + 4]); a[i] += dpp<ROR8>(a[i]); }
    if ((r & 1) == 0) *(LAS v4f*)(L + L_SCR + (c * 128 + (lane >> 5) * 64 + 8 * j + 4 * ((lane >> 4) & 1)) * 4) = v4f{a[0], a[1], a[2], a[3]};
    lds_set(ctl + C_P1 + c, 1u);
    if (c < 2) {
      lds_wait_all<NC, 0>(ctl + C_P1, j);
      const LAS float* pp = (const LAS float*)(L + L_SCR) + c * 64 + lane;
      const float s0 = pp[0] + pp[256] + pp[512], s1 = pp[128] + pp[384] + pp[640];
      *(LAS unsigned short*)(L + L_HM + lane * 4 + c * 2) = (unsigned short)bfbits(bfr(siluf(bfr(s0))) * bfr(s1));
      lds_set(ctl + C_HM + c, 1u);
    }
    if (c < 5) {
      lds_wait_all<2, 1>(ctl + C_HM, j);
#pragma unroll
      for (int i = 0; i < 16; ++i) a[i] = 0.f;
#pragma nounroll
      for (int t = 0; t < NS_DN; ++t) {
        lds_wait<1>(ctl + C_LANDED, NS_GU + t + 1);
        const v4u w = *(const LAS v4u*)(L + pos * SLOTB + c * 1024 + lane * 16);
        const unsigned xx = ((const LAS unsigned*)(L + L_HM))[4 * t + (c < 4 ? c : lane >> 4)];
        lds_set(ctl + C_NEXT + c, t + 1 == NS_DN ? NS_GU + NS_DN + c : NS_GU + t + 1);
        dot16(a, w, xx & 0xffffu, xx >> 16);
        pos = pos + 1 == NSLOT ? 0 : pos + 1;
      }
      if (c < 4) {
#pragma unroll
        for (int u = 0; u < 4; ++u) *(LAS v4f*)(L + L_SCR + c * 4096 + u * 1024 + lane * 16) = v4f{a[4 * u], a[4 * u + 1], a[4 * u + 2], a[4 * u + 3]};
        lds_set(ctl + C_Y + c, 1u);
        lds_wait_all<4, 0>(ctl + C_Y, j);
        v4f y = {0.f, 0.f, 0.f, 0.f};
#pragma unroll
        for (int w = 0; w < 4; ++w) y += *(const LAS v4f*)(L + L_SCR + w * 4096 + c * 1024 + lane * 16);
        lds_set(ctl + C_Y + 4 + c, 1u);
        const int off = W_YP + (((c >> 1) * 80 + lane) * NMLP + h) * 64 + (c & 1) * 32;
        __builtin_amdgcn_raw_buffer_store_b128(v4u{asu(y.x), tag, asu(y.y), tag}, rw, off, 0, 16);
        __builtin_amdgcn_raw_buffer_store_b128(v4u{asu(y.z), tag, asu(y.w), tag}, rw, off + 16, 0, 16);
      } else {
#pragma unroll
        for (int i = 0; i < 16; ++i) a[i] = sum_rows(a[i]);
        if (lane < 16) {
#pragma unroll
          for (int u = 0; u < 8; ++u)
            __builtin_amdgcn_raw_buffer_store_b128(v4u{asu(a[2 * u]), tag, asu(a[2 * u + 1]), tag}, rw,
                                                   W_YP + (((u >> 2) * 80 + 64 + lane) * NMLP + h) * 64 + (u & 3) * 16, 0, 16);
        }
      }
    }
  }
  if (c == 0) STAMP(15);
  if (c == 5) {
    gather_q(p, L, h, ORDER, tag, lane);
    lds_set(ctl + C_Q0, 1u);
  }
  if (has_mlp) lds_wait_all<8, 1>(ctl + C_Y, j);

  int s = npro + c, pos = s % NSLOT;
  const int koff = r * 384 + j * 16;
#pragma nounroll
  for (int ph = 0; ph < 2; ++ph) {
    lds_wait<0>(ctl + C_Q0 + ph, 1);
    if (c == 0 && ph == 0) STAMP(8);
    v4u q[3];
#pragma unroll
    for (int u = 0; u < 3; ++u) q[u] = *(const LAS v4u*)(L + L_QB + ((ph ^ ORDER) * DH + (j + 8 * u) * 8) * 2);
    float mx = -3.0e38f, ls = 0.f, acc[24];
#pragma unroll
    for (int i = 0; i < 24; ++i) acc[i] = 0.f;
    const int send = ph ? ntot : ntot - 128;
#pragma nounroll
    for (; s < send; s += NC) {
      lds_wait<1>(ctl + C_LANDED, s + 1);
      const LAS unsigned char* sp = L + pos * SLOTB + koff;
      v4u kk[3], vv[3];
#pragma unroll
      for (int u = 0; u < 3; ++u) kk[u] = *(const LAS v4u*)(sp + u * 128);
#pragma unroll
      for (int u = 0; u < 3; ++u) vv[u] = *(const LAS v4u*)(sp + 3072 + u * 128);
      lds_set(ctl + C_NEXT + c, s + NC);
      float d0 = 0.f, d1 = 0.f, d2 = 0.f;
#pragma unroll
      for (int e = 0; e < 4; ++e) { d0 = dot2(kk[0][e], q[0][e], d0); d1 = dot2(kk[1][e], q[1][e], d1); d2 = dot2(kk[2][e], q[2][e], d2); }
      float d = d0 + d1 + d2;
      d += dpp<XOR1>(d); d += dpp<XOR2>(d); d += dpp<MIR8>(d);
      const float t = bfr(bfr(d) * 0.07216878364870322f) * LOG2E;
      const float mn = fmaxf(mx, t);
      const float al = exp2f_(mx - mn), pr = exp2f_(t - mn);
      mx = mn;
      ls = fmaf(ls, al, pr);
#pragma unroll
      for (int u = 0; u < 3; ++u)
#pragma unroll
        for (int e = 0; e < 4; ++e) {
          const int k = 8 * u + 2 * e;
          acc[k] = fmaf(pr, lo16(vv[u][e]), acc[k] * al);
          acc[k + 1] = fmaf(pr, hi16(vv[u][e]), acc[k + 1] * al);
        }
      pos += NC;
      if (pos >= NSLOT) pos -= NSLOT;
    }
    if (ph == 1 && c == 0) STAMP(9);
    const float M = max_rows(fmaxf(mx, dpp<ROR8>(mx)));
    const float sc = exp2f_(mx - M);
    ls *= sc;
    ls += dpp<ROR8>(ls);
    ls = sum_rows(ls);
#pragma unroll
    for (int i = 0; i < 24; ++i) acc[i] *= sc;
#pragma unroll
    for (int i = 0; i < 12; ++i) acc[i] = fold32(acc[i], acc[i + 12]);
#pragma unroll
    for (int i = 0; i < 6; ++i) { acc[i] = fold16(acc[i], acc[i + 6]); acc[i] += dpp<ROR8>(acc[i]); }
    LAS float* rec = (LAS float*)(L + L_SCR) + (ph * NC + c) * 200;
    if ((r & 1) == 0) {
#pragma unroll
      for (int u = 0; u < 3; ++u) {
        const int v = 6 * (lane >> 4) + 2 * u;
        *(LAS v2f*)(rec + (j + 8 * (v >> 3)) * 8 + (v & 7)) = v2f{acc[2 * u], acc[2 * u + 1]};
      }
    }
    if (lane == 0) *(LAS v2f*)(rec + 192) = v2f{M, ls};
    lds_set(ctl + C_R0 + 8 * ph + c, 1u);
  }
  if (c == 5) return;
  if (c < 3) {
    lds_wait_all<NC, 0>(ctl + C_R1, j);
    if (c == 0) STAMP(10);
    combine<1, 0>(p, L, 1, h, m, c * 64 + lane, c == 0, tag, lane);
    lds_set(ctl + C_O + c, 1u);
    if (c == 0) STAMP(11);
  }
  lds_wait<0>(ctl + C_LANDED, ntot + NWO);
  v4u bf[6];
  load_bt<6, 32 * 160, 4 * 160>(L + wo_pos * SLOTB + ((lane >> 4) * 8 + ((lane & 15) >> 2)) * 160 + c * 32 + (lane & 3) * 8, bf);
  lds_wait_all<4, 0>(ctl + C_O, j);
  const float sv = mm2<6>(L + L_OA + (lane & 1) * (DH * 2) + (lane >> 4) * 16, bf, lane);
  const int col = m * 80 + c * 16 + (lane & 15);
  if (lane < 32) st_gran(p.ws, W_SP + (((lane >> 4) * 80 + (col >> 3)) * NH + h) * 64 + (col & 7) * 8, sv, tag);
  lds_set(ctl + C_T3 + c, 1u);
  if (c == 0) STAMP(12);
}

}

namespace {
__global__ void k_report(Params p) {
  const GAS u64* st = (const GAS u64*)(p.ws + W_ST);
  u64 t0 = ~0ull, mx = 0, mn = ~0ull, sum = 0; int n = 0;
  for (int i = 0; i < 256; ++i) t0 = st[i * 32] < t0 ? st[i * 32] : t0;
  for (int i = 0; i < 256; ++i) { const u64 d = st[i * 32 + IDX] - t0; if (d < 100000 && !(SKIPMLP && (i >> 5) == 7)) { mx = d > mx ? d : mx; mn = d < mn ? d : mn; sum += d; ++n; } }
  const u64 v = MODE == 0 ? mx : MODE == 1 ? sum / (n ? n : 1) : mn;
  const u64 t1 = __builtin_amdgcn_s_memrealtime();
  while (__builtin_amdgcn_s_memrealtime() - t1 < 10 * v) __builtin_amdgcn_s_sleep(8);
}
}
extern "C" void kernel_launch(void* const* d_in, const int* in_sizes, int n_in, void* d_out, int out_size, void* d_ws,
                              size_t ws_size, hipStream_t stream) {
  Params p;
  p.x = (const unsigned short*)d_in[0];
  p.k = (const unsigned short*)d_in[1];
  p.v = (const unsigned short*)d_in[2];
  p.wq = (const unsigned short*)d_in[3];
  p.wo = (const unsigned short*)d_in[4];
  p.wr = (const unsigned short*)d_in[6];
  p.wg = (const unsigned short*)d_in[7];
  p.wu = (const unsigned short*)d_in[8];
  p.wd = (const unsigned short*)d_in[9];
  p.out = (unsigned short*)d_out;
  p.ws = (unsigned char*)d_ws;
  (void)hipFuncSetAttribute((const void*)fused_decode, hipFuncAttributeMaxDynamicSharedMemorySize, L_END);
  fused_decode<<<NWG, NTHR, L_END, stream>>>(p);
  k_report<<<1, 64, 0, stream>>>(p);
}
```

```cpp
#include <hip/hip_runtime.h>
#include <hip/hip_bf16.h>

namespace {

#define GAS __attribute__((address_space(1)))
#define LAS __attribute__((address_space(3)))
#define RLX_AGENT __ATOMIC_RELAXED, __HIP_MEMORY_SCOPE_AGENT
#define RLX_WG __ATOMIC_RELAXED, __HIP_MEMORY_SCOPE_WORKGROUP
typedef unsigned v4u __attribute__((ext_vector_type(4)));
typedef unsigned v2u __attribute__((ext_vector_type(2)));
typedef float v4f __attribute__((ext_vector_type(4)));
typedef float v2f __attribute__((ext_vector_type(2)));
typedef short v4s __attribute__((ext_vector_type(4)));
typedef __bf16 bf16x2 __attribute__((ext_vector_type(2)));
typedef __bf16 bf16x8 __attribute__((ext_vector_type(8)));
typedef unsigned long long u64;

#define K_INF 21
#define K_NSLOT 22
#define K_ORDER 0
#define K_PK0 0
#define K_PK1 0
#define K_PV0 0
#define K_PV1 0
#define K_PW 0

constexpr int DM = 640, NH = 32, DH = 192, SK = 8192, FF = 1536;
constexpr int NWG = 256, NC = 6, NTHR = (NC + 2) * 64;
constexpr int INF = K_INF;
constexpr int NSLOT = K_NSLOT;
constexpr int ORDER = K_ORDER;
constexpr int SLOTB = 6144, NWO = 5, NS_WQ = 7, NS_GU = 27, NS_DN = 16;
constexpr unsigned KV_BYTES = 2u * NH * SK * DH * 2u;
constexpr int SLAB = SK * DH * 2;
constexpr int NMLP = FF / 64;
constexpr int NFIN = 2 * DM / 8;
static_assert(INF >= 7 && INF <= 60, "vmcnt is 6 bits");

constexpr int L_XP = NSLOT * SLOTB;
constexpr int L_SCR = L_XP + 2560;
constexpr int L_QB = L_SCR + 16384;
constexpr int L_OA = L_QB + 768;
constexpr int L_HM = L_OA + 768;
constexpr int L_CTL = L_HM + 256;
constexpr int L_END = L_CTL + 320;
static_assert(L_END <= 160 * 1024 && L_END > 80 * 1024, "one workgroup per CU");
enum { C_NEXT = 0, C_P1 = 8, C_HM = 16, C_Y = 24, C_R0 = 32, C_R1 = 40, C_O = 48, C_T3 = 56, C_LANDED = 64, C_XOK, C_TAG, C_Q0, C_Q1 };

constexpr int W_EPOCH = 0;
constexpr int W_ML = 1024;
constexpr int W_OP = W_ML + 2 * NH * 8 * 16;
constexpr int W_SP = W_OP + 2 * NH * 8 * DH * 8;
constexpr int W_YP = W_SP + NFIN * NH * 64;
constexpr int W_QP = W_YP + NFIN * NMLP * 64;
constexpr int W_END = W_QP + NH * 2 * 384 * 8;

constexpr unsigned LDS_SPIN = 1u << 20, GLB_SPIN = 1u << 16;
constexpr float LOG2E = 1.4426950408889634f;

struct Params {
  const unsigned short *x, *k, *v, *wq, *wo, *wr, *wg, *wu, *wd;
  unsigned short* out;
  unsigned char* ws;
};

__device__ __forceinline__ int rfl(int v) { return __builtin_amdgcn_readfirstlane(v); }
__device__ __forceinline__ float bfr(float x) { return (float)(__bf16)x; }
__device__ __forceinline__ unsigned bfbits(float x) { return (unsigned)__builtin_bit_cast(unsigned short, (__bf16)x); }
__device__ __forceinline__ float lo16(unsigned u) { return __builtin_bit_cast(float, u << 16); }
__device__ __forceinline__ float hi16(unsigned u) { return __builtin_bit_cast(float, u & 0xffff0000u); }
__device__ __forceinline__ float asf(unsigned u) { return __builtin_bit_cast(float, u); }
__device__ __forceinline__ unsigned asu(float f) { return __builtin_bit_cast(unsigned, f); }
__device__ __forceinline__ float dot2(unsigned a, unsigned b, float c) {
  return __builtin_amdgcn_fdot2_f32_bf16(__builtin_bit_cast(bf16x2, a), __builtin_bit_cast(bf16x2, b), c, false);
}
template <int CTRL> __device__ __forceinline__ float dpp(float x) {
  return __builtin_bit_cast(float, __builtin_amdgcn_mov_dpp(__builtin_bit_cast(int, x), CTRL, 0xf, 0xf, true));
}
template <int CTRL> __device__ __forceinline__ int dppi(int x) { return __builtin_amdgcn_mov_dpp(x, CTRL, 0xf, 0xf, true); }
constexpr int XOR1 = 0xB1, XOR2 = 0x4E, MIR8 = 0x141, ROR4 = 0x124, ROR8 = 0x128;
__device__ __forceinline__ float fold32(float a, float b) {
  auto s = __builtin_amdgcn_permlane32_swap(asu(a), asu(b), false, false);
  return asf(s[0]) + asf(s[1]);
}
__device__ __forceinline__ float fold16(float a, float b) {
  auto s = __builtin_amdgcn_permlane16_swap(asu(a), asu(b), false, false);
  return asf(s[0]) + asf(s[1]);
}
__device__ __forceinline__ float max_rows(float x) {
  auto s = __builtin_amdgcn_permlane16_swap(asu(x), asu(x), false, false);
  x = fmaxf(asf(s[0]), asf(s[1]));
  auto t = __builtin_amdgcn_permlane32_swap(asu(x), asu(x), false, false);
  return fmaxf(asf(t[0]), asf(t[1]));
}
__device__ __forceinline__ float sum_rows(float x) { x = fold16(x, x); return fold32(x, x); }
__device__ __forceinline__ float wave_sum(float x) {
  x += dpp<XOR1>(x); x += dpp<XOR2>(x); x += dpp<ROR4>(x); x += dpp<ROR8>(x);
  return sum_rows(x);
}
__device__ __forceinline__ float exp2f_(float x) { return __builtin_amdgcn_exp2f(x); }
__device__ __forceinline__ float siluf(float x) { return x / (1.0f + expf(-x)); }
#define CBAR() asm volatile("" ::: "memory")

__device__ __forceinline__ __amdgpu_buffer_rsrc_t rsrc(const void* p, unsigned bytes) {
  return __builtin_amdgcn_make_buffer_rsrc((void*)p, 0, bytes, 0x00020000);
}
__device__ __forceinline__ void lds_set(LAS unsigned* w, unsigned v) {
  CBAR();
  __hip_atomic_store(w, v, RLX_WG);
  CBAR();
}
template <int SLEEP> __device__ __forceinline__ void lds_wait(LAS unsigned* w, unsigned want) {
#pragma nounroll
  for (unsigned n = 0; n < LDS_SPIN; ++n) {
    if ((unsigned)rfl((int)__hip_atomic_load(w, RLX_WG)) >= want) break;
    if (SLEEP) __builtin_amdgcn_s_sleep(SLEEP);
  }
  CBAR();
}
template <int N, int SLEEP> __device__ __forceinline__ void lds_wait_all(LAS unsigned* w, int j) {
  LAS unsigned* a = w + (j < N ? j : 0);
#pragma nounroll
  for (unsigned n = 0; n < LDS_SPIN; ++n) {
    if (__all(__hip_atomic_load(a, RLX_WG) != 0u)) break;
    if (SLEEP) __builtin_amdgcn_s_sleep(SLEEP);
  }
  CBAR();
}
__device__ __forceinline__ void st_gran(unsigned char* ws, int off, float v, unsigned tag) {
  __hip_atomic_store((GAS u64*)(ws + off), ((u64)tag << 32) | asu(v), RLX_AGENT);
}
__device__ __forceinline__ u64 ld_gran(const unsigned char* ws, int off) { return __hip_atomic_load((GAS u64*)(ws + off), RLX_AGENT); }

#define STAMP(i) do { } while (0)

__device__ __forceinline__ void layer_norm(v2u (&xr)[5], LAS unsigned char* L, int lane) {
  asm volatile("" : "+v"(xr[0]), "+v"(xr[1]), "+v"(xr[2]), "+v"(xr[3]), "+v"(xr[4]));
  v2f x[10];
#pragma unroll
  for (int i = 0; i < 5; ++i) { x[2 * i] = v2f{lo16(xr[i].x), hi16(xr[i].x)}; x[2 * i + 1] = v2f{lo16(xr[i].y), hi16(xr[i].y)}; }
  const float p0 = asf((unsigned)__builtin_amdgcn_readlane((int)asu(x[0].x), 0)), p1 = asf((unsigned)__builtin_amdgcn_readlane((int)asu(x[0].x), 32));
  const float piv = lane < 32 ? p0 : p1;
  v2f s1 = {0.f, 0.f}, s2 = {0.f, 0.f};
#pragma unroll
  for (int i = 0; i < 10; ++i) { const v2f d = x[i] - piv; s1 += d; s2 += d * d; }
  float a = s1.x + s1.y, q = s2.x + s2.y;
  a += dpp<XOR1>(a); q += dpp<XOR1>(q); a += dpp<XOR2>(a); q += dpp<XOR2>(q);
  a += dpp<ROR4>(a); q += dpp<ROR4>(q); a += dpp<ROR8>(a); q += dpp<ROR8>(q);
  a = fold16(a, a) * (1.0f / DM); q = fold16(q, q) * (1.0f / DM);
  const float mb = bfr(piv + a), var = bfr(fmaf(-a, a, q));
  const float den = bfr(__builtin_amdgcn_sqrtf(bfr(var + 1e-5f)));
  const float rcp = __builtin_amdgcn_rcpf(den);
#pragma unroll
  for (int i = 0; i < 5; ++i) {
    unsigned o[2];
#pragma unroll
    for (int e = 0; e < 2; ++e) {
      const v2f t = x[2 * i + e] - mb;
      const v2f d = {bfr(t.x), bfr(t.y)}, q0 = d * rcp;
      const v2f qq = (d - q0 * den) * rcp + q0;
      o[e] = bfbits(qq.x) | (bfbits(qq.y) << 16);
    }
    *(LAS v2u*)(L + L_XP + (lane >> 5) * 1280 + ((lane & 31) + 32 * i) * 8) = v2u{o[0], o[1]};
  }
}

__device__ __forceinline__ void gather_q(const Params& p, LAS unsigned char* L, int h, int b, unsigned tag, int lane) {
  const __amdgpu_buffer_rsrc_t rw = rsrc(p.ws, W_END);
  const int l = min(lane, 47), off = W_QP + (h * 768 + b * DH + 4 * l) * 8;
  v4u g[4];
#pragma nounroll
  for (unsigned n = 0; n < GLB_SPIN; ++n) {
    CBAR();
#pragma unroll
    for (int i = 0; i < 4; ++i) g[i] = __builtin_amdgcn_raw_buffer_load_b128(rw, off + (i & 1) * 16 + (i >> 1) * 3072, 0, 16);
    bool ok = true;
#pragma unroll
    for (int i = 0; i < 4; ++i) ok &= g[i].y == tag && g[i].w == tag;
    if (__all(ok)) break;
  }
  *(LAS v2u*)(L + L_QB + (b * DH + 4 * l) * 2) = v2u{bfbits(asf(g[0].x) + asf(g[2].x)) | (bfbits(asf(g[0].z) + asf(g[2].z)) << 16),
                                                      bfbits(asf(g[1].x) + asf(g[3].x)) | (bfbits(asf(g[1].z) + asf(g[3].z)) << 16)};
}

template <int NK, int KSTEP, int HALF>
__device__ __forceinline__ void load_bt(const LAS unsigned char* b, v4u (&f)[NK]) {
#pragma unroll
  for (int ks = 0; ks < NK; ++ks) {
    const v2u lo = __builtin_bit_cast(v2u, __builtin_amdgcn_ds_read_tr16_b64_v4i16((LAS v4s*)(b + ks * KSTEP)));
    const v2u hi = __builtin_bit_cast(v2u, __builtin_amdgcn_ds_read_tr16_b64_v4i16((LAS v4s*)(b + ks * KSTEP + HALF)));
    f[ks] = v4u{lo.x, lo.y, hi.x, hi.y};
  }
}
template <int NK> __device__ __forceinline__ float mm2(const LAS unsigned char* a, const v4u (&f)[NK], int lane) {
  v4u av[NK];
#pragma unroll
  for (int ks = 0; ks < NK; ++ks) av[ks] = *(const LAS v4u*)(a + ks * 64);
  v4f c = {0.f, 0.f, 0.f, 0.f};
#pragma unroll
  for (int ks = 0; ks < NK; ++ks) c = __builtin_amdgcn_mfma_f32_16x16x32_bf16(__builtin_bit_cast(bf16x8, av[ks]), __builtin_bit_cast(bf16x8, f[ks]), c, 0, 0, 0);
  return lane & 16 ? c[1] : c[0];
}

template <int ND, int SLEEP>
__device__ __forceinline__ void combine(const Params& p, LAS unsigned char* L, int ph, int h, int m, int d0, bool lead, unsigned tag, int lane) {
  const int b = ph ^ ORDER;
  const LAS float* rec = (const LAS float*)(L + L_SCR) + ph * NC * 200;
  float M = rec[192];
#pragma unroll
  for (int c = 1; c < NC; ++c) M = fmaxf(M, rec[c * 200 + 192]);
  float Ls = 0.f, o[ND];
#pragma unroll
  for (int i = 0; i < ND; ++i) o[i] = 0.f;
#pragma unroll
  for (int c = 0; c < NC; ++c) {
    const float w = exp2f_(rec[c * 200 + 192] - M);
    Ls = fmaf(w, rec[c * 200 + 193], Ls);
#pragma unroll
    for (int i = 0; i < ND; ++i) o[i] = fmaf(w, rec[c * 200 + d0 + 64 * i], o[i]);
  }
  const int bh = b * NH + h;
  const __amdgpu_buffer_rsrc_t rw = rsrc(p.ws, W_END);
#pragma unroll
  for (int i = 0; i < ND; ++i) st_gran(p.ws, W_OP + ((bh * 8 + m) * DH + d0 + 64 * i) * 8, o[i], tag);
  if (lead && lane == 0) __builtin_amdgcn_raw_buffer_store_b128(v4u{asu(M), tag, asu(Ls), tag}, rw, W_ML + (bh * 8 + m) * 16, 0, 16);
  u64 g[ND][8];
  v4u ml;
#pragma nounroll
  for (unsigned n = 0; n < GLB_SPIN; ++n) {
    CBAR();
    ml = __builtin_amdgcn_raw_buffer_load_b128(rw, W_ML + (bh * 8 + (lane & 7)) * 16, 0, 16);
#pragma unroll
    for (int i = 0; i < ND; ++i)
#pragma unroll
      for (int j = 0; j < 8; ++j) g[i][j] = ld_gran(p.ws, W_OP + ((bh * 8 + j) * DH + d0 + 64 * i) * 8);
    bool ok = ml.y == tag && ml.w == tag;
#pragma unroll
    for (int i = 0; i < ND; ++i)
#pragma unroll
      for (int j = 0; j < 8; ++j) ok &= (unsigned)(g[i][j] >> 32) == tag;
    if (__all(ok)) break;
    if (SLEEP) __builtin_amdgcn_s_sleep(SLEEP);
  }
  float Mg = asf(ml.x);
  Mg = fmaxf(Mg, dpp<XOR1>(Mg)); Mg = fmaxf(Mg, dpp<XOR2>(Mg)); Mg = fmaxf(Mg, dpp<MIR8>(Mg));
  const float w = exp2f_(asf(ml.x) - Mg);
  float Lg = w * asf(ml.z);
  Lg += dpp<XOR1>(Lg); Lg += dpp<XOR2>(Lg); Lg += dpp<MIR8>(Lg);
  const float wn = w / Lg;
#pragma unroll
  for (int i = 0; i < ND; ++i) {
    float a = 0.f;
#pragma unroll
    for (int j = 0; j < 8; ++j) a = fmaf(asf((unsigned)__builtin_amdgcn_readlane((int)asu(wn), j)), asf((unsigned)g[i][j]), a);
    *(LAS unsigned short*)(L + L_OA + (b * DH + d0 + 64 * i) * 2) = (unsigned short)bfbits(a);
  }
}

template <int NP, int SLEEP>
__device__ __forceinline__ v2f gather8(const Params& p, int base, unsigned tag, int lane) {
  static_assert(NP > 16 && NP <= 32, "two 16-byte pieces per lane");
  const __amdgpu_buffer_rsrc_t rw = rsrc(p.ws, W_END);
  const bool two = 64 + lane < NP * 4;
  v4u a, c;
#pragma nounroll
  for (unsigned n = 0; n < GLB_SPIN; ++n) {
    CBAR();
    a = __builtin_amdgcn_raw_buffer_load_b128(rw, base + lane * 16, 0, 16);
    c = __builtin_amdgcn_raw_buffer_load_b128(rw, base + (two ? 64 + lane : lane) * 16, 0, 16);
    if (__all(a.y == tag && a.w == tag && c.y == tag && c.w == tag)) break;
    if (SLEEP) __builtin_amdgcn_s_sleep(SLEEP);
  }
  float s0 = asf(a.x) + (two ? asf(c.x) : 0.f), s1 = asf(a.z) + (two ? asf(c.z) : 0.f);
  s0 += dpp<ROR4>(s0); s1 += dpp<ROR4>(s1);
  s0 += dpp<ROR8>(s0); s1 += dpp<ROR8>(s1);
  return v2f{sum_rows(s0), sum_rows(s1)};
}

#define DMA3(POL)                                                                    \
  asm volatile("s_mov_b32 m0, %3\n\t"                                                \
               "s_waitcnt vmcnt(%4)\n\t"                                             \
               "buffer_load_dwordx4 %0, %1, %2 offen " POL "lds\n\t"                 \
               "s_waitcnt vmcnt(%4)\n\t"                                             \
               "buffer_load_dwordx4 %0, %1, %2 offen offset:1024 " POL "lds\n\t"     \
               "s_waitcnt vmcnt(%4)\n\t"                                             \
               "buffer_load_dwordx4 %0, %1, %2 offen offset:2048 " POL "lds"         \
               :: "v"(voff), "s"(srd), "s"(soff), "s"(lds), "i"(N) : "memory")
#define DMA1(POL)                                                                    \
  asm volatile("s_mov_b32 m0, %3\n\t"                                                \
               "s_waitcnt vmcnt(%4)\n\t"                                             \
               "buffer_load_dwordx4 %0, %1, %2 offen " POL "lds"                     \
               :: "v"(voff), "s"(srd), "s"(soff), "s"(lds), "i"(N) : "memory")
template <int N, int POL> __device__ __forceinline__ void dma3(int voff, v4u srd, int soff, unsigned lds) {
  if constexpr (POL == 0) DMA3("nt "); else DMA3("");
}
template <int N, int POL> __device__ __forceinline__ void dma1(int voff, v4u srd, int soff, unsigned lds) {
  if constexpr (POL == 0) DMA1("nt "); else DMA1("");
}
__device__ __forceinline__ v4u srd_of(const void* ptr, unsigned bytes) {
  const u64 a = (u64)ptr;
  return v4u{(unsigned)a, (unsigned)(a >> 32) & 0xffffu, bytes, 0x00020000u};
}
constexpr int fdiv6(int a) { return a >= 0 ? a / 6 : -((5 - a) / 6); }
constexpr int LAG3 = fdiv6(3 - INF), LAG6 = fdiv6(6 - INF);

struct Loader {
  LAS unsigned* ctl;
  unsigned lbase;
  int s, pos, limit, j;
  __device__ __forceinline__ void credit(int last) {
    if (__builtin_expect(last >= limit, 0)) {
#pragma nounroll
      for (unsigned n = 0; n < LDS_SPIN; ++n) {
        int v = (int)__hip_atomic_load(ctl + C_NEXT + (j < NC ? j : 0), RLX_WG);
        v = min(v, dppi<XOR1>(v)); v = min(v, dppi<XOR2>(v)); v = min(v, dppi<MIR8>(v));
        limit = rfl(v) + NSLOT;
        if (last < limit) break;
        __builtin_amdgcn_s_sleep(1);
      }
      CBAR();
    }
  }
  __device__ __forceinline__ void publish(int n) { __hip_atomic_store(ctl + C_LANDED, (unsigned)max(n, 0), RLX_WG); }
  __device__ __forceinline__ unsigned dst() const { return lbase + pos * SLOTB; }
  __device__ __forceinline__ void advance() { ++s; pos = pos + 1 == NSLOT ? 0 : pos + 1; }
  template <int PK, int PV> __device__ __forceinline__ void kv(int i0, int i1, int n1, int st, int m, int slab, int voff, v4u rk, v4u rv) {
#pragma nounroll
    for (int i = i0; i < i1; ++i) {
      credit(s + 3);
      const int piece = i < n1 ? min(st * i + m, 63) : 64 + 8 * (i - n1) + m;
      const int so = slab + (piece & 255) * 12288;
#pragma unroll
      for (int u = 0; u < 4; ++u) {
        dma3<INF - 1, PK>(voff, rk, so + u * 3072, dst());
        if (LAG3 == LAG6) publish(s + LAG3);
        dma3<INF - 1, PV>(voff, rv, so + u * 3072, dst() + 3072);
        if (LAG3 != LAG6) publish(s + LAG6);
        advance();
      }
    }
  }
};

__device__ __forceinline__ void dot16(float (&a)[16], v4u w, unsigned x0l, unsigned x1l) {
  const unsigned x0h = x0l << 16, x1h = x1l << 16;
#pragma unroll
  for (int e = 0; e < 4; ++e) {
    a[2 * e] = dot2(w[e], x0l, a[2 * e]);         a[2 * e + 1] = dot2(w[e], x0h, a[2 * e + 1]);
    a[8 + 2 * e] = dot2(w[e], x1l, a[8 + 2 * e]); a[9 + 2 * e] = dot2(w[e], x1h, a[9 + 2 * e]);
  }
}

__global__ void __launch_bounds__(NTHR, 2) fused_decode(Params p) {
  extern __shared__ __attribute__((aligned(16))) unsigned char smem[];
  LAS unsigned char* L = (LAS unsigned char*)smem;
  LAS unsigned* ctl = (LAS unsigned*)(L + L_CTL);
  const int tid = threadIdx.x, lane = tid & 63, wave = rfl(tid >> 6);
  const int bid = blockIdx.x, h = bid & 31, m = bid >> 5;
  const int r = lane >> 3, j = lane & 7;
  const bool has_wq = m < 6, has_mlp = m == 7 && h < NMLP;
  const int npro = has_wq ? NS_WQ : has_mlp ? NS_GU + NS_DN : 0;
  const int st = h < NMLP ? 7 : 8, n1 = h < NMLP ? (m < 6 ? 9 : m == 6 ? 10 : 0) : 8;
  const int ntot = npro + 4 * (n1 + 56);
  const int wo_skip = ntot % NSLOT + NWO > NSLOT ? NSLOT - ntot % NSLOT : 0, wo_pos = (ntot + wo_skip) % NSLOT;

  if (wave == 0) {
    __builtin_amdgcn_s_setprio(3);
    STAMP(0);
    Loader ld{ctl, (unsigned)(size_t)L, 0, 0, NSLOT, j};
    const v4u rk = srd_of(p.k, KV_BYTES), rv = srd_of(p.v, KV_BYTES);
    const int voff = lane * 16;
    const int slab0 = (ORDER * NH + h) * SLAB, slab1 = ((ORDER ^ 1) * NH + h) * SLAB;
    int i0 = 0;
    if (has_wq) {
      const v4u rq = srd_of(p.wq, DM * NH * DH * 2);
      const int vo = r * (NH * DH * 2) + (j ^ (r & 2)) * 16, so = (m / 3) * 320 * (NH * DH * 2) + h * (DH * 2) + (m % 3) * 128;
#pragma unroll 6
      for (int g = 0; g < 6 * NS_WQ; ++g) dma1<63, K_PW>(g & 1 ? vo ^ 64 : vo, rq, so + min(g, 39) * (8 * NH * DH * 2), ld.lbase + g * 1024);
      ld.s = ld.pos = NS_WQ;
      __builtin_amdgcn_s_barrier();
    } else if (has_mlp) {
      __builtin_amdgcn_s_barrier();
      const v4u rg = srd_of(p.wg, DM * FF * 2), ru = srd_of(p.wu, DM * FF * 2), rd = srd_of(p.wd, FF * DM * 2);
      const int vo = r * (FF * 2) + j * 16, ve = (lane >> 4) * (DM * 2) + 1024 + (lane & 15) * 16;
#pragma nounroll
      for (int t = 0; t < NS_GU; ++t) {
        ld.credit(ld.s);
#pragma unroll
        for (int q = 0; q < 6; ++q) dma1<INF - 1, K_PW>(vo, q & 1 ? ru : rg, min(3 * t + (q >> 1), 79) * (8 * FF * 2) + h * 128, ld.dst() + q * 1024);
        ld.publish(ld.s + LAG6);
        ld.advance();
      }
#pragma nounroll
      for (int t = 0; t < NS_DN; ++t) {
        ld.credit(ld.s);
#pragma unroll
        for (int q = 0; q < 6; ++q) dma1<INF - 1, K_PW>(q < 4 ? voff : ve, rd, (64 * h + 4 * t + (q < 4 ? q : 0)) * (DM * 2), ld.dst() + q * 1024);
        ld.publish(ld.s + LAG6);
        ld.advance();
      }
    } else {
      const int so = slab0 + (n1 ? m : 64 + m) * 12288;
#pragma unroll
      for (int u = 0; u < 4; ++u) {
        dma3<63, ORDER ? K_PK1 : K_PK0>(voff, rk, so + u * 3072, ld.dst());
        dma3<63, ORDER ? K_PV1 : K_PV0>(voff, rv, so + u * 3072, ld.dst() + 3072);
        ld.advance();
      }
      i0 = 1;
      __builtin_amdgcn_s_barrier();
    }
    CBAR();
    ld.kv<ORDER ? K_PK1 : K_PK0, ORDER ? K_PV1 : K_PV0>(i0, n1 + 24, n1, st, m, slab0, voff, rk, rv);
    ld.kv<ORDER ? K_PK0 : K_PK1, ORDER ? K_PV0 : K_PV1>(n1 + 24, n1 + 56, n1, st, m, slab1, voff, rk, rv);
    STAMP(1);
    const v4u ro = srd_of(p.wo, NH * DH * DM * 2);
    int row = lane / 10, ch = lane - row * 10;
    ld.credit(ld.s + wo_skip + NWO - 1);
    ld.pos = wo_pos;
#pragma nounroll
    for (int t = 0; t < NWO; ++t) {
#pragma unroll
      for (int q = 0; q < 6; ++q) {
        dma1<INF - 1, K_PW>(row * (DM * 2) + ch * 16, ro, h * DH * (DM * 2) + m * 160, ld.dst() + q * 1024);
        if (q == 2 && LAG3 == LAG6) ld.publish(ld.s + LAG3);
        if (q == 5 && LAG3 != LAG6) ld.publish(ld.s + LAG6);
        row += 6; ch += 4;
        if (ch >= 10) { ch -= 10; row += 1; }
      }
      ld.advance();
    }
    if (INF > 30) { asm volatile("s_waitcnt vmcnt(30)" ::: "memory"); ld.publish(ld.s - NWO); }
    asm volatile("s_waitcnt vmcnt(0)" ::: "memory");
    ld.publish(ld.s);
    STAMP(2);
    return;
  }

  if (wave == NC + 1) {
    v2u xr[5];
#pragma unroll
    for (int i = 0; i < 5; ++i) xr[i] = ((const GAS v2u*)p.x)[(lane >> 5) * 160 + (lane & 31) + 32 * i];
    const unsigned e0 = __hip_atomic_load((GAS unsigned*)(p.ws + W_EPOCH), RLX_AGENT);
    ctl[lane] = lane < NC && (npro == 0 || (has_wq && lane >= 4)) ? npro + lane : 0;
    if (lane < 16) ctl[64 + lane] = 0u;
    asm volatile("s_waitcnt lgkmcnt(0)" ::: "memory");
    __builtin_amdgcn_s_barrier();
    CBAR();
    layer_norm(xr, L, lane);
    const unsigned tag = (unsigned)rfl((int)e0) + 1u;
    ctl[C_TAG] = tag;
    lds_set(ctl + C_XOK, 1u);
    STAMP(3);
    lds_wait<8>(ctl + C_Q0, 1);
    gather_q(p, L, h, ORDER ^ 1, tag, lane);
    lds_set(ctl + C_Q1, 1u);
    lds_wait_all<NC, 16>(ctl + C_R0, j);
    combine<3, 8>(p, L, 0, h, m, lane, true, tag, lane);
    lds_set(ctl + C_O + 3, 1u);
    STAMP(4);
    if (bid >= NFIN) return;
    const int fb = bid / 80, col = (bid - fb * 80) * 8 + 2 * (lane & 3);
    float rs = 0.f;
#pragma unroll
    for (int i = 0; i < 5; ++i) {
      const unsigned w = ((const GAS unsigned*)p.wr)[lane + 64 * i];
      rs = dot2(w, *(const LAS unsigned*)(L + L_XP + fb * 1280 + (lane + 64 * i) * 4), rs);
    }
    const float sg = bfr(1.0f / (1.0f + expf(-bfr(wave_sum(rs)))));
    const unsigned xo = ((const GAS unsigned*)p.x)[(fb * DM + col) >> 1];
    const v2f y = gather8<NMLP, 8>(p, W_YP + bid * NMLP * 64, tag, lane);
    const unsigned xx = *(const LAS unsigned*)(L + L_XP + fb * 1280 + col * 2);
    const float g0 = bfr(siluf(bfr(lo16(xx) + bfr(bfr(y.x) * sg))));
    const float g1 = bfr(siluf(bfr(hi16(xx) + bfr(bfr(y.y) * sg))));
    STAMP(5);
    lds_wait_all<5, 2>(ctl + C_T3, j);
    const v2f s = gather8<NH, 0>(p, W_SP + bid * NH * 64, tag, lane);
    const unsigned o = bfbits(lo16(xo) + bfr(bfr(s.x) * g0)) | (bfbits(hi16(xo) + bfr(bfr(s.y) * g1)) << 16);
    if (lane < 4) ((GAS unsigned*)p.out)[(fb * DM + col) >> 1] = o;
    if (bid == 0 && lane == 0) __hip_atomic_store((GAS unsigned*)(p.ws + W_EPOCH), tag, RLX_AGENT);
    STAMP(6);
    return;
  }

  const int c = wave - 1;
  __builtin_amdgcn_s_barrier();
  CBAR();
  lds_wait<1>(ctl + C_XOK, 1);
  const unsigned tag = (unsigned)rfl((int)ctl[C_TAG]);
  const __amdgpu_buffer_rsrc_t rw = rsrc(p.ws, W_END);
  if (c == 0) STAMP(13);
  if (has_wq) {
    if (c < 4) {
      const int sw = ((lane >> 3) & 1) | ((lane >> 3) & 2);
      lds_wait<0>(ctl + C_LANDED, NS_WQ);
      v4u bf[10];
      load_bt<10, 4096, 512>(L + (lane >> 4) * 1024 + ((lane & 15) >> 2) * 128 + ((2 * c + ((lane & 3) >> 1)) ^ (2 * sw)) * 16 + (lane & 1) * 8, bf);
      lds_set(ctl + C_NEXT + c, NS_WQ + c);
      const float qv = mm2<10>(L + L_XP + (lane & 1) * 1280 + (m / 3) * 640 + (lane >> 4) * 16, bf, lane);
      if (lane < 32) st_gran(p.ws, W_QP + ((h * 2 + m / 3) * 384 + (lane >> 4) * DH + (m % 3) * 64 + 16 * c + (lane & 15)) * 8, qv, tag);
    }
  } else if (has_mlp) {
    const LAS unsigned short* xp = (const LAS unsigned short*)(L + L_XP);
    int pos = 0;
    float a[16];
#pragma unroll
    for (int i = 0; i < 16; ++i) a[i] = 0.f;
#pragma nounroll
    for (int t = 0; t < NS_GU; ++t) {
      const int g = 3 * t + (c >> 1);
      lds_wait<1>(ctl + C_LANDED, t + 1);
      const v4u w = *(const LAS v4u*)(L + pos * SLOTB + c * 1024 + lane * 16);
      const unsigned x0 = xp[min(g, 79) * 8 + r], x1 = xp[DM + min(g, 79) * 8 + r];
      lds_set(ctl + C_NEXT + c, t + 1 == NS_GU && c == 5 ? NS_GU + NS_DN + c : t + 1);
      if (g < 80) dot16(a, w, x0, x1);
      pos = pos + 1 == NSLOT ? 0 : pos + 1;
    }
#pragma unroll
    for (int i = 0; i < 8; ++i) a[i] = fold32(a[i], a[i + 8]);
#pragma unroll
    for (int i = 0; i < 4; ++i) { a[i] = fold16(a[i], a[i + 4]); a[i] += dpp<ROR8>(a[i]); }
    if ((r & 1) == 0) *(LAS v4f*)(L + L_SCR + (c * 128 + (lane >> 5) * 64 + 8 * j + 4 * ((lane >> 4) & 1)) * 4) = v4f{a[0], a[1], a[2], a[3]};
    lds_set(ctl + C_P1 + c, 1u);
    if (c < 2) {
      lds_wait_all<NC, 0>(ctl + C_P1, j);
      const LAS float* pp = (const LAS float*)(L + L_SCR) + c * 64 + lane;
      const float s0 = pp[0] + pp[256] + pp[512], s1 = pp[128] + pp[384] + pp[640];
      *(LAS unsigned short*)(L + L_HM + lane * 4 + c * 2) = (unsigned short)bfbits(bfr(siluf(bfr(s0))) * bfr(s1));
      lds_set(ctl + C_HM + c, 1u);
    }
    if (c < 5) {
      lds_wait_all<2, 1>(ctl + C_HM, j);
#pragma unroll
      for (int i = 0; i < 16; ++i) a[i] = 0.f;
#pragma nounroll
      for (int t = 0; t < NS_DN; ++t) {
        lds_wait<1>(ctl + C_LANDED, NS_GU + t + 1);
        const v4u w = *(const LAS v4u*)(L + pos * SLOTB + c * 1024 + lane * 16);
        const unsigned xx = ((const LAS unsigned*)(L + L_HM))[4 * t + (c < 4 ? c : lane >> 4)];
        lds_set(ctl + C_NEXT + c, t + 1 == NS_DN ? NS_GU + NS_DN + c : NS_GU + t + 1);
        dot16(a, w, xx & 0xffffu, xx >> 16);
        pos = pos + 1 == NSLOT ? 0 : pos + 1;
      }
      if (c < 4) {
#pragma unroll
        for (int u = 0; u < 4; ++u) *(LAS v4f*)(L + L_SCR + c * 4096 + u * 1024 + lane * 16) = v4f{a[4 * u], a[4 * u + 1], a[4 * u + 2], a[4 * u + 3]};
        lds_set(ctl + C_Y + c, 1u);
        lds_wait_all<4, 0>(ctl + C_Y, j);
        v4f y = {0.f, 0.f, 0.f, 0.f};
#pragma unroll
        for (int w = 0; w < 4; ++w) y += *(const LAS v4f*)(L + L_SCR + w * 4096 + c * 1024 + lane * 16);
        lds_set(ctl + C_Y + 4 + c, 1u);
        const int off = W_YP + (((c >> 1) * 80 + lane) * NMLP + h) * 64 + (c & 1) * 32;
        __builtin_amdgcn_raw_buffer_store_b128(v4u{asu(y.x), tag, asu(y.y), tag}, rw, off, 0, 16);
        __builtin_amdgcn_raw_buffer_store_b128(v4u{asu(y.z), tag, asu(y.w), tag}, rw, off + 16, 0, 16);
      } else {
#pragma unroll
        for (int i = 0; i < 16; ++i) a[i] = sum_rows(a[i]);
        if (lane < 16) {
#pragma unroll
          for (int u = 0; u < 8; ++u)
            __builtin_amdgcn_raw_buffer_store_b128(v4u{asu(a[2 * u]), tag, asu(a[2 * u + 1]), tag}, rw,
                                                   W_YP + (((u >> 2) * 80 + 64 + lane) * NMLP + h) * 64 + (u & 3) * 16, 0, 16);
        }
      }
    }
  }
  if (c == 0) STAMP(15);
  if (c == 5) {
    gather_q(p, L, h, ORDER, tag, lane);
    lds_set(ctl + C_Q0, 1u);
  }
  if (has_mlp) lds_wait_all<8, 1>(ctl + C_Y, j);

  int s = npro + c, pos = s % NSLOT;
  const int koff = r * 384 + j * 16;
#pragma nounroll
  for (int ph = 0; ph < 2; ++ph) {
    lds_wait<0>(ctl + C_Q0 + ph, 1);
    if (c == 0 && ph == 0) STAMP(8);
    v4u q[3];
#pragma unroll
    for (int u = 0; u < 3; ++u) q[u] = *(const LAS v4u*)(L + L_QB + ((ph ^ ORDER) * DH + (j + 8 * u) * 8) * 2);
    float mx = -3.0e38f, ls = 0.f, acc[24];
#pragma unroll
    for (int i = 0; i < 24; ++i) acc[i] = 0.f;
    const int send = ph ? ntot : ntot - 128;
#pragma nounroll
    for (; s < send; s += NC) {
      lds_wait<1>(ctl + C_LANDED, s + 1);
      const LAS unsigned char* sp = L + pos * SLOTB + koff;
      v4u kk[3], vv[3];
#pragma unroll
      for (int u = 0; u < 3; ++u) kk[u] = *(const LAS v4u*)(sp + u * 128);
#pragma unroll
      for (int u = 0; u < 3; ++u) vv[u] = *(const LAS v4u*)(sp + 3072 + u * 128);
      lds_set(ctl + C_NEXT + c, s + NC);
      float d0 = 0.f, d1 = 0.f, d2 = 0.f;
#pragma unroll
      for (int e = 0; e < 4; ++e) { d0 = dot2(kk[0][e], q[0][e], d0); d1 = dot2(kk[1][e], q[1][e], d1); d2 = dot2(kk[2][e], q[2][e], d2); }
      float d = d0 + d1 + d2;
      d += dpp<XOR1>(d); d += dpp<XOR2>(d); d += dpp<MIR8>(d);
      const float t = bfr(bfr(d) * 0.07216878364870322f) * LOG2E;
      const float mn = fmaxf(mx, t);
      const float al = exp2f_(mx - mn), pr = exp2f_(t - mn);
      mx = mn;
      ls = fmaf(ls, al, pr);
#pragma unroll
      for (int u = 0; u < 3; ++u)
#pragma unroll
        for (int e = 0; e < 4; ++e) {
          const int k = 8 * u + 2 * e;
          acc[k] = fmaf(pr, lo16(vv[u][e]), acc[k] * al);
          acc[k + 1] = fmaf(pr, hi16(vv[u][e]), acc[k + 1] * al);
        }
      pos += NC;
      if (pos >= NSLOT) pos -= NSLOT;
    }
    if (ph == 1 && c == 0) STAMP(9);
    const float M = max_rows(fmaxf(mx, dpp<ROR8>(mx)));
    const float sc = exp2f_(mx - M);
    ls *= sc;
    ls += dpp<ROR8>(ls);
    ls = sum_rows(ls);
#pragma unroll
    for (int i = 0; i < 24; ++i) acc[i] *= sc;
#pragma unroll
    for (int i = 0; i < 12; ++i) acc[i] = fold32(acc[i], acc[i + 12]);
#pragma unroll
    for (int i = 0; i < 6; ++i) { acc[i] = fold16(acc[i], acc[i + 6]); acc[i] += dpp<ROR8>(acc[i]); }
    LAS float* rec = (LAS float*)(L + L_SCR) + (ph * NC + c) * 200;
    if ((r & 1) == 0) {
#pragma unroll
      for (int u = 0; u < 3; ++u) {
        const int v = 6 * (lane >> 4) + 2 * u;
        *(LAS v2f*)(rec + (j + 8 * (v >> 3)) * 8 + (v & 7)) = v2f{acc[2 * u], acc[2 * u + 1]};
      }
    }
    if (lane == 0) *(LAS v2f*)(rec + 192) = v2f{M, ls};
    lds_set(ctl + C_R0 + 8 * ph + c, 1u);
  }
  if (c == 5) return;
  if (c < 3) {
    lds_wait_all<NC, 0>(ctl + C_R1, j);
    if (c == 0) STAMP(10);
    combine<1, 0>(p, L, 1, h, m, c * 64 + lane, c == 0, tag, lane);
    lds_set(ctl + C_O + c, 1u);
    if (c == 0) STAMP(11);
  }
  lds_wait<0>(ctl + C_LANDED, ntot + NWO);
  v4u bf[6];
  load_bt<6, 32 * 160, 4 * 160>(L + wo_pos * SLOTB + ((lane >> 4) * 8 + ((lane & 15) >> 2)) * 160 + c * 32 + (lane & 3) * 8, bf);
  lds_wait_all<4, 0>(ctl + C_O, j);
  const float sv = mm2<6>(L + L_OA + (lane & 1) * (DH * 2) + (lane >> 4) * 16, bf, lane);
  const int col = m * 80 + c * 16 + (lane & 15);
  if (lane < 32) st_gran(p.ws, W_SP + (((lane >> 4) * 80 + (col >> 3)) * NH + h) * 64 + (col & 7) * 8, sv, tag);
  lds_set(ctl + C_T3 + c, 1u);
  if (c == 0) STAMP(12);
}

}

namespace {
__global__ void k_null() {}
__global__ void k_mark(Params p) { ((GAS u64*)(p.ws + (5 << 20)))[0] = __builtin_amdgcn_s_memrealtime(); }
__global__ void __launch_bounds__(512) k_big(Params p) { extern __shared__ unsigned char sm[]; if (p.ws == nullptr) sm[threadIdx.x] = 1; }
}
extern "C" void kernel_launch(void* const* d_in, const int* in_sizes, int n_in, void* d_out, int out_size, void* d_ws,
                              size_t ws_size, hipStream_t stream) {
  Params p;
  p.x = (const unsigned short*)d_in[0];
  p.k = (const unsigned short*)d_in[1];
  p.v = (const unsigned short*)d_in[2];
  p.wq = (const unsigned short*)d_in[3];
  p.wo = (const unsigned short*)d_in[4];
  p.wr = (const unsigned short*)d_in[6];
  p.wg = (const unsigned short*)d_in[7];
  p.wu = (const unsigned short*)d_in[8];
  p.wd = (const unsigned short*)d_in[9];
  p.out = (unsigned short*)d_out;
  p.ws = (unsigned char*)d_ws;
  (void)hipFuncSetAttribute((const void*)fused_decode, hipFuncAttributeMaxDynamicSharedMemorySize, L_END);
  fused_decode<<<NWG, NTHR, L_END, stream>>>(p);
}
```

```cpp
#include <hip/hip_runtime.h>
#include <hip/hip_bf16.h>

namespace {

#define GAS __attribute__((address_space(1)))
#define LAS __attribute__((address_space(3)))
#define RLX_AGENT __ATOMIC_RELAXED, __HIP_MEMORY_SCOPE_AGENT
#define RLX_WG __ATOMIC_RELAXED, __HIP_MEMORY_SCOPE_WORKGROUP
typedef unsigned v4u __attribute__((ext_vector_type(4)));
typedef unsigned v2u __attribute__((ext_vector_type(2)));
typedef float v4f __attribute__((ext_vector_type(4)));
typedef float v2f __attribute__((ext_vector_type(2)));
typedef short v4s __attribute__((ext_vector_type(4)));
typedef __bf16 bf16x2 __attribute__((ext_vector_type(2)));
typedef __bf16 bf16x8 __attribute__((ext_vector_type(8)));
typedef unsigned long long u64;

#define K_INF 21
#define K_NSLOT 22
#define K_ORDER 0
#define K_PK0 0
#define K_PK1 0
#define K_PV0 0
#define K_PV1 0
#define K_PW 0

constexpr int DM = 640, NH = 32, DH = 192, SK = 8192, FF = 1536;
constexpr int NWG = 256, NC = 6, NTHR = (NC + 2) * 64;
constexpr int INF = K_INF;
constexpr int NSLOT = K_NSLOT;
constexpr int ORDER = K_ORDER;
constexpr int SLOTB = 6144, NWO = 5, NS_WQ = 7, NS_GU = 27, NS_DN = 16;
constexpr unsigned KV_BYTES = 2u * NH * SK * DH * 2u;
constexpr int SLAB = SK * DH * 2;
constexpr int NMLP = FF / 64;
constexpr int NFIN = 2 * DM / 8;
static_assert(INF >= 7 && INF <= 60, "vmcnt is 6 bits");

constexpr int L_XP = NSLOT * SLOTB;
constexpr int L_SCR = L_XP + 2560;
constexpr int L_QB = L_SCR + 16384;
constexpr int L_OA = L_QB + 768;
constexpr int L_HM = L_OA + 768;
constexpr int L_CTL = L_HM + 256;
constexpr int L_END = L_CTL + 320;
static_assert(L_END <= 160 * 1024 && L_END > 80 * 1024, "one workgroup per CU");
enum { C_NEXT = 0, C_P1 = 8, C_HM = 16, C_Y = 24, C_R0 = 32, C_R1 = 40, C_O = 48, C_T3 = 56, C_LANDED = 64, C_XOK, C_TAG, C_Q0, C_Q1 };

constexpr int W_EPOCH = 0;
constexpr int W_ML = 1024;
constexpr int W_OP = W_ML + 2 * NH * 8 * 16;
constexpr int W_SP = W_OP + 2 * NH * 8 * DH * 8;
constexpr int W_YP = W_SP + NFIN * NH * 64;
constexpr int W_QP = W_YP + NFIN * NMLP * 64;
constexpr int W_END = W_QP + NH * 2 * 384 * 8;

constexpr unsigned LDS_SPIN = 1u << 20, GLB_SPIN = 1u << 16;
constexpr float LOG2E = 1.4426950408889634f;

struct Params {
  const unsigned short *x, *k, *v, *wq, *wo, *wr, *wg, *wu, *wd;
  unsigned short* out;
  unsigned char* ws;
};

__device__ __forceinline__ int rfl(int v) { return __builtin_amdgcn_readfirstlane(v); }
__device__ __forceinline__ float bfr(float x) { return (float)(__bf16)x; }
__device__ __forceinline__ unsigned bfbits(float x) { return (unsigned)__builtin_bit_cast(unsigned short, (__bf16)x); }
__device__ __forceinline__ float lo16(unsigned u) { return __builtin_bit_cast(float, u << 16); }
__device__ __forceinline__ float hi16(unsigned u) { return __builtin_bit_cast(float, u & 0xffff0000u); }
__device__ __forceinline__ float asf(unsigned u) { return __builtin_bit_cast(float, u); }
__device__ __forceinline__ unsigned asu(float f) { return __builtin_bit_cast(unsigned, f); }
__device__ __forceinline__ float dot2(unsigned a, unsigned b, float c) {
  return __builtin_amdgcn_fdot2_f32_bf16(__builtin_bit_cast(bf16x2, a), __builtin_bit_cast(bf16x2, b), c, false);
}
template <int CTRL> __device__ __forceinline__ float dpp(float x) {
  return __builtin_bit_cast(float, __builtin_amdgcn_mov_dpp(__builtin_bit_cast(int, x), CTRL, 0xf, 0xf, true));
}
template <int CTRL> __device__ __forceinline__ int dppi(int x) { return __builtin_amdgcn_mov_dpp(x, CTRL, 0xf, 0xf, true); }
constexpr int XOR1 = 0xB1, XOR2 = 0x4E, MIR8 = 0x141, ROR4 = 0x124, ROR8 = 0x128;
__device__ __forceinline__ float fold32(float a, float b) {
  auto s = __builtin_amdgcn_permlane32_swap(asu(a), asu(b), false, false);
  return asf(s[0]) + asf(s[1]);
}
__device__ __forceinline__ float fold16(float a, float b) {
  auto s = __builtin_amdgcn_permlane16_swap(asu(a), asu(b), false, false);
  return asf(s[0]) + asf(s[1]);
}
__device__ __forceinline__ float max_rows(float x) {
  auto s = __builtin_amdgcn_permlane16_swap(asu(x), asu(x), false, false);
  x = fmaxf(asf(s[0]), asf(s[1]));
  auto t = __builtin_amdgcn_permlane32_swap(asu(x), asu(x), false, false);
  return fmaxf(asf(t[0]), asf(t[1]));
}
__device__ __forceinline__ float sum_rows(float x) { x = fold16(x, x); return fold32(x, x); }
__device__ __forceinline__ float wave_sum(float x) {
  x += dpp<XOR1>(x); x += dpp<XOR2>(x); x += dpp<ROR4>(x); x += dpp<ROR8>(x);
  return sum_rows(x);
}
__device__ __forceinline__ float exp2f_(float x) { return __builtin_amdgcn_exp2f(x); }
__device__ __forceinline__ float siluf(float x) { return x / (1.0f + expf(-x)); }
#define CBAR() asm volatile("" ::: "memory")

__device__ __forceinline__ __amdgpu_buffer_rsrc_t rsrc(const void* p, unsigned bytes) {
  return __builtin_amdgcn_make_buffer_rsrc((void*)p, 0, bytes, 0x00020000);
}
__device__ __forceinline__ void lds_set(LAS unsigned* w, unsigned v) {
  CBAR();
  __hip_atomic_store(w, v, RLX_WG);
  CBAR();
}
template <int SLEEP> __device__ __forceinline__ void lds_wait(LAS unsigned* w, unsigned want) {
#pragma nounroll
  for (unsigned n = 0; n < LDS_SPIN; ++n) {
    if ((unsigned)rfl((int)__hip_atomic_load(w, RLX_WG)) >= want) break;
    if (SLEEP) __builtin_amdgcn_s_sleep(SLEEP);
  }
  CBAR();
}
template <int N, int SLEEP> __device__ __forceinline__ void lds_wait_all(LAS unsigned* w, int j) {
  LAS unsigned* a = w + (j < N ? j : 0);
#pragma nounroll
  for (unsigned n = 0; n < LDS_SPIN; ++n) {
    if (__all(__hip_atomic_load(a, RLX_WG) != 0u)) break;
    if (SLEEP) __builtin_amdgcn_s_sleep(SLEEP);
  }
  CBAR();
}
__device__ __forceinline__ void st_gran(unsigned char* ws, int off, float v, unsigned tag) {
  __hip_atomic_store((GAS u64*)(ws + off), ((u64)tag << 32) | asu(v), RLX_AGENT);
}
__device__ __forceinline__ u64 ld_gran(const unsigned char* ws, int off) { return __hip_atomic_load((GAS u64*)(ws + off), RLX_AGENT); }

#define STAMP(i) do { } while (0)

__device__ __forceinline__ void layer_norm(v2u (&xr)[5], LAS unsigned char* L, int lane) {
  asm volatile("" : "+v"(xr[0]), "+v"(xr[1]), "+v"(xr[2]), "+v"(xr[3]), "+v"(xr[4]));
  v2f x[10];
#pragma unroll
  for (int i = 0; i < 5; ++i) { x[2 * i] = v2f{lo16(xr[i].x), hi16(xr[i].x)}; x[2 * i + 1] = v2f{lo16(xr[i].y), hi16(xr[i].y)}; }
  const float p0 = asf((unsigned)__builtin_amdgcn_readlane((int)asu(x[0].x), 0)), p1 = asf((unsigned)__builtin_amdgcn_readlane((int)asu(x[0].x), 32));
  const float piv = lane < 32 ? p0 : p1;
  v2f s1 = {0.f, 0.f}, s2 = {0.f, 0.f};
#pragma unroll
  for (int i = 0; i < 10; ++i) { const v2f d = x[i] - piv; s1 += d; s2 += d * d; }
  float a = s1.x + s1.y, q = s2.x + s2.y;
  a += dpp<XOR1>(a); q += dpp<XOR1>(q); a += dpp<XOR2>(a); q += dpp<XOR2>(q);
  a += dpp<ROR4>(a); q += dpp<ROR4>(q); a += dpp<ROR8>(a); q += dpp<ROR8>(q);
  a = fold16(a, a) * (1.0f / DM); q = fold16(q, q) * (1.0f / DM);
  const float mb = bfr(piv + a), var = bfr(fmaf(-a, a, q));
  const float den = bfr(__builtin_amdgcn_sqrtf(bfr(var + 1e-5f)));
  const float rcp = __builtin_amdgcn_rcpf(den);
#pragma unroll
  for (int i = 0; i < 5; ++i) {
    unsigned o[2];
#pragma unroll
    for (int e = 0; e < 2; ++e) {
      const v2f t = x[2 * i + e] - mb;
      const v2f d = {bfr(t.x), bfr(t.y)}, q0 = d * rcp;
      const v2f qq = (d - q0 * den) * rcp + q0;
      o[e] = bfbits(qq.x) | (bfbits(qq.y) << 16);
    }
    *(LAS v2u*)(L + L_XP + (lane >> 5) * 1280 + ((lane & 31) + 32 * i) * 8) = v2u{o[0], o[1]};
  }
}

__device__ __forceinline__ void gather_q(const Params& p, LAS unsigned char* L, int h, int b, unsigned tag, int lane) {
  const __amdgpu_buffer_rsrc_t rw = rsrc(p.ws, W_END);
  const int l = min(lane, 47), off = W_QP + (h * 768 + b * DH + 4 * l) * 8;
  v4u g[4];
#pragma nounroll
  for (unsigned n = 0; n < GLB_SPIN; ++n) {
    CBAR();
#pragma unroll
    for (int i = 0; i < 4; ++i) g[i] = __builtin_amdgcn_raw_buffer_load_b128(rw, off + (i & 1) * 16 + (i >> 1) * 3072, 0, 16);
    bool ok = true;
#pragma unroll
    for (int i = 0; i < 4; ++i) ok &= g[i].y == tag && g[i].w == tag;
    if (__all(ok)) break;
  }
  *(LAS v2u*)(L + L_QB + (b * DH + 4 * l) * 2) = v2u{bfbits(asf(g[0].x) + asf(g[2].x)) | (bfbits(asf(g[0].z) + asf(g[2].z)) << 16),
                                                      bfbits(asf(g[1].x) + asf(g[3].x)) | (bfbits(asf(g[1].z) + asf(g[3].z)) << 16)};
}

template <int NK, int KSTEP, int HALF>
__device__ __forceinline__ void load_bt(const LAS unsigned char* b, v4u (&f)[NK]) {
#pragma unroll
  for (int ks = 0; ks < NK; ++ks) {
    const v2u lo = __builtin_bit_cast(v2u, __builtin_amdgcn_ds_read_tr16_b64_v4i16((LAS v4s*)(b + ks * KSTEP)));
    const v2u hi = __builtin_bit_cast(v2u, __builtin_amdgcn_ds_read_tr16_b64_v4i16((LAS v4s*)(b + ks * KSTEP + HALF)));
    f[ks] = v4u{lo.x, lo.y, hi.x, hi.y};
  }
}
template <int NK> __device__ __forceinline__ float mm2(const LAS unsigned char* a, const v4u (&f)[NK], int lane) {
  v4u av[NK];
#pragma unroll
  for (int ks = 0; ks < NK; ++ks) av[ks] = *(const LAS v4u*)(a + ks * 64);
  v4f c = {0.f, 0.f, 0.f, 0.f};
#pragma unroll
  for (int ks = 0; ks < NK; ++ks) c = __builtin_amdgcn_mfma_f32_16x16x32_bf16(__builtin_bit_cast(bf16x8, av[ks]), __builtin_bit_cast(bf16x8, f[ks]), c, 0, 0, 0);
  return lane & 16 ? c[1] : c[0];
}

template <int ND, int SLEEP>
__device__ __forceinline__ void combine(const Params& p, LAS unsigned char* L, int ph, int h, int m, int d0, bool lead, unsigned tag, int lane) {
  const int b = ph ^ ORDER;
  const LAS float* rec = (const LAS float*)(L + L_SCR) + ph * NC * 200;
  float M = rec[192];
#pragma unroll
  for (int c = 1; c < NC; ++c) M = fmaxf(M, rec[c * 200 + 192]);
  float Ls = 0.f, o[ND];
#pragma unroll
  for (int i = 0; i < ND; ++i) o[i] = 0.f;
#pragma unroll
  for (int c = 0; c < NC; ++c) {
    const float w = exp2f_(rec[c * 200 + 192] - M);
    Ls = fmaf(w, rec[c * 200 + 193], Ls);
#pragma unroll
    for (int i = 0; i < ND; ++i) o[i] = fmaf(w, rec[c * 200 + d0 + 64 * i], o[i]);
  }
  const int bh = b * NH + h;
  const __amdgpu_buffer_rsrc_t rw = rsrc(p.ws, W_END);
#pragma unroll
  for (int i = 0; i < ND; ++i) st_gran(p.ws, W_OP + ((bh * 8 + m) * DH + d0 + 64 * i) * 8, o[i], tag);
  if (lead && lane == 0) __builtin_amdgcn_raw_buffer_store_b128(v4u{asu(M), tag, asu(Ls), tag}, rw, W_ML + (bh * 8 + m) * 16, 0, 16);
  u64 g[ND][8];
  v4u ml;
#pragma nounroll
  for (unsigned n = 0; n < GLB_SPIN; ++n) {
    CBAR();
    ml = __builtin_amdgcn_raw_buffer_load_b128(rw, W_ML + (bh * 8 + (lane & 7)) * 16, 0, 16);
#pragma unroll
    for (int i = 0; i < ND; ++i)
#pragma unroll
      for (int j = 0; j < 8; ++j) g[i][j] = ld_gran(p.ws, W_OP + ((bh * 8 + j) * DH + d0 + 64 * i) * 8);
    bool ok = ml.y == tag && ml.w == tag;
#pragma unroll
    for (int i = 0; i < ND; ++i)
#pragma unroll
      for (int j = 0; j < 8; ++j) ok &= (unsigned)(g[i][j] >> 32) == tag;
    if (__all(ok)) break;
    if (SLEEP) __builtin_amdgcn_s_sleep(SLEEP);
  }
  float Mg = asf(ml.x);
  Mg = fmaxf(Mg, dpp<XOR1>(Mg)); Mg = fmaxf(Mg, dpp<XOR2>(Mg)); Mg = fmaxf(Mg, dpp<MIR8>(Mg));
  const float w = exp2f_(asf(ml.x) - Mg);
  float Lg = w * asf(ml.z);
  Lg += dpp<XOR1>(Lg); Lg += dpp<XOR2>(Lg); Lg += dpp<MIR8>(Lg);
  const float wn = w / Lg;
#pragma unroll
  for (int i = 0; i < ND; ++i) {
    float a = 0.f;
#pragma unroll
    for (int j = 0; j < 8; ++j) a = fmaf(asf((unsigned)__builtin_amdgcn_readlane((int)asu(wn), j)), asf((unsigned)g[i][j]), a);
    *(LAS unsigned short*)(L + L_OA + (b * DH + d0 + 64 * i) * 2) = (unsigned short)bfbits(a);
  }
}

template <int NP, int SLEEP>
__device__ __forceinline__ v2f gather8(const Params& p, int base, unsigned tag, int lane) {
  static_assert(NP > 16 && NP <= 32, "two 16-byte pieces per lane");
  const __amdgpu_buffer_rsrc_t rw = rsrc(p.ws, W_END);
  const bool two = 64 + lane < NP * 4;
  v4u a, c;
#pragma nounroll
  for (unsigned n = 0; n < GLB_SPIN; ++n) {
    CBAR();
    a = __builtin_amdgcn_raw_buffer_load_b128(rw, base + lane * 16, 0, 16);
    c = __builtin_amdgcn_raw_buffer_load_b128(rw, base + (two ? 64 + lane : lane) * 16, 0, 16);
    if (__all(a.y == tag && a.w == tag && c.y == tag && c.w == tag)) break;
    if (SLEEP) __builtin_amdgcn_s_sleep(SLEEP);
  }
  float s0 = asf(a.x) + (two ? asf(c.x) : 0.f), s1 = asf(a.z) + (two ? asf(c.z) : 0.f);
  s0 += dpp<ROR4>(s0); s1 += dpp<ROR4>(s1);
  s0 += dpp<ROR8>(s0); s1 += dpp<ROR8>(s1);
  return v2f{sum_rows(s0), sum_rows(s1)};
}

#define DMA3(POL)                                                                    \
  asm volatile("s_mov_b32 m0, %3\n\t"                                                \
               "s_waitcnt vmcnt(%4)\n\t"                                             \
               "buffer_load_dwordx4 %0, %1, %2 offen " POL "lds\n\t"                 \
               "s_waitcnt vmcnt(%4)\n\t"                                             \
               "buffer_load_dwordx4 %0, %1, %2 offen offset:1024 " POL "lds\n\t"     \
               "s_waitcnt vmcnt(%4)\n\t"                                             \
               "buffer_load_dwordx4 %0, %1, %2 offen offset:2048 " POL "lds"         \
               :: "v"(voff), "s"(srd), "s"(soff), "s"(lds), "i"(N) : "memory")
#define DMA1(POL)                                                                    \
  asm volatile("s_mov_b32 m0, %3\n\t"                                                \
               "s_waitcnt vmcnt(%4)\n\t"                                             \
               "buffer_load_dwordx4 %0, %1, %2 offen " POL "lds"                     \
               :: "v"(voff), "s"(srd), "s"(soff), "s"(lds), "i"(N) : "memory")
template <int N, int POL> __device__ __forceinline__ void dma3(int voff, v4u srd, int soff, unsigned lds) {
  if constexpr (POL == 0) DMA3("nt "); else DMA3("");
}
template <int N, int POL> __device__ __forceinline__ void dma1(int voff, v4u srd, int soff, unsigned lds) {
  if constexpr (POL == 0) DMA1("nt "); else DMA1("");
}
__device__ __forceinline__ v4u srd_of(const void* ptr, unsigned bytes) {
  const u64 a = (u64)ptr;
  return v4u{(unsigned)a, (unsigned)(a >> 32) & 0xffffu, bytes, 0x00020000u};
}
constexpr int fdiv6(int a) { return a >= 0 ? a / 6 : -((5 - a) / 6); }
constexpr int LAG3 = fdiv6(3 - INF), LAG6 = fdiv6(6 - INF);

struct Loader {
  LAS unsigned* ctl;
  unsigned lbase;
  int s, pos, limit, j;
  __device__ __forceinline__ void credit(int last) {
    if (__builtin_expect(last >= limit, 0)) {
#pragma nounroll
      for (unsigned n = 0; n < LDS_SPIN; ++n) {
        int v = (int)__hip_atomic_load(ctl + C_NEXT + (j < NC ? j : 0), RLX_WG);
        v = min(v, dppi<XOR1>(v)); v = min(v, dppi<XOR2>(v)); v = min(v, dppi<MIR8>(v));
        limit = rfl(v) + NSLOT;
        if (last < limit) break;
        __builtin_amdgcn_s_sleep(1);
      }
      CBAR();
    }
  }
  __device__ __forceinline__ void publish(int n) { __hip_atomic_store(ctl + C_LANDED, (unsigned)max(n, 0), RLX_WG); }
  __device__ __forceinline__ unsigned dst() const { return lbase + pos * SLOTB; }
  __device__ __forceinline__ void advance() { ++s; pos = pos + 1 == NSLOT ? 0 : pos + 1; }
  template <int PK, int PV> __device__ __forceinline__ void kv(int i0, int i1, int n1, int st, int m, int slab, int voff, v4u rk, v4u rv) {
#pragma nounroll
    for (int i = i0; i < i1; ++i) {
      credit(s + 3);
      const int piece = i < n1 ? min(st * i + m, 63) : 64 + 8 * (i - n1) + m;
      const int so = slab + (piece & 255) * 12288;
#pragma unroll
      for (int u = 0; u < 4; ++u) {
        dma3<INF - 1, PK>(voff, rk, so + u * 3072, dst());
        if (LAG3 == LAG6) publish(s + LAG3);
        dma3<INF - 1, PV>(voff, rv, so + u * 3072, dst() + 3072);
        if (LAG3 != LAG6) publish(s + LAG6);
        advance();
      }
    }
  }
};

__device__ __forceinline__ void dot16(float (&a)[16], v4u w, unsigned x0l, unsigned x1l) {
  const unsigned x0h = x0l << 16, x1h = x1l << 16;
#pragma unroll
  for (int e = 0; e < 4; ++e) {
    a[2 * e] = dot2(w[e], x0l, a[2 * e]);         a[2 * e + 1] = dot2(w[e], x0h, a[2 * e + 1]);
    a[8 + 2 * e] = dot2(w[e], x1l, a[8 + 2 * e]); a[9 + 2 * e] = dot2(w[e], x1h, a[9 + 2 * e]);
  }
}

__global__ void __launch_bounds__(NTHR, 2) fused_decode(Params p) {
  extern __shared__ __attribute__((aligned(16))) unsigned char smem[];
  LAS unsigned char* L = (LAS unsigned char*)smem;
  LAS unsigned* ctl = (LAS unsigned*)(L + L_CTL);
  const int tid = threadIdx.x, lane = tid & 63, wave = rfl(tid >> 6);
  const int bid = blockIdx.x, h = bid & 31, m = bid >> 5;
  const int r = lane >> 3, j = lane & 7;
  const bool has_wq = m < 6, has_mlp = m == 7 && h < NMLP;
  const int npro = has_wq ? NS_WQ : has_mlp ? NS_GU + NS_DN : 0;
  const int st = h < NMLP ? 7 : 8, n1 = h < NMLP ? (m < 6 ? 9 : m == 6 ? 10 : 0) : 8;
  const int ntot = npro + 4 * (n1 + 56);
  const int wo_skip = ntot % NSLOT + NWO > NSLOT ? NSLOT - ntot % NSLOT : 0, wo_pos = (ntot + wo_skip) % NSLOT;

  if (wave == 0) {
    __builtin_amdgcn_s_setprio(3);
    STAMP(0);
    Loader ld{ctl, (unsigned)(size_t)L, 0, 0, NSLOT, j};
    const v4u rk = srd_of(p.k, KV_BYTES), rv = srd_of(p.v, KV_BYTES);
    const int voff = lane * 16;
    const int slab0 = (ORDER * NH + h) * SLAB, slab1 = ((ORDER ^ 1) * NH + h) * SLAB;
    int i0 = 0;
    if (has_wq) {
      const v4u rq = srd_of(p.wq, DM * NH * DH * 2);
      const int vo = r * (NH * DH * 2) + (j ^ (r & 2)) * 16, so = (m / 3) * 320 * (NH * DH * 2) + h * (DH * 2) + (m % 3) * 128;
#pragma unroll 6
      for (int g = 0; g < 6 * NS_WQ; ++g) dma1<63, K_PW>(g & 1 ? vo ^ 64 : vo, rq, so + min(g, 39) * (8 * NH * DH * 2), ld.lbase + g * 1024);
      ld.s = ld.pos = NS_WQ;
      __builtin_amdgcn_s_barrier();
    } else if (has_mlp) {
      __builtin_amdgcn_s_barrier();
      const v4u rg = srd_of(p.wg, DM * FF * 2), ru = srd_of(p.wu, DM * FF * 2), rd = srd_of(p.wd, FF * DM * 2);
      const int vo = r * (FF * 2) + j * 16, ve = (lane >> 4) * (DM * 2) + 1024 + (lane & 15) * 16;
#pragma nounroll
      for (int t = 0; t < NS_GU; ++t) {
        ld.credit(ld.s);
#pragma unroll
        for (int q = 0; q < 6; ++q) dma1<INF - 1, K_PW>(vo, q & 1 ? ru : rg, min(3 * t + (q >> 1), 79) * (8 * FF * 2) + h * 128, ld.dst() + q * 1024);
        ld.publish(ld.s + LAG6);
        ld.advance();
      }
#pragma nounroll
      for (int t = 0; t < NS_DN; ++t) {
        ld.credit(ld.s);
#pragma unroll
        for (int q = 0; q < 6; ++q) dma1<INF - 1, K_PW>(q < 4 ? voff : ve, rd, (64 * h + 4 * t + (q < 4 ? q : 0)) * (DM * 2), ld.dst() + q * 1024);
        ld.publish(ld.s + LAG6);
        ld.advance();
      }
    } else {
      const int so = slab0 + (n1 ? m : 64 + m) * 12288;
#pragma unroll
      for (int u = 0; u < 4; ++u) {
        dma3<63, ORDER ? K_PK1 : K_PK0>(voff, rk, so + u * 3072, ld.dst());
        dma3<63, ORDER ? K_PV1 : K_PV0>(voff, rv, so + u * 3072, ld.dst() + 3072);
        ld.advance();
      }
      i0 = 1;
      __builtin_amdgcn_s_barrier();
    }
    CBAR();
    ld.kv<ORDER ? K_PK1 : K_PK0, ORDER ? K_PV1 : K_PV0>(i0, n1 + 24, n1, st, m, slab0, voff, rk, rv);
    ld.kv<ORDER ? K_PK0 : K_PK1, ORDER ? K_PV0 : K_PV1>(n1 + 24, n1 + 56, n1, st, m, slab1, voff, rk, rv);
    STAMP(1);
    const v4u ro = srd_of(p.wo, NH * DH * DM * 2);
    int row = lane / 10, ch = lane - row * 10;
    ld.credit(ld.s + wo_skip + NWO - 1);
    ld.pos = wo_pos;
#pragma nounroll
    for (int t = 0; t < NWO; ++t) {
#pragma unroll
      for (int q = 0; q < 6; ++q) {
        dma1<INF - 1, K_PW>(row * (DM * 2) + ch * 16, ro, h * DH * (DM * 2) + m * 160, ld.dst() + q * 1024);
        if (q == 2 && LAG3 == LAG6) ld.publish(ld.s + LAG3);
        if (q == 5 && LAG3 != LAG6) ld.publish(ld.s + LAG6);
        row += 6; ch += 4;
        if (ch >= 10) { ch -= 10; row += 1; }
      }
      ld.advance();
    }
    if (INF > 30) { asm volatile("s_waitcnt vmcnt(30)" ::: "memory"); ld.publish(ld.s - NWO); }
    asm volatile("s_waitcnt vmcnt(0)" ::: "memory");
    ld.publish(ld.s);
    STAMP(2);
    return;
  }

  if (wave == NC + 1) {
    v2u xr[5];
#pragma unroll
    for (int i = 0; i < 5; ++i) xr[i] = ((const GAS v2u*)p.x)[(lane >> 5) * 160 + (lane & 31) + 32 * i];
    const unsigned e0 = __hip_atomic_load((GAS unsigned*)(p.ws + W_EPOCH), RLX_AGENT);
    ctl[lane] = lane < NC && (npro == 0 || (has_wq && lane >= 4)) ? npro + lane : 0;
    if (lane < 16) ctl[64 + lane] = 0u;
    asm volatile("s_waitcnt lgkmcnt(0)" ::: "memory");
    __builtin_amdgcn_s_barrier();
    CBAR();
    layer_norm(xr, L, lane);
    const unsigned tag = (unsigned)rfl((int)e0) + 1u;
    ctl[C_TAG] = tag;
    lds_set(ctl + C_XOK, 1u);
    STAMP(3);
    lds_wait<8>(ctl + C_Q0, 1);
    gather_q(p, L, h, ORDER ^ 1, tag, lane);
    lds_set(ctl + C_Q1, 1u);
    lds_wait_all<NC, 16>(ctl + C_R0, j);
    combine<3, 8>(p, L, 0, h, m, lane, true, tag, lane);
    lds_set(ctl + C_O + 3, 1u);
    STAMP(4);
    if (bid >= NFIN) return;
    const int fb = bid / 80, col = (bid - fb * 80) * 8 + 2 * (lane & 3);
    float rs = 0.f;
#pragma unroll
    for (int i = 0; i < 5; ++i) {
      const unsigned w = ((const GAS unsigned*)p.wr)[lane + 64 * i];
      rs = dot2(w, *(const LAS unsigned*)(L + L_XP + fb * 1280 + (lane + 64 * i) * 4), rs);
    }
    const float sg = bfr(1.0f / (1.0f + expf(-bfr(wave_sum(rs)))));
    const unsigned xo = ((const GAS unsigned*)p.x)[(fb * DM + col) >> 1];
    const v2f y = gather8<NMLP, 8>(p, W_YP + bid * NMLP * 64, tag, lane);
    const unsigned xx = *(const LAS unsigned*)(L + L_XP + fb * 1280 + col * 2);
    const float g0 = bfr(siluf(bfr(lo16(xx) + bfr(bfr(y.x) * sg))));
    const float g1 = bfr(siluf(bfr(hi16(xx) + bfr(bfr(y.y) * sg))));
    STAMP(5);
    lds_wait_all<5, 2>(ctl + C_T3, j);
    const v2f s = gather8<NH, 0>(p, W_SP + bid * NH * 64, tag, lane);
    const unsigned o = bfbits(lo16(xo) + bfr(bfr(s.x) * g0)) | (bfbits(hi16(xo) + bfr(bfr(s.y) * g1)) << 16);
    if (lane < 4) ((GAS unsigned*)p.out)[(fb * DM + col) >> 1] = o;
    if (bid == 0 && lane == 0) __hip_atomic_store((GAS unsigned*)(p.ws + W_EPOCH), tag, RLX_AGENT);
    STAMP(6);
    return;
  }

  const int c = wave - 1;
  __builtin_amdgcn_s_barrier();
  CBAR();
  lds_wait<1>(ctl + C_XOK, 1);
  const unsigned tag = (unsigned)rfl((int)ctl[C_TAG]);
  const __amdgpu_buffer_rsrc_t rw = rsrc(p.ws, W_END);
  if (c == 0) STAMP(13);
  if (has_wq) {
    if (c < 4) {
      const int sw = ((lane >> 3) & 1) | ((lane >> 3) & 2);
      lds_wait<0>(ctl + C_LANDED, NS_WQ);
      v4u bf[10];
      load_bt<10, 4096, 512>(L + (lane >> 4) * 1024 + ((lane & 15) >> 2) * 128 + ((2 * c + ((lane & 3) >> 1)) ^ (2 * sw)) * 16 + (lane & 1) * 8, bf);
      lds_set(ctl + C_NEXT + c, NS_WQ + c);
      const float qv = mm2<10>(L + L_XP + (lane & 1) * 1280 + (m / 3) * 640 + (lane >> 4) * 16, bf, lane);
      if (lane < 32) st_gran(p.ws, W_QP + ((h * 2 + m / 3) * 384 + (lane >> 4) * DH + (m % 3) * 64 + 16 * c + (lane & 15)) * 8, qv, tag);
    }
  } else if (has_mlp) {
    const LAS unsigned short* xp = (const LAS unsigned short*)(L + L_XP);
    int pos = 0;
    float a[16];
#pragma unroll
    for (int i = 0; i < 16; ++i) a[i] = 0.f;
#pragma nounroll
    for (int t = 0; t < NS_GU; ++t) {
      const int g = 3 * t + (c >> 1);
      lds_wait<1>(ctl + C_LANDED, t + 1);
      const v4u w = *(const LAS v4u*)(L + pos * SLOTB + c * 1024 + lane * 16);
      const unsigned x0 = xp[min(g, 79) * 8 + r], x1 = xp[DM + min(g, 79) * 8 + r];
      lds_set(ctl + C_NEXT + c, t + 1 == NS_GU && c == 5 ? NS_GU + NS_DN + c : t + 1);
      if (g < 80) dot16(a, w, x0, x1);
      pos = pos + 1 == NSLOT ? 0 : pos + 1;
    }
#pragma unroll
    for (int i = 0; i < 8; ++i) a[i] = fold32(a[i], a[i + 8]);
#pragma unroll
    for (int i = 0; i < 4; ++i) { a[i] = fold16(a[i], a[i + 4]); a[i] += dpp<ROR8>(a[i]); }
    if ((r & 1) == 0) *(LAS v4f*)(L + L_SCR + (c * 128 + (lane >> 5) * 64 + 8 * j + 4 * ((lane >> 4) & 1)) * 4) = v4f{a[0], a[1], a[2], a[3]};
    lds_set(ctl + C_P1 + c, 1u);
    if (c < 2) {
      lds_wait_all<NC, 0>(ctl + C_P1, j);
      const LAS float* pp = (const LAS float*)(L + L_SCR) + c * 64 + lane;
      const float s0 = pp[0] + pp[256] + pp[512], s1 = pp[128] + pp[384] + pp[640];
      *(LAS unsigned short*)(L + L_HM + lane * 4 + c * 2) = (unsigned short)bfbits(bfr(siluf(bfr(s0))) * bfr(s1));
      lds_set(ctl + C_HM + c, 1u);
    }
    if (c < 5) {
      lds_wait_all<2, 1>(ctl + C_HM, j);
#pragma unroll
      for (int i = 0; i < 16; ++i) a[i] = 0.f;
#pragma nounroll
      for (int t = 0; t < NS_DN; ++t) {
        lds_wait<1>(ctl + C_LANDED, NS_GU + t + 1);
        const v4u w = *(const LAS v4u*)(L + pos * SLOTB + c * 1024 + lane * 16);
        const unsigned xx = ((const LAS unsigned*)(L + L_HM))[4 * t + (c < 4 ? c : lane >> 4)];
        lds_set(ctl + C_NEXT + c, t + 1 == NS_DN ? NS_GU + NS_DN + c : NS_GU + t + 1);
        dot16(a, w, xx & 0xffffu, xx >> 16);
        pos = pos + 1 == NSLOT ? 0 : pos + 1;
      }
      if (c < 4) {
#pragma unroll
        for (int u = 0; u < 4; ++u) *(LAS v4f*)(L + L_SCR + c * 4096 + u * 1024 + lane * 16) = v4f{a[4 * u], a[4 * u + 1], a[4 * u + 2], a[4 * u + 3]};
        lds_set(ctl + C_Y + c, 1u);
        lds_wait_all<4, 0>(ctl + C_Y, j);
        v4f y = {0.f, 0.f, 0.f, 0.f};
#pragma unroll
        for (int w = 0; w < 4; ++w) y += *(const LAS v4f*)(L + L_SCR + w * 4096 + c * 1024 + lane * 16);
        lds_set(ctl + C_Y + 4 + c, 1u);
        const int off = W_YP + (((c >> 1) * 80 + lane) * NMLP + h) * 64 + (c & 1) * 32;
        __builtin_amdgcn_raw_buffer_store_b128(v4u{asu(y.x), tag, asu(y.y), tag}, rw, off, 0, 16);
        __builtin_amdgcn_raw_buffer_store_b128(v4u{asu(y.z), tag, asu(y.w), tag}, rw, off + 16, 0, 16);
      } else {
#pragma unroll
        for (int i = 0; i < 16; ++i) a[i] = sum_rows(a[i]);
        if (lane < 16) {
#pragma unroll
          for (int u = 0; u < 8; ++u)
            __builtin_amdgcn_raw_buffer_store_b128(v4u{asu(a[2 * u]), tag, asu(a[2 * u + 1]), tag}, rw,
                                                   W_YP + (((u >> 2) * 80 + 64 + lane) * NMLP + h) * 64 + (u & 3) * 16, 0, 16);
        }
      }
    }
  }
  if (c == 0) STAMP(15);
  if (c == 5) {
    gather_q(p, L, h, ORDER, tag, lane);
    lds_set(ctl + C_Q0, 1u);
  }
  if (has_mlp) lds_wait_all<8, 1>(ctl + C_Y, j);

  int s = npro + c, pos = s % NSLOT;
  const int koff = r * 384 + j * 16;
#pragma nounroll
  for (int ph = 0; ph < 2; ++ph) {
    lds_wait<0>(ctl + C_Q0 + ph, 1);
    if (c == 0 && ph == 0) STAMP(8);
    v4u q[3];
#pragma unroll
    for (int u = 0; u < 3; ++u) q[u] = *(const LAS v4u*)(L + L_QB + ((ph ^ ORDER) * DH + (j + 8 * u) * 8) * 2);
    float mx = -3.0e38f, ls = 0.f, acc[24];
#pragma unroll
    for (int i = 0; i < 24; ++i) acc[i] = 0.f;
    const int send = ph ? ntot : ntot - 128;
#pragma nounroll
    for (; s < send; s += NC) {
      lds_wait<1>(ctl + C_LANDED, s + 1);
      const LAS unsigned char* sp = L + pos * SLOTB + koff;
      v4u kk[3], vv[3];
#pragma unroll
      for (int u = 0; u < 3; ++u) kk[u] = *(const LAS v4u*)(sp + u * 128);
#pragma unroll
      for (int u = 0; u < 3; ++u) vv[u] = *(const LAS v4u*)(sp + 3072 + u * 128);
      lds_set(ctl + C_NEXT + c, s + NC);
      float d0 = 0.f, d1 = 0.f, d2 = 0.f;
#pragma unroll
      for (int e = 0; e < 4; ++e) { d0 = dot2(kk[0][e], q[0][e], d0); d1 = dot2(kk[1][e], q[1][e], d1); d2 = dot2(kk[2][e], q[2][e], d2); }
      float d = d0 + d1 + d2;
      d += dpp<XOR1>(d); d += dpp<XOR2>(d); d += dpp<MIR8>(d);
      const float t = bfr(bfr(d) * 0.07216878364870322f) * LOG2E;
      const float mn = fmaxf(mx, t);
      const float al = exp2f_(mx - mn), pr = exp2f_(t - mn);
      mx = mn;
      ls = fmaf(ls, al, pr);
#pragma unroll
      for (int u = 0; u < 3; ++u)
#pragma unroll
        for (int e = 0; e < 4; ++e) {
          const int k = 8 * u + 2 * e;
          acc[k] = fmaf(pr, lo16(vv[u][e]), acc[k] * al);
          acc[k + 1] = fmaf(pr, hi16(vv[u][e]), acc[k + 1] * al);
        }
      pos += NC;
      if (pos >= NSLOT) pos -= NSLOT;
    }
    if (ph == 1 && c == 0) STAMP(9);
    const float M = max_rows(fmaxf(mx, dpp<ROR8>(mx)));
    const float sc = exp2f_(mx - M);
    ls *= sc;
    ls += dpp<ROR8>(ls);
    ls = sum_rows(ls);
#pragma unroll
    for (int i = 0; i < 24; ++i) acc[i] *= sc;
#pragma unroll
    for (int i = 0; i < 12; ++i) acc[i] = fold32(acc[i], acc[i + 12]);
#pragma unroll
    for (int i = 0; i < 6; ++i) { acc[i] = fold16(acc[i], acc[i + 6]); acc[i] += dpp<ROR8>(acc[i]); }
    LAS float* rec = (LAS float*)(L + L_SCR) + (ph * NC + c) * 200;
    if ((r & 1) == 0) {
#pragma unroll
      for (int u = 0; u < 3; ++u) {
        const int v = 6 * (lane >> 4) + 2 * u;
        *(LAS v2f*)(rec + (j + 8 * (v >> 3)) * 8 + (v & 7)) = v2f{acc[2 * u], acc[2 * u + 1]};
      }
    }
    if (lane == 0) *(LAS v2f*)(rec + 192) = v2f{M, ls};
    lds_set(ctl + C_R0 + 8 * ph + c, 1u);
  }
  if (c == 5) return;
  if (c < 3) {
    lds_wait_all<NC, 0>(ctl + C_R1, j);
    if (c == 0) STAMP(10);
    combine<1, 0>(p, L, 1, h, m, c * 64 + lane, c == 0, tag, lane);
    lds_set(ctl + C_O + c, 1u);
    if (c == 0) STAMP(11);
  }
  lds_wait<0>(ctl + C_LANDED, ntot + NWO);
  v4u bf[6];
  load_bt<6, 32 * 160, 4 * 160>(L + wo_pos * SLOTB + ((lane >> 4) * 8 + ((lane & 15) >> 2)) * 160 + c * 32 + (lane & 3) * 8, bf);
  lds_wait_all<4, 0>(ctl + C_O, j);
  const float sv = mm2<6>(L + L_OA + (lane & 1) * (DH * 2) + (lane >> 4) * 16, bf, lane);
  const int col = m * 80 + c * 16 + (lane & 15);
  if (lane < 32) st_gran(p.ws, W_SP + (((lane >> 4) * 80 + (col >> 3)) * NH + h) * 64 + (col & 7) * 8, sv, tag);
  lds_set(ctl + C_T3 + c, 1u);
  if (c == 0) STAMP(12);
}

}

namespace {
__global__ void __launch_bounds__(512) k_big(Params p) { extern __shared__ unsigned char sm[]; if (p.ws == nullptr) sm[threadIdx.x] = 1; }
__global__ void __launch_bounds__(512) k_bigv(Params p) { extern __shared__ unsigned char sm[]; asm volatile("v_mov_b32 v127, 0" ::: "v127"); if (p.ws == nullptr) sm[threadIdx.x] = 1; }
}
extern "C" void kernel_launch(void* const* d_in, const int* in_sizes, int n_in, void* d_out, int out_size, void* d_ws,
                              size_t ws_size, hipStream_t stream) {
  Params p;
  p.x = (const unsigned short*)d_in[0];
  p.k = (const unsigned short*)d_in[1];
  p.v = (const unsigned short*)d_in[2];
  p.wq = (const unsigned short*)d_in[3];
  p.wo = (const unsigned short*)d_in[4];
  p.wr = (const unsigned short*)d_in[6];
  p.wg = (const unsigned short*)d_in[7];
  p.wu = (const unsigned short*)d_in[8];
  p.wd = (const unsigned short*)d_in[9];
  p.out = (unsigned short*)d_out;
  p.ws = (unsigned char*)d_ws;
  (void)hipFuncSetAttribute((const void*)fused_decode, hipFuncAttributeMaxDynamicSharedMemorySize, L_END);
  (void)hipFuncSetAttribute((const void*)k_big, hipFuncAttributeMaxDynamicSharedMemorySize, L_END);
  (void)hipFuncSetAttribute((const void*)k_bigv, hipFuncAttributeMaxDynamicSharedMemorySize, L_END);
  fused_decode<<<NWG, NTHR, L_END, stream>>>(p);
  k_big<<<1, 64, 0, stream>>>(p);
  k_big<<<1, 64, 0, stream>>>(p);
  k_big<<<1, 64, 0, stream>>>(p);
  k_big<<<1, 64, 0, stream>>>(p);
  k_big<<<1, 64, 0, stream>>>(p);
  k_big<<<1, 64, 0, stream>>>(p);
  k_big<<<1, 64, 0, stream>>>(p);
  k_big<<<1, 64, 0, stream>>>(p);

}
```

```cpp
#define K_STAMPS 1
#define IDX 15
#define MODE 0
#define SCALE 0
#define NWGS 80
#include <hip/hip_runtime.h>
#include <hip/hip_bf16.h>

namespace {

#define GAS __attribute__((address_space(1)))
#define LAS __attribute__((address_space(3)))
#define RLX_AGENT __ATOMIC_RELAXED, __HIP_MEMORY_SCOPE_AGENT
#define RLX_WG __ATOMIC_RELAXED, __HIP_MEMORY_SCOPE_WORKGROUP
typedef unsigned v4u __attribute__((ext_vector_type(4)));
typedef unsigned v2u __attribute__((ext_vector_type(2)));
typedef float v4f __attribute__((ext_vector_type(4)));
typedef float v2f __attribute__((ext_vector_type(2)));
typedef short v4s __attribute__((ext_vector_type(4)));
typedef __bf16 bf16x2 __attribute__((ext_vector_type(2)));
typedef __bf16 bf16x8 __attribute__((ext_vector_type(8)));
typedef unsigned long long u64;

#define K_INF 21
#define K_NSLOT 24
#define K_A1 39
#define K_C1 12
#define K_A2 35
#define K_WMLP 33
#define K_Q4 1
#define K_HROT 1
#define K_G0 10
#define K_BLIND 2
#define K_SL1 4
#define K_SL2 16
#define K_LEFT 4
#define K_ABL 0xff
#define K_XMAP 0
#define K_STR 21
#define K_ROT 1
#define K_QWAIT 8

constexpr int DM = 640, NH = 32, DH = 192, SK = 8192, FF = 1536;
constexpr int NWG = 256, NC = 6, NTHR = (NC + 2) * 64;
constexpr int INF = K_INF;
constexpr int NSLOT = K_NSLOT;
constexpr int SLOTB = 6144, RINGB = NSLOT * SLOTB, NWO = 5, NS_WQ = 7, NS_MLP = 40;
constexpr unsigned KV_BYTES = 2u * NH * SK * DH * 2u;
constexpr int SLAB = SK * DH * 2;
constexpr int NMLP = FF / 64;
constexpr int NFIN = DM / 8;
static_assert(NSLOT == 24, "the MLP images rely on where the ring wraps");
static_assert(INF >= 7 && INF <= 30, "vmcnt is 6 bits; the last K/V slots are published while wo is requested");

constexpr int L_XP = NSLOT * SLOTB;
constexpr int L_SCR = L_XP + 2560;
constexpr int L_QB = L_SCR + 9600;
constexpr int L_OA = L_QB + 768;
constexpr int L_HM = L_OA + 768;
constexpr int L_CTL = L_HM + 256;
constexpr int L_END = L_CTL + 320;
static_assert(L_END <= 160 * 1024 && L_END > 80 * 1024, "one workgroup per CU");
enum { C_NEXT = 0, C_HM = 16, C_R0 = 32, C_R1 = 40, C_T3 = 48, C_LANDED = 64, C_XOK, C_TAG, C_Q, C_O };

constexpr int W_EPOCH = 0;
constexpr int W_ML = 1024;
constexpr int W_OP = W_ML + 2 * NH * 8 * 16;
constexpr int W_SP = W_OP + 2 * NH * 8 * 768;
constexpr int W_YP = W_SP + NH * DM * 8;
constexpr int W_QP = W_YP + NMLP * DM * 8;
constexpr int W_END = W_QP + NH * 2 * DH * 8;
constexpr int W_ST = 4 << 20;

constexpr unsigned LDS_SPIN = 1u << 20, GLB_SPIN = 1u << 16;
constexpr float LOG2E = 1.4426950408889634f;

struct Params {
  const unsigned short *x, *k, *v, *wq, *wo, *wr, *wg, *wu, *wd;
  unsigned short* out;
  unsigned char* ws;
};

__device__ __forceinline__ int rfl(int v) { return __builtin_amdgcn_readfirstlane(v); }
__device__ __forceinline__ float bfr(float x) { return (float)(__bf16)x; }
__device__ __forceinline__ unsigned bfbits(float x) { return (unsigned)__builtin_bit_cast(unsigned short, (__bf16)x); }
__device__ __forceinline__ float lo16(unsigned u) { return __builtin_bit_cast(float, u << 16); }
__device__ __forceinline__ float hi16(unsigned u) { return __builtin_bit_cast(float, u & 0xffff0000u); }
__device__ __forceinline__ float asf(unsigned u) { return __builtin_bit_cast(float, u); }
__device__ __forceinline__ unsigned asu(float f) { return __builtin_bit_cast(unsigned, f); }
__device__ __forceinline__ float dot2(unsigned a, unsigned b, float c) {
  return __builtin_amdgcn_fdot2_f32_bf16(__builtin_bit_cast(bf16x2, a), __builtin_bit_cast(bf16x2, b), c, false);
}
template <int CTRL> __device__ __forceinline__ float dpp(float x) {
  return __builtin_bit_cast(float, __builtin_amdgcn_mov_dpp(__builtin_bit_cast(int, x), CTRL, 0xf, 0xf, true));
}
template <int CTRL> __device__ __forceinline__ int dppi(int x) { return __builtin_amdgcn_mov_dpp(x, CTRL, 0xf, 0xf, true); }
constexpr int XOR1 = 0xB1, XOR2 = 0x4E, MIR8 = 0x141, ROR4 = 0x124, ROR8 = 0x128;
__device__ __forceinline__ float fold32(float a, float b) {
  auto s = __builtin_amdgcn_permlane32_swap(asu(a), asu(b), false, false);
  return asf(s[0]) + asf(s[1]);
}
__device__ __forceinline__ float fold16(float a, float b) {
  auto s = __builtin_amdgcn_permlane16_swap(asu(a), asu(b), false, false);
  return asf(s[0]) + asf(s[1]);
}
__device__ __forceinline__ float max_rows(float x) {
  auto s = __builtin_amdgcn_permlane16_swap(asu(x), asu(x), false, false);
  x = fmaxf(asf(s[0]), asf(s[1]));
  auto t = __builtin_amdgcn_permlane32_swap(asu(x), asu(x), false, false);
  return fmaxf(asf(t[0]), asf(t[1]));
}
__device__ __forceinline__ float sum_rows(float x) { x = fold16(x, x); return fold32(x, x); }
__device__ __forceinline__ float wave_sum(float x) {
  x += dpp<XOR1>(x); x += dpp<XOR2>(x); x += dpp<ROR4>(x); x += dpp<ROR8>(x);
  return sum_rows(x);
}
__device__ __forceinline__ float exp2f_(float x) { return __builtin_amdgcn_exp2f(x); }
__device__ __forceinline__ float siluf(float x) { return x / (1.0f + expf(-x)); }
#define CBAR() asm volatile("" ::: "memory")

__device__ __forceinline__ __amdgpu_buffer_rsrc_t rsrc(const void* p, unsigned bytes) {
  return __builtin_amdgcn_make_buffer_rsrc((void*)p, 0, bytes, 0x00020000);
}
__device__ __forceinline__ void lds_set(LAS unsigned* w, unsigned v) {
  CBAR();
  __hip_atomic_store(w, v, RLX_WG);
  CBAR();
}
template <int SLEEP> __device__ __forceinline__ void lds_wait(LAS unsigned* w, unsigned want) {
#pragma nounroll
  for (unsigned n = 0; n < LDS_SPIN; ++n) {
    if ((unsigned)rfl((int)__hip_atomic_load(w, RLX_WG)) >= want) break;
    if (SLEEP) __builtin_amdgcn_s_sleep(SLEEP);
  }
  CBAR();
}
template <int N, int SLEEP> __device__ __forceinline__ void lds_wait_all(LAS unsigned* w, int j) {
  LAS unsigned* a = w + (j < N ? j : 0);
#pragma nounroll
  for (unsigned n = 0; n < LDS_SPIN; ++n) {
    if (__all(__hip_atomic_load(a, RLX_WG) != 0u)) break;
    if (SLEEP) __builtin_amdgcn_s_sleep(SLEEP);
  }
  CBAR();
}
__device__ __forceinline__ unsigned tag_of(unsigned e) { return ((e & 0xffu) | (((e >> 8) ^ e) & 0xffu) << 8) * 0x10001u; }
__device__ __forceinline__ v2u pack2(float a, float b, unsigned tag) {
  return v2u{__builtin_amdgcn_perm(asu(a), tag, 0x07060500u), __builtin_amdgcn_perm(asu(b), tag, 0x07060501u)};
}
__device__ __forceinline__ v4u pack4(float a, float b, float c, float d, unsigned tag) {
  const v2u x = pack2(a, b, tag), y = pack2(c, d, tag);
  return v4u{x.x, x.y, y.x, y.y};
}
__device__ __forceinline__ bool fresh(v4u g, unsigned tag) {
  return (__builtin_amdgcn_perm(g.y, g.x, 0x0c0c0400u) | __builtin_amdgcn_perm(g.w, g.z, 0x04000c0cu)) == tag;
}
__device__ __forceinline__ float val(unsigned w) { return asf(w & 0xffffff00u); }
__device__ __forceinline__ v4u ld16(__amdgpu_buffer_rsrc_t rw, int off) { return __builtin_amdgcn_raw_buffer_load_b128(rw, off, 0, 16); }
__device__ __forceinline__ void st16(__amdgpu_buffer_rsrc_t rw, int off, v4u v) { __builtin_amdgcn_raw_buffer_store_b128(v, rw, off, 0, 16); }
__device__ __forceinline__ void st8(__amdgpu_buffer_rsrc_t rw, int off, v2u v) { __builtin_amdgcn_raw_buffer_store_b64(v, rw, off, 0, 16); }

struct Stamps {
  u64 t[8];
  __device__ __forceinline__ void flush(const Params& p, int base, int n) {
#pragma unroll
    for (int i = 0; i < 8; ++i) if (i < n) ((GAS u64*)(p.ws + W_ST))[blockIdx.x * 32 + base + i] = t[i];
  }
};
#define STAMP(i) do { __builtin_amdgcn_sched_barrier(0); asm volatile("s_memrealtime %0\n\ts_waitcnt lgkmcnt(0)" : "=s"(st.t[i]) :: "memory"); __builtin_amdgcn_sched_barrier(0); } while (0)
#define STAMPS() Stamps st = {}
#define FLUSH(base, n) st.flush(p, base, n)

__device__ __forceinline__ void layer_norm(v2u (&xr)[5], LAS unsigned char* L, int lane) {
  v2f x[10];
#pragma unroll
  for (int i = 0; i < 5; ++i) { x[2 * i] = v2f{lo16(xr[i].x), hi16(xr[i].x)}; x[2 * i + 1] = v2f{lo16(xr[i].y), hi16(xr[i].y)}; }
  const float p0 = asf((unsigned)__builtin_amdgcn_readlane((int)asu(x[0].x), 0)), p1 = asf((unsigned)__builtin_amdgcn_readlane((int)asu(x[0].x), 32));
  const float piv = lane < 32 ? p0 : p1;
  v2f s1 = {0.f, 0.f}, s2 = {0.f, 0.f};
#pragma unroll
  for (int i = 0; i < 10; ++i) { const v2f d = x[i] - piv; s1 += d; s2 += d * d; }
  float a = s1.x + s1.y, q = s2.x + s2.y;
  a += dpp<XOR1>(a); q += dpp<XOR1>(q); a += dpp<XOR2>(a); q += dpp<XOR2>(q);
  a += dpp<ROR4>(a); q += dpp<ROR4>(q); a += dpp<ROR8>(a); q += dpp<ROR8>(q);
  a = fold16(a, a) * (1.0f / DM); q = fold16(q, q) * (1.0f / DM);
  const float mb = bfr(piv + a), var = bfr(fmaf(-a, a, q));
  const float den = bfr(__builtin_amdgcn_sqrtf(bfr(var + 1e-5f)));
  const float rcp = __builtin_amdgcn_rcpf(den);
#pragma unroll
  for (int i = 0; i < 5; ++i) {
    unsigned o[2];
#pragma unroll
    for (int e = 0; e < 2; ++e) {
      const v2f t = x[2 * i + e] - mb;
      const v2f d = {bfr(t.x), bfr(t.y)}, q0 = d * rcp;
      const v2f qq = (d - q0 * den) * rcp + q0;
      o[e] = bfbits(qq.x) | (bfbits(qq.y) << 16);
    }
    *(LAS v2u*)(L + L_XP + (lane >> 5) * 1280 + ((lane & 31) + 32 * i) * 8) = v2u{o[0], o[1]};
  }
}

template <int NK, int KSTEP, int HALF>
__device__ __forceinline__ void load_bt(const LAS unsigned char* b, v4u (&f)[NK]) {
#pragma unroll
  for (int ks = 0; ks < NK; ++ks) {
    const v2u lo = __builtin_bit_cast(v2u, __builtin_amdgcn_ds_read_tr16_b64_v4i16((LAS v4s*)(b + ks * KSTEP)));
    const v2u hi = __builtin_bit_cast(v2u, __builtin_amdgcn_ds_read_tr16_b64_v4i16((LAS v4s*)(b + ks * KSTEP + HALF)));
    f[ks] = v4u{lo.x, lo.y, hi.x, hi.y};
  }
}
template <int NK> __device__ __forceinline__ v4f mm2(const LAS unsigned char* a, const v4u (&f)[NK], v4f c = v4f{0.f, 0.f, 0.f, 0.f}) {
  v4u av[NK];
#pragma unroll
  for (int ks = 0; ks < NK; ++ks) av[ks] = *(const LAS v4u*)(a + ks * 64);
#pragma unroll
  for (int ks = 0; ks < NK; ++ks) c = __builtin_amdgcn_mfma_f32_16x16x32_bf16(__builtin_bit_cast(bf16x8, av[ks]), __builtin_bit_cast(bf16x8, f[ks]), c, 0, 0, 0);
  return c;
}

#define DMA(OFF) "s_waitcnt vmcnt(%4)\n\tbuffer_load_dwordx4 %0, %1, %2 offen " OFF "nt lds"
template <int N> __device__ __forceinline__ void dma3(int voff, v4u srd, int soff, unsigned lds) {
  asm volatile("s_mov_b32 m0, %3\n\t" DMA("") "\n\t" DMA("offset:1024 ") "\n\t" DMA("offset:2048 ")
               :: "v"(voff), "s"(srd), "s"(soff), "s"(lds), "i"(N) : "memory");
}
template <int N> __device__ __forceinline__ void dma1(int voff, v4u srd, int soff, unsigned lds) {
  asm volatile("s_mov_b32 m0, %3\n\t" DMA("") :: "v"(voff), "s"(srd), "s"(soff), "s"(lds), "i"(N) : "memory");
}
__device__ __forceinline__ v4u srd_of(const void* ptr, unsigned bytes) {
  const u64 a = (u64)ptr;
  return v4u{(unsigned)a, (unsigned)(a >> 32) & 0xffffu, bytes, 0x00020000u};
}
constexpr int fdiv6(int a) { return a >= 0 ? a / 6 : -((5 - a) / 6); }
constexpr int lag3(int w) { return fdiv6(3 - w); }
constexpr int lag6(int w) { return fdiv6(6 - w); }

constexpr int R0 = 9, NPC = 64 - R0, A1 = K_A1, C1 = K_C1, B1 = 32 * R0 - 6 * A1 - C1, A2 = K_A2, B2 = 16 * R0 - 3 * A2;
static_assert(B1 >= A1 && C1 >= 0 && B2 >= A2, "members without a prologue take more blocks");
struct Plan {
  bool mlp;
  int npro, n1;
  __device__ __forceinline__ Plan(int h, int m) {
    mlp = m == 7 && h < NMLP;
    npro = m < 6 ? NS_WQ : mlp ? NS_MLP : 0;
    n1 = h < NMLP ? (m < 6 ? A1 : m == 6 ? B1 : C1) : (m < 6 ? A2 : B2);
  }
  __device__ __forceinline__ static int block(int i, int h, int m) {
    constexpr int Q1 = K_Q4 ? A1 / 4 * 4 : 0, Q2 = K_Q4 ? A2 / 4 * 4 : 0;
    if (h < NMLP) return m == 7 ? 6 * A1 + B1 + i : i < Q1 ? 7 * (i & ~3) + 4 * m + (i & 3) : i < A1 ? 7 * i + m : 6 * A1 + i;
    return i < Q2 ? 8 * (i & ~3) + 4 * m + (i & 3) : i < A2 ? 8 * i + m : 6 * A2 + 2 * i + m - 6;
  }
  __device__ __forceinline__ int ntot() const { return npro + n1 + 4 * NPC; }
  __device__ __forceinline__ int wo_skip() const { const int r = ntot() % NSLOT; return r + NWO > NSLOT ? NSLOT - r : 0; }
};
__device__ __forceinline__ int round_off(int rho, int h) { return ((rho >> 5) * NH + h) * SLAB + ((rho * K_STR + h * K_HROT) & 31) * 98304; }

struct Loader {
  LAS unsigned* ctl;
  unsigned lbase;
  int s, pos, limit, pub;
  unsigned next_addr;
  u64 stall = 0;
  __device__ __forceinline__ void publish(int n) { pub = max(pub, n); __hip_atomic_store(ctl + C_LANDED, (unsigned)pub, RLX_WG); }
  __device__ __forceinline__ void credit(int last) {
    if (__builtin_expect(last >= limit, 0)) {
      stall -= __builtin_amdgcn_s_memrealtime();
#pragma nounroll
      for (unsigned n = 0; n < LDS_SPIN; ++n) {
        int v = (int)__hip_atomic_load((LAS unsigned*)(size_t)next_addr, RLX_WG);
        v = min(v, dppi<XOR1>(v)); v = min(v, dppi<XOR2>(v)); v = min(v, dppi<MIR8>(v));
        limit = rfl(v) + NSLOT;
        if (last < limit) break;
        if (n == 8) { asm volatile("s_waitcnt vmcnt(0)" ::: "memory"); publish(s); }
        __builtin_amdgcn_s_sleep(1);
      }
      stall += __builtin_amdgcn_s_memrealtime();
      CBAR();
    }
  }
  __device__ __forceinline__ unsigned dst() const { return lbase + pos * SLOTB; }
  __device__ __forceinline__ void advance() { ++s; pos = pos + 1 == NSLOT ? 0 : pos + 1; }
  template <int W> __device__ __forceinline__ void kv(int i0, int i1, int h, int m, int voff, v4u rk, v4u rv) {
#pragma nounroll
    for (int rho = i0; rho < i1; ++rho) {
      credit(s + 3);
      const int so = round_off(rho, h) + ((m + rho * K_ROT) & 7) * 12288;
      int v;
#pragma unroll
      for (int u = 0; u < 4; ++u) {
        if (u == 2) asm volatile("ds_read_b32 %0, %1" : "=v"(v) : "v"(next_addr) : "memory");
        dma3<W - 1>(voff, rk, so + u * 3072, dst());
        if (u == 2) { asm volatile("s_waitcnt lgkmcnt(0)" : "+v"(v)); v = min(v, dppi<XOR1>(v)); v = min(v, dppi<XOR2>(v)); asm volatile("" : "+v"(v)); }
        if (u == 3) limit = rfl(min(v, dppi<MIR8>(v))) + NSLOT;
        if (lag3(W) == lag6(W)) publish(s + lag3(W));
        dma3<W - 1>(voff, rv, so + u * 3072, dst() + 3072);
        if (lag3(W) != lag6(W)) publish(s + lag6(W));
        advance();
      }
    }
  }
};

__device__ __forceinline__ void run_loader(const Params& p, LAS unsigned char* L, int lane, u64 tentry) {
  LAS unsigned* ctl = (LAS unsigned*)(L + L_CTL);
  const int bid = blockIdx.x, h = K_XMAP ? bid >> 3 : bid & 31, m = K_XMAP ? bid & 7 : bid >> 5;
  const int r = lane >> 3, j = lane & 7;
  __builtin_amdgcn_s_setprio(3);
  STAMPS();
  Loader ld{ctl, (unsigned)(size_t)L, 0, 0, NSLOT, 0, (unsigned)(size_t)(ctl + C_NEXT + (j < NC ? j : 0))};
  const int voff = lane * 16;
  if (__builtin_expect(m < 6, 1)) {
    const v4u rq = srd_of(p.wq, DM * NH * DH * 2);
    const int vo = r * (NH * DH * 2) + (j ^ (r & 2)) * 16, so = (m / 3) * 320 * (NH * DH * 2) + h * (DH * 2) + (m % 3) * 128;
#pragma unroll 2
    for (int g = 0; g < 6 * NS_WQ; ++g) dma1<63>(g & 1 ? vo ^ 64 : vo, rq, so + min(g, 39) * (8 * NH * DH * 2), ld.lbase + g * 1024);
    ld.s = ld.pos = NS_WQ;
  }
  STAMP(1);
  const Plan pl(h, m);
  if (pl.mlp) {
    const v4u rg = srd_of(p.wg, DM * FF * 2), ru = srd_of(p.wu, DM * FF * 2), rd = srd_of(p.wd, FF * DM * 2);
    const int vo = r * (FF * 2) + (j ^ (r & 2)) * 16;
    constexpr int W = K_WMLP;
    const int sd = h * 64 * (DM * 2);
    auto slot = [&](int pc0, int sg) {
#pragma unroll
      for (int q = 0; q < 6; ++q) {
        const int pc = pc0 + q;
        if (pc < 16 || sg >= 0) dma1<W - 1>(q & 1 ? vo ^ 64 : vo, pc & 4 ? ru : rg, (sg < 0 ? 72 : sg) * (8 * FF * 2) + ((pc >> 3) * 4 + (pc & 3)) * (8 * FF * 2) + h * 128, ld.dst() + q * 1024);
        else dma1<W - 1>(voff, rd, sd + (pc - 16) * 1024, ld.dst() + q * 1024);
      }
      ld.publish(ld.s + lag6(W));
      ld.advance();
    };
#pragma nounroll
    for (int t = 0; t < 6; ++t) {
#pragma unroll
      for (int u = 0; u < 4; ++u) slot(6 * u, 12 * t);
    }
#pragma unroll
    for (int u = 0; u < 3; ++u) { ld.credit(ld.s); slot(6 * u, -1); }
#pragma nounroll
    for (int t = 27; t < NS_MLP; ++t) {
      ld.credit(ld.s);
      dma3<W - 1>(voff, rd, sd + (6 * t - 160) * 1024, ld.dst());
      dma3<W - 1>(voff, rd, sd + (6 * t - 157) * 1024, ld.dst() + 3072);
      ld.publish(ld.s + lag6(W));
      ld.advance();
    }
  }
  STAMP(2);
  const v4u rk = srd_of(p.k, KV_BYTES), rv = srd_of(p.v, KV_BYTES);
#pragma nounroll
  for (int i = 0; i < pl.n1; ++i) {
    ld.credit(ld.s);
    const int n = Plan::block(i, h, m);
    const int so = round_off(n >> 5, h) + (n & 31) * 3072;
    dma3<INF - 1>(voff, rk, so, ld.dst());
    dma3<INF - 1>(voff, rv, so, ld.dst() + 3072);
    ld.publish(ld.s + lag6(INF));
    ld.advance();
  }
  ld.kv<INF>(R0, 64, h, m, voff, rk, rv);
  STAMP(3);
  const v4u ro = srd_of(p.wo, NH * DH * DM * 2);
  int row = lane / 10, ch = lane - row * 10;
  ld.credit(ld.s + pl.wo_skip() + NWO - 1);
  if (pl.wo_skip()) ld.pos = 0;
#pragma nounroll
  for (int t = 0; t < NWO; ++t) {
#pragma unroll
    for (int q = 0; q < 6; ++q) {
      dma1<INF - 1>(row * (DM * 2) + ch * 16, ro, h * DH * (DM * 2) + m * 160, ld.dst() + q * 1024);
      if (q == 2 && lag3(INF) == lag6(INF)) ld.publish(ld.s + lag3(INF));
      if (q == 5 && lag3(INF) != lag6(INF)) ld.publish(ld.s + lag6(INF));
      row += 6; ch += 4;
      if (ch >= 10) { ch -= 10; row += 1; }
    }
    ld.advance();
  }
  asm volatile("s_waitcnt vmcnt(0)" ::: "memory");
  ld.publish(ld.s);
  STAMP(4);
  st.t[0] = tentry; st.t[5] = tentry + ld.stall;
  FLUSH(0, 6);
}

struct Early { v2u xr[5], wr[5]; unsigned e0, xo; };
__device__ __forceinline__ void early_loads(const Params& p, Early& e, int lane) {
#pragma unroll
  for (int i = 0; i < 5; ++i) e.xr[i] = ((const GAS v2u*)p.x)[(lane >> 5) * 160 + (lane & 31) + 32 * i];
  e.e0 = __hip_atomic_load((GAS unsigned*)(p.ws + W_EPOCH), RLX_AGENT);
}
__device__ __forceinline__ void run_helper(const Params& p, LAS unsigned char* L, int lane, Early& e) {
  LAS unsigned* ctl = (LAS unsigned*)(L + L_CTL);
  const int bid = blockIdx.x, h = K_XMAP ? bid >> 3 : bid & 31, m = K_XMAP ? bid & 7 : bid >> 5;
  const int j = lane & 7;
  const __amdgpu_buffer_rsrc_t rw = rsrc(p.ws, W_END);
  STAMPS();
  const int fb = lane >> 5, col = min(bid, NFIN - 1) * 8 + 2 * (lane & 3), l48 = min(lane, 47);
#pragma unroll
  for (int i = 0; i < 5; ++i) e.wr[i] = ((const GAS v2u*)p.wr)[(lane & 31) + 32 * i];
  e.xo = ((const GAS unsigned*)p.x)[(fb * DM + col) >> 1];
  __builtin_amdgcn_sched_barrier(0);
  layer_norm(e.xr, L, lane);
  const unsigned epoch = (unsigned)rfl((int)e.e0) + 1u, tag = tag_of(epoch);
  ctl[C_TAG] = tag;
  lds_set(ctl + C_XOK, 1u);
  STAMP(0);
  if (K_ABL & 1) {
    lds_wait<2>(ctl + C_LANDED, K_QWAIT);
    const int off = W_QP + (h * 2 * DH + 4 * l48) * 8;
    v4u g[4];
#pragma nounroll
    for (unsigned n = 0; n < GLB_SPIN; ++n) {
      CBAR();
#pragma unroll
      for (int i = 0; i < 4; ++i) g[i] = ld16(rw, off + (i & 1) * 16 + (i >> 1) * (DH * 8));
      if (__all(fresh(g[0], tag) && fresh(g[1], tag) && fresh(g[2], tag) && fresh(g[3], tag))) break;
    }
#pragma unroll
    for (int b = 0; b < 2; ++b)
      *(LAS v2u*)(L + L_QB + b * (DH * 2) + l48 * 8) = v2u{bfbits(val(g[0][b]) + val(g[2][b])) | (bfbits(val(g[0][2 + b]) + val(g[2][2 + b])) << 16),
                                                            bfbits(val(g[1][b]) + val(g[3][b])) | (bfbits(val(g[1][2 + b]) + val(g[3][2 + b])) << 16)};
    lds_set(ctl + C_Q, 1u);
  }
  float rs = 0.f;
#pragma unroll
  for (int i = 0; i < 5; ++i) {
    const v2u xx = *(const LAS v2u*)(L + L_XP + fb * 1280 + ((lane & 31) + 32 * i) * 8);
    rs = dot2(e.wr[i].y, xx.y, dot2(e.wr[i].x, xx.x, rs));
  }
  rs += dpp<XOR1>(rs); rs += dpp<XOR2>(rs); rs += dpp<ROR4>(rs); rs += dpp<ROR8>(rs);
  const float sg = bfr(1.0f / (1.0f + expf(-bfr(fold16(rs, rs)))));
  const unsigned xn = *(const LAS unsigned*)(L + L_XP + fb * 1280 + col * 2);
  float g0 = 0.f, g1 = 0.f;
#pragma nounroll
  for (int b = 0; b < 2; ++b) {
    if (b == 0) lds_wait_all<NC, 16>(ctl + C_R0, j); else lds_wait_all<NC, 0>(ctl + C_R1, j);
    STAMP(1);
    const LAS float* rec = (const LAS float*)(L + L_SCR) + b * NC * 200;
    v2f ml[NC];
    v4f ov[NC];
#pragma unroll
    for (int c = 0; c < NC; ++c) { ml[c] = *(const LAS v2f*)(rec + c * 200 + 192); ov[c] = *(const LAS v4f*)(rec + c * 200 + 4 * l48); }
    float M = ml[0].x;
#pragma unroll
    for (int c = 1; c < NC; ++c) M = fmaxf(M, ml[c].x);
    float Ls = 0.f;
    v4f o = {0.f, 0.f, 0.f, 0.f};
#pragma unroll
    for (int c = 0; c < NC; ++c) { const float w = exp2f_(ml[c].x - M); Ls = fmaf(w, ml[c].y, Ls); o += w * ov[c]; }
    const int bh8 = (b * NH + h) * 8;
    if (K_ABL & (b ? 8 : 2)) {
    if (lane < 48) st16(rw, W_OP + (bh8 + m) * 768 + lane * 16, pack4(o.x, o.y, o.z, o.w, tag));
    if (lane == 0) st16(rw, W_ML + (bh8 + m) * 16, v4u{asu(M), epoch, asu(Ls), epoch});
    }
    STAMP(2);
    if (!(K_ABL & (b ? 16 : 4))) continue;
    v4u g[8], mv;
    if (b == 0) lds_wait<32>(ctl + C_LANDED, Plan(h, m).ntot() - K_G0);
    unsigned want = 0xffu;
#pragma unroll
    for (int k = 0; k < 8; ++k) g[k] = v4u{~tag, 0u, 0u, 0u};
#pragma nounroll
    for (unsigned n = 0; n < GLB_SPIN; ++n) {
      CBAR();
      mv = ld16(rw, W_ML + (bh8 + j) * 16);
#pragma unroll
      for (int k = 0; k < 8; ++k)
        if (want >> k & 1) g[k] = ld16(rw, W_OP + (bh8 + k) * 768 + l48 * 16);
      const bool mok = mv.y == epoch && mv.w == epoch;
      bool ok[8], all = mok;
#pragma unroll
      for (int k = 0; k < 8; ++k) { ok[k] = fresh(g[k], tag); all &= ok[k]; }
      if (__all(all)) break;
      const unsigned seen = (unsigned)__builtin_amdgcn_ballot_w64(mok) & 0xffu;
      unsigned miss = 0u;
#pragma unroll
      for (int k = 0; k < 8; ++k) miss |= __all(ok[k]) ? 0u : 1u << k;
      want = __builtin_popcount(miss) <= K_BLIND ? miss : miss & seen;
      if (want == 0u) __builtin_amdgcn_s_sleep(K_SL1);
    }
    STAMP(3);
    float Mg = asf(mv.x);
    Mg = fmaxf(Mg, dpp<XOR1>(Mg)); Mg = fmaxf(Mg, dpp<XOR2>(Mg)); Mg = fmaxf(Mg, dpp<MIR8>(Mg));
    const float w = exp2f_(asf(mv.x) - Mg);
    float Lg = w * asf(mv.z);
    Lg += dpp<XOR1>(Lg); Lg += dpp<XOR2>(Lg); Lg += dpp<MIR8>(Lg);
    const float wn = w * __builtin_amdgcn_rcpf(Lg);
    o = v4f{0.f, 0.f, 0.f, 0.f};
#pragma unroll
    for (int k = 0; k < 8; ++k) o += asf((unsigned)__builtin_amdgcn_readlane((int)asu(wn), k)) * v4f{val(g[k].x), val(g[k].y), val(g[k].z), val(g[k].w)};
    *(LAS v2u*)(L + L_OA + b * (DH * 2) + l48 * 8) = v2u{bfbits(o.x) | (bfbits(o.y) << 16), bfbits(o.z) | (bfbits(o.w) << 16)};
    lds_set(ctl + C_O, b + 1);
    STAMP(4);
    if (bid >= NFIN || (b && !(K_ABL & 64))) continue;
    if (b) lds_wait_all<5, 0>(ctl + C_T3, j);
    STAMP(5);
    const int off = (b ? W_SP : W_YP) + (lane >> 2) * (DM * 8) + bid * 64 + (lane & 3) * 16;
    const bool two = b || lane < 4 * (NMLP - 16);
    v4u a, c = {0u, 0u, 0u, 0u};
    bool fa = false, fc = !two;
#pragma nounroll
    for (unsigned n = 0; n < GLB_SPIN; ++n) {
      CBAR();
      if (!fa) a = ld16(rw, off);
      if (!fc) c = ld16(rw, off + 16 * (DM * 8));
      fa = fresh(a, tag); fc = !two || fresh(c, tag);
      const int left = __builtin_popcountll(__builtin_amdgcn_ballot_w64(!fa)) + __builtin_popcountll(__builtin_amdgcn_ballot_w64(!fc));
      if (left == 0) break;
      if (left > 4 * K_LEFT) __builtin_amdgcn_s_sleep(K_SL2);
    }
    STAMP(6);
    float s0 = fold32(val(a.x) + val(c.x), val(a.y) + val(c.y)), s1 = fold32(val(a.z) + val(c.z), val(a.w) + val(c.w));
    s0 = fold16(s0, s0); s1 = fold16(s1, s1);
    s0 += dpp<ROR8>(s0); s1 += dpp<ROR8>(s1);
    s0 = bfr(s0 + dpp<ROR4>(s0)); s1 = bfr(s1 + dpp<ROR4>(s1));
    if (b == 0) {
      g0 = bfr(siluf(bfr(lo16(xn) + bfr(s0 * sg))));
      g1 = bfr(siluf(bfr(hi16(xn) + bfr(s1 * sg))));
    } else {
      if ((lane & 31) < 4) ((GAS unsigned*)p.out)[(fb * DM + col) >> 1] = bfbits(lo16(e.xo) + bfr(s0 * g0)) | (bfbits(hi16(e.xo) + bfr(s1 * g1)) << 16);
      if (bid == 0 && lane == 0) __hip_atomic_store((GAS unsigned*)(p.ws + W_EPOCH), epoch, RLX_AGENT);
      STAMP(7);
    }
  }
  FLUSH(8, bid < NFIN ? 8 : 5);
}

__device__ __forceinline__ void run_consumer(const Params& p, LAS unsigned char* L, int lane, int c) {
  LAS unsigned* ctl = (LAS unsigned*)(L + L_CTL);
  const int bid = blockIdx.x, h = K_XMAP ? bid >> 3 : bid & 31, m = K_XMAP ? bid & 7 : bid >> 5;
  const int r = lane >> 3, j = lane & 7;
  const Plan pl(h, m);
  const __amdgpu_buffer_rsrc_t rw = rsrc(p.ws, W_END);
  STAMPS();
  if (pl.npro == 0 || (m < 6 && c >= 4)) lds_set(ctl + C_NEXT + c, pl.npro + c);
  lds_wait<1>(ctl + C_XOK, 1);
  const unsigned tag = (unsigned)rfl((int)ctl[C_TAG]);
  const int bq = (lane >> 4) * 1024 + ((lane & 15) >> 2) * 128 + ((2 * c + ((lane & 3) >> 1)) ^ (2 * (r & 3))) * 16 + (lane & 1) * 8;
  if (m < 6) {
    if (c < 4) {
      lds_wait<0>(ctl + C_LANDED, NS_WQ);
      v4u bf[10];
      load_bt<10, 4096, 512>(L + bq, bf);
      lds_set(ctl + C_NEXT + c, NS_WQ + c);
      const v4f qv = mm2<10>(L + L_XP + (lane & 1) * 1280 + (m / 3) * 640 + (lane >> 4) * 16, bf);
      if (lane < 16) st8(rw, W_QP + ((h * 2 + m / 3) * DH + (m % 3) * 64 + 16 * c + lane) * 8, pack2(qv.x, qv.y, tag));
    }
  } else if (pl.mlp) {
    const int g = lane >> 4;
    if (c < 4) {
      const LAS unsigned char* ap = L + L_XP + (lane & 1) * 1280 + g * 16;
      v4f ga = {0.f, 0.f, 0.f, 0.f}, ua = {0.f, 0.f, 0.f, 0.f};
#pragma nounroll
      for (int t = 0; t < 20; t += 2) {
        lds_wait<1>(ctl + C_LANDED, (8 * t + 21) / 6);
        v4u gf[2], uf[2];
        load_bt<2, 8192, 512>(L + (t < 18 ? t : t - 18) * 8192 + bq, gf);
        load_bt<2, 8192, 512>(L + (t < 18 ? t : t - 18) * 8192 + 4096 + bq, uf);
        lds_set(ctl + C_NEXT + c, (8 * t + 16) / 6);
        ga = mm2<2>(ap + t * 64, gf, ga);
        ua = mm2<2>(ap + t * 64, uf, ua);
      }
      if (lane < 32) *(LAS unsigned short*)(L + L_HM + g * 128 + (16 * c + (lane & 15)) * 2) = (unsigned short)bfbits(bfr(siluf(bfr(g ? ga[1] : ga[0]))) * bfr(g ? ua[1] : ua[0]));
      lds_set(ctl + C_HM + c, 1u);
    } else {
      lds_set(ctl + C_NEXT + c, 160 / 6);
    }
    lds_wait_all<4, 1>(ctl + C_HM, j);
    lds_wait<1>(ctl + C_LANDED, NS_MLP);
#pragma nounroll
    for (int n = c; n < DM / 16; n += NC) {
      v4u bf[2];
      load_bt<2, 32 * 1280, 4 * 1280>(L + (160 * 1024 - RINGB) + (8 * g + ((lane & 15) >> 2)) * 1280 + n * 32 + (lane & 3) * 8, bf);
      const v4f y = mm2<2>(L + L_HM + (lane & 1) * 128 + g * 16, bf);
      if (lane < 16) st8(rw, W_YP + (h * DM + n * 16 + lane) * 8, pack2(y[0], y[1], tag));
    }
    lds_set(ctl + C_NEXT + c, NS_MLP + c);
  }
  STAMP(0);
  lds_wait<1>(ctl + C_Q, 1);
  STAMP(1);

  const int ntot = pl.ntot();
  int s = pl.npro + c, pos = s % NSLOT;
  const int koff = r * 384 + j * 16;
#pragma nounroll
  for (int b = 0; b < 2; ++b) {
    v4u q[3];
#pragma unroll
    for (int u = 0; u < 3; ++u) q[u] = *(const LAS v4u*)(L + L_QB + (b * DH + (j + 8 * u) * 8) * 2);
    float mx = -3.0e38f, ls = 0.f, acc[24];
#pragma unroll
    for (int i = 0; i < 24; ++i) acc[i] = 0.f;
    const int send = b ? ntot : ntot - 128;
#pragma nounroll
    for (; s < send; s += NC) {
      lds_wait<1>(ctl + C_LANDED, s + 1);
      const LAS unsigned char* sp = L + pos * SLOTB + koff;
      v4u kk[3], vv[3];
#pragma unroll
      for (int u = 0; u < 3; ++u) kk[u] = *(const LAS v4u*)(sp + u * 128);
#pragma unroll
      for (int u = 0; u < 3; ++u) vv[u] = *(const LAS v4u*)(sp + 3072 + u * 128);
      lds_set(ctl + C_NEXT + c, s + NC);
      float d0 = 0.f, d1 = 0.f, d2 = 0.f;
#pragma unroll
      for (int e = 0; e < 4; ++e) { d0 = dot2(kk[0][e], q[0][e], d0); d1 = dot2(kk[1][e], q[1][e], d1); d2 = dot2(kk[2][e], q[2][e], d2); }
      float d = d0 + d1 + d2;
      d += dpp<XOR1>(d); d += dpp<XOR2>(d); d += dpp<MIR8>(d);
      const float t = bfr(bfr(d) * 0.07216878364870322f) * LOG2E;
      const float mn = fmaxf(mx, t);
      const float al = exp2f_(mx - mn), pr = exp2f_(t - mn);
      mx = mn;
      ls = fmaf(ls, al, pr);
#pragma unroll
      for (int u = 0; u < 3; ++u)
#pragma unroll
        for (int e = 0; e < 4; ++e) {
          const int k = 8 * u + 2 * e;
          acc[k] = fmaf(pr, lo16(vv[u][e]), acc[k] * al);
          acc[k + 1] = fmaf(pr, hi16(vv[u][e]), acc[k + 1] * al);
        }
      pos += NC;
      if (pos >= NSLOT) pos -= NSLOT;
    }
    if (b) STAMP(3); else STAMP(2);
    const float M = max_rows(fmaxf(mx, dpp<ROR8>(mx)));
    const float sc = exp2f_(mx - M);
    ls *= sc;
    ls += dpp<ROR8>(ls);
    ls = sum_rows(ls);
#pragma unroll
    for (int i = 0; i < 24; ++i) acc[i] *= sc;
#pragma unroll
    for (int i = 0; i < 12; ++i) acc[i] = fold32(acc[i], acc[i + 12]);
#pragma unroll
    for (int i = 0; i < 6; ++i) { acc[i] = fold16(acc[i], acc[i + 6]); acc[i] += dpp<ROR8>(acc[i]); }
    LAS float* rec = (LAS float*)(L + L_SCR) + (b * NC + c) * 200;
    if ((r & 1) == 0) {
#pragma unroll
      for (int u = 0; u < 3; ++u) {
        const int v = 6 * (lane >> 4) + 2 * u;
        *(LAS v2f*)(rec + (j + 8 * (v >> 3)) * 8 + (v & 7)) = v2f{acc[2 * u], acc[2 * u + 1]};
      }
    }
    if (lane == 0) *(LAS v2f*)(rec + 192) = v2f{M, ls};
    lds_set(ctl + C_R0 + 8 * b + c, 1u);
  }
  STAMP(4);
  if (c == 5) return;
  if (!(K_ABL & 32)) return;
  lds_wait<0>(ctl + C_LANDED, ntot + NWO);
  v4u bf[6];
  load_bt<6, 32 * 160, 4 * 160>(L + (ntot + pl.wo_skip()) % NSLOT * SLOTB + ((lane >> 4) * 8 + ((lane & 15) >> 2)) * 160 + c * 32 + (lane & 3) * 8, bf);
  STAMP(5);
  lds_wait<0>(ctl + C_O, 2);
  STAMP(6);
  const v4f sv = mm2<6>(L + L_OA + (lane & 1) * (DH * 2) + (lane >> 4) * 16, bf);
  if (lane < 16) st8(rw, W_SP + (h * DM + m * 80 + c * 16 + lane) * 8, pack2(sv.x, sv.y, tag));
  lds_set(ctl + C_T3 + c, 1u);
  STAMP(7);
  if (c == 0) FLUSH(16, 8);
  if (c == 4) FLUSH(24, 8);
}

__global__ void __launch_bounds__(NTHR, 2) fused_decode(Params p) {
  extern __shared__ __attribute__((aligned(16))) unsigned char smem[];
  u64 tentry = 0;
  asm volatile("s_memrealtime %0" : "=s"(tentry));
  __builtin_amdgcn_sched_barrier(0);
  LAS unsigned char* L = (LAS unsigned char*)smem;
  const int tid = threadIdx.x, lane = tid & 63, wave = rfl(tid >> 6);
  if (tid < 80) ((LAS unsigned*)(L + L_CTL))[tid] = 0u;
  Early e;
  if (wave == NC + 1) early_loads(p, e, lane);
  asm volatile("s_waitcnt lgkmcnt(0)\n\ts_barrier" ::: "memory");
  int role = wave;
  asm volatile("" : "+s"(role));
  if (role > 0) {
    if (wave != NC + 1) run_consumer(p, L, lane, wave - 1);
    else run_helper(p, L, lane, e);
  } else {
    run_loader(p, L, lane, tentry);
  }
}

}

namespace {
__global__ void k_rep(Params p) {
  const u64 now = __builtin_amdgcn_s_memrealtime();
  const GAS u64* st = (const GAS u64*)(p.ws + W_ST);
  const int l = threadIdx.x;
  u64 t0 = ~0ull, mx = 0, sum = 0;
  for (int i = l; i < 256; i += 64) { const u64 d = st[i * 32]; t0 = d < t0 ? d : t0; }
  for (int o = 32; o; o >>= 1) { const u64 b = __shfl_xor(t0, o); t0 = b < t0 ? b : t0; }
  for (int i = l; i < NWGS; i += 64) { const u64 d = st[i * 32 + IDX] - t0; mx = d > mx ? d : mx; sum += d; }
  for (int o = 32; o; o >>= 1) { const u64 b = __shfl_xor(mx, o); mx = b > mx ? b : mx; sum += __shfl_xor(sum, o); }
  u64 v = MODE == 0 ? mx : MODE == 1 ? sum / NWGS : now - t0;
  if (v > 20000) v = 20000;
  while (__builtin_amdgcn_s_memrealtime() - now < 1000 + SCALE * v) __builtin_amdgcn_s_sleep(8);
}
}
extern "C" void kernel_launch(void* const* d_in, const int* in_sizes, int n_in, void* d_out, int out_size, void* d_ws,
                              size_t ws_size, hipStream_t stream) {
  Params p;
  p.x = (const unsigned short*)d_in[0];
  p.k = (const unsigned short*)d_in[1];
  p.v = (const unsigned short*)d_in[2];
  p.wq = (const unsigned short*)d_in[3];
  p.wo = (const unsigned short*)d_in[4];
  p.wr = (const unsigned short*)d_in[6];
  p.wg = (const unsigned short*)d_in[7];
  p.wu = (const unsigned short*)d_in[8];
  p.wd = (const unsigned short*)d_in[9];
  p.out = (unsigned short*)d_out;
  p.ws = (unsigned char*)d_ws;
  (void)hipFuncSetAttribute((const void*)fused_decode, hipFuncAttributeMaxDynamicSharedMemorySize, L_END);
  fused_decode<<<NWG, NTHR, L_END, stream>>>(p);
  k_rep<<<1, 64, 0, stream>>>(p);
}
```

```cpp
#define PIDX 15
#define K_STAMPS 1
#define IDX 15
#define MODE 0
#define SCALE 0
#define NWGS 80
#include <hip/hip_runtime.h>
#include <hip/hip_bf16.h>

namespace {

#define GAS __attribute__((address_space(1)))
#define LAS __attribute__((address_space(3)))
#define RLX_AGENT __ATOMIC_RELAXED, __HIP_MEMORY_SCOPE_AGENT
#define RLX_WG __ATOMIC_RELAXED, __HIP_MEMORY_SCOPE_WORKGROUP
typedef unsigned v4u __attribute__((ext_vector_type(4)));
typedef unsigned v2u __attribute__((ext_vector_type(2)));
typedef float v4f __attribute__((ext_vector_type(4)));
typedef float v2f __attribute__((ext_vector_type(2)));
typedef short v4s __attribute__((ext_vector_type(4)));
typedef __bf16 bf16x2 __attribute__((ext_vector_type(2)));
typedef __bf16 bf16x8 __attribute__((ext_vector_type(8)));
typedef unsigned long long u64;

#define K_INF 21
#define K_NSLOT 24
#define K_A1 10
#define K_C1 1
#define K_WMLP 33
#define K_HROT 1
#define K_XMAP 0
#define K_STR 21
#define K_ROT 1
#define K_FEW 2
#define K_SL1 8
#define K_SL2 8
#define K_TEND 5370
#define K_LATE 40
#define K_WB 33
#define K_D1 12
#define K_D2 12
#define K_NCP 16
#define K_CP0 1075
#define K_CP 275
#define K_CPD 125
#define K_L0 60.f
#define K_GMIN 2.5f
#define K_STEP 200.f
#define K_KEEP 0
#define K_XW 2.f
#define K_XD 6.f
#define K_BASE 400.f
#define K_FLOOR 600.f
#define K_MIN 8.f
#define K_STALL 160
#define K_GAIN 1.3f
#define K_DEAD 50.f
#define K_R0 9
#define K_QWAIT 22

constexpr int DM = 640, NH = 32, DH = 192, SK = 8192, FF = 1536;
constexpr int NWG = 256, NC = 6, NTHR = (NC + 3) * 64;
constexpr int INF = K_INF;
constexpr int NSLOT = K_NSLOT;
constexpr int SLOTB = 6144, RINGB = NSLOT * SLOTB, NWO = 5, NS_WQ = 7, NS_MLP = 40;
constexpr unsigned KV_BYTES = 2u * NH * SK * DH * 2u;
constexpr int SLAB = SK * DH * 2;
constexpr int NMLP = FF / 64;
constexpr int NFIN = DM / 8;
static_assert(NSLOT == 24, "the MLP images rely on where the ring wraps");
static_assert(INF >= 7 && INF <= 30 && K_WB <= 60, "vmcnt is 6 bits; the last K/V slots are published while wo is requested");

constexpr int L_XP = NSLOT * SLOTB;
constexpr int L_REC = L_XP + 2560;
constexpr int L_QB = L_REC + 4608;
constexpr int L_OA = L_QB + 768;
constexpr int L_HM = L_OA + 768;
constexpr int L_P = L_HM + 256;
constexpr int L_MLW = L_P + 3072;
constexpr int L_F = L_MLW + 128;
constexpr int L_G = L_F + 64;
constexpr int L_CTL = L_G + 768;
constexpr int L_END = L_CTL + 320;
static_assert(L_END <= 160 * 1024 && L_END > 80 * 1024, "one workgroup per CU");
enum { C_NEXT = 0, C_HM = 16, C_ML = 24, C_R = 32, C_T3 = 48, C_O = 56, C_LANDED = 64, C_XOK, C_TAG, C_Q, C_F, C_RFREE, C_G, C_PUB, C_LATE, C_TEND };

constexpr int W_EPOCH = 0;
constexpr int W_ML = 1024;
constexpr int W_OP = W_ML + NH * 8 * 16;
constexpr int W_SP = W_OP + 2 * NH * 8 * 768;
constexpr int W_YP = W_SP + NH * DM * 8;
constexpr int W_QP = W_YP + NMLP * DM * 8;
constexpr int W_PC = W_QP + NH * 2 * DH * 8;
constexpr int W_END = W_PC + 24 * 512;
constexpr int W_ST = 4 << 20;

constexpr unsigned LDS_SPIN = 1u << 20, GLB_SPIN = 1u << 16;
constexpr float LOG2E = 1.4426950408889634f;

struct Params {
  const unsigned short *x, *k, *v, *wq, *wo, *wr, *wg, *wu, *wd;
  unsigned short* out;
  unsigned char* ws;
};

__device__ __forceinline__ int rfl(int v) { return __builtin_amdgcn_readfirstlane(v); }
__device__ __forceinline__ float bfr(float x) { return (float)(__bf16)x; }
__device__ __forceinline__ unsigned bfbits(float x) { return (unsigned)__builtin_bit_cast(unsigned short, (__bf16)x); }
__device__ __forceinline__ float lo16(unsigned u) { return __builtin_bit_cast(float, u << 16); }
__device__ __forceinline__ float hi16(unsigned u) { return __builtin_bit_cast(float, u & 0xffff0000u); }
__device__ __forceinline__ float asf(unsigned u) { return __builtin_bit_cast(float, u); }
__device__ __forceinline__ unsigned asu(float f) { return __builtin_bit_cast(unsigned, f); }
__device__ __forceinline__ float dot2(unsigned a, unsigned b, float c) {
  return __builtin_amdgcn_fdot2_f32_bf16(__builtin_bit_cast(bf16x2, a), __builtin_bit_cast(bf16x2, b), c, false);
}
template <int CTRL> __device__ __forceinline__ float dpp(float x) {
  return __builtin_bit_cast(float, __builtin_amdgcn_mov_dpp(__builtin_bit_cast(int, x), CTRL, 0xf, 0xf, true));
}
template <int CTRL> __device__ __forceinline__ int dppi(int x) { return __builtin_amdgcn_mov_dpp(x, CTRL, 0xf, 0xf, true); }
constexpr int XOR1 = 0xB1, XOR2 = 0x4E, MIR8 = 0x141, ROR4 = 0x124, ROR8 = 0x128;
__device__ __forceinline__ float fold32(float a, float b) {
  auto s = __builtin_amdgcn_permlane32_swap(asu(a), asu(b), false, false);
  return asf(s[0]) + asf(s[1]);
}
__device__ __forceinline__ float fold16(float a, float b) {
  auto s = __builtin_amdgcn_permlane16_swap(asu(a), asu(b), false, false);
  return asf(s[0]) + asf(s[1]);
}
__device__ __forceinline__ float max_rows(float x) {
  auto s = __builtin_amdgcn_permlane16_swap(asu(x), asu(x), false, false);
  x = fmaxf(asf(s[0]), asf(s[1]));
  auto t = __builtin_amdgcn_permlane32_swap(asu(x), asu(x), false, false);
  return fmaxf(asf(t[0]), asf(t[1]));
}
__device__ __forceinline__ float sum_rows(float x) { x = fold16(x, x); return fold32(x, x); }
__device__ __forceinline__ float wave_sum(float x) {
  x += dpp<XOR1>(x); x += dpp<XOR2>(x); x += dpp<ROR4>(x); x += dpp<ROR8>(x);
  return sum_rows(x);
}
__device__ __forceinline__ float exp2f_(float x) { return __builtin_amdgcn_exp2f(x); }
__device__ __forceinline__ float siluf(float x) { return x / (1.0f + expf(-x)); }
#define CBAR() asm volatile("" ::: "memory")

__device__ __forceinline__ __amdgpu_buffer_rsrc_t rsrc(const void* p, unsigned bytes) {
  return __builtin_amdgcn_make_buffer_rsrc((void*)p, 0, bytes, 0x00020000);
}
__device__ __forceinline__ void lds_set(LAS unsigned* w, unsigned v) {
  CBAR();
  __hip_atomic_store(w, v, RLX_WG);
  CBAR();
}
template <int SLEEP> __device__ __forceinline__ void lds_wait(LAS unsigned* w, unsigned want) {
#pragma nounroll
  for (unsigned n = 0; n < LDS_SPIN; ++n) {
    if ((unsigned)rfl((int)__hip_atomic_load(w, RLX_WG)) >= want) break;
    if (SLEEP) __builtin_amdgcn_s_sleep(SLEEP);
  }
  CBAR();
}
template <int N, int SLEEP> __device__ __forceinline__ void lds_wait_all(LAS unsigned* w, int j, unsigned want = 1u) {
  LAS unsigned* a = w + (j < N ? j : 0);
#pragma nounroll
  for (unsigned n = 0; n < LDS_SPIN; ++n) {
    if (__all(__hip_atomic_load(a, RLX_WG) >= want)) break;
    if (SLEEP) __builtin_amdgcn_s_sleep(SLEEP);
  }
  CBAR();
}
__device__ __forceinline__ unsigned tag_of(unsigned e) { return ((e & 0xffu) | (((e >> 8) ^ e) & 0xffu) << 8) * 0x10001u; }
__device__ __forceinline__ v2u pack2(float a, float b, unsigned tag) {
  return v2u{__builtin_amdgcn_perm(asu(a), tag, 0x07060500u), __builtin_amdgcn_perm(asu(b), tag, 0x07060501u)};
}
__device__ __forceinline__ v4u pack4(float a, float b, float c, float d, unsigned tag) {
  const v2u x = pack2(a, b, tag), y = pack2(c, d, tag);
  return v4u{x.x, x.y, y.x, y.y};
}
__device__ __forceinline__ bool fresh(v4u g, unsigned tag) {
  return (__builtin_amdgcn_perm(g.y, g.x, 0x0c0c0400u) | __builtin_amdgcn_perm(g.w, g.z, 0x04000c0cu)) == tag;
}
__device__ __forceinline__ float val(unsigned w) { return asf(w & 0xffffff00u); }
__device__ __forceinline__ v4u ld16(__amdgpu_buffer_rsrc_t rw, int off) { return __builtin_amdgcn_raw_buffer_load_b128(rw, off, 0, 16); }
__device__ __forceinline__ void st16(__amdgpu_buffer_rsrc_t rw, int off, v4u v) { __builtin_amdgcn_raw_buffer_store_b128(v, rw, off, 0, 16); }
__device__ __forceinline__ void st8(__amdgpu_buffer_rsrc_t rw, int off, v2u v) { __builtin_amdgcn_raw_buffer_store_b64(v, rw, off, 0, 16); }

struct Stamps {
  u64 t[8];
  __device__ __forceinline__ void flush(const Params& p, int base, int n) {
#pragma unroll
    for (int i = 0; i < 8; ++i) if (i < n) ((GAS u64*)(p.ws + W_ST))[blockIdx.x * 32 + base + i] = t[i];
  }
};
#define STAMP(i) do { __builtin_amdgcn_sched_barrier(0); asm volatile("s_memrealtime %0\n\ts_waitcnt lgkmcnt(0)" : "=s"(st.t[i]) :: "memory"); __builtin_amdgcn_sched_barrier(0); } while (0)
#define STAMPS() Stamps st = {}
#define FLUSH(base, n) st.flush(p, base, n)

__device__ __forceinline__ void layer_norm(v2u (&xr)[5], LAS unsigned char* L, int lane) {
  v2f x[10];
#pragma unroll
  for (int i = 0; i < 5; ++i) { x[2 * i] = v2f{lo16(xr[i].x), hi16(xr[i].x)}; x[2 * i + 1] = v2f{lo16(xr[i].y), hi16(xr[i].y)}; }
  const float p0 = asf((unsigned)__builtin_amdgcn_readlane((int)asu(x[0].x), 0)), p1 = asf((unsigned)__builtin_amdgcn_readlane((int)asu(x[0].x), 32));
  const float piv = lane < 32 ? p0 : p1;
  v2f s1 = {0.f, 0.f}, s2 = {0.f, 0.f};
#pragma unroll
  for (int i = 0; i < 10; ++i) { const v2f d = x[i] - piv; s1 += d; s2 += d * d; }
  float a = s1.x + s1.y, q = s2.x + s2.y;
  a += dpp<XOR1>(a); q += dpp<XOR1>(q); a += dpp<XOR2>(a); q += dpp<XOR2>(q);
  a += dpp<ROR4>(a); q += dpp<ROR4>(q); a += dpp<ROR8>(a); q += dpp<ROR8>(q);
  a = fold16(a, a) * (1.0f / DM); q = fold16(q, q) * (1.0f / DM);
  const float mb = bfr(piv + a), var = bfr(fmaf(-a, a, q));
  const float den = bfr(__builtin_amdgcn_sqrtf(bfr(var + 1e-5f)));
  const float rcp = __builtin_amdgcn_rcpf(den);
#pragma unroll
  for (int i = 0; i < 5; ++i) {
    unsigned o[2];
#pragma unroll
    for (int e = 0; e < 2; ++e) {
      const v2f t = x[2 * i + e] - mb;
      const v2f d = {bfr(t.x), bfr(t.y)}, q0 = d * rcp;
      const v2f qq = (d - q0 * den) * rcp + q0;
      o[e] = bfbits(qq.x) | (bfbits(qq.y) << 16);
    }
    *(LAS v2u*)(L + L_XP + (lane >> 5) * 1280 + ((lane & 31) + 32 * i) * 8) = v2u{o[0], o[1]};
  }
}

template <int NK, int KSTEP, int HALF>
__device__ __forceinline__ void load_bt(const LAS unsigned char* b, v4u (&f)[NK]) {
#pragma unroll
  for (int ks = 0; ks < NK; ++ks) {
    const v2u lo = __builtin_bit_cast(v2u, __builtin_amdgcn_ds_read_tr16_b64_v4i16((LAS v4s*)(b + ks * KSTEP)));
    const v2u hi = __builtin_bit_cast(v2u, __builtin_amdgcn_ds_read_tr16_b64_v4i16((LAS v4s*)(b + ks * KSTEP + HALF)));
    f[ks] = v4u{lo.x, lo.y, hi.x, hi.y};
  }
}
template <int NK> __device__ __forceinline__ v4f mm2(const LAS unsigned char* a, const v4u (&f)[NK], v4f c = v4f{0.f, 0.f, 0.f, 0.f}) {
  v4u av[NK];
#pragma unroll
  for (int ks = 0; ks < NK; ++ks) av[ks] = *(const LAS v4u*)(a + ks * 64);
#pragma unroll
  for (int ks = 0; ks < NK; ++ks) c = __builtin_amdgcn_mfma_f32_16x16x32_bf16(__builtin_bit_cast(bf16x8, av[ks]), __builtin_bit_cast(bf16x8, f[ks]), c, 0, 0, 0);
  return c;
}

#define DMA(OFF) "s_waitcnt vmcnt(%4)\n\tbuffer_load_dwordx4 %0, %1, %2 offen " OFF "nt lds"
template <int N> __device__ __forceinline__ void dma3(int voff, v4u srd, int soff, unsigned lds) {
  asm volatile("s_mov_b32 m0, %3\n\t" DMA("") "\n\t" DMA("offset:1024 ") "\n\t" DMA("offset:2048 ")
               :: "v"(voff), "s"(srd), "s"(soff), "s"(lds), "i"(N) : "memory");
}
template <int N> __device__ __forceinline__ void dma1(int voff, v4u srd, int soff, unsigned lds) {
  asm volatile("s_mov_b32 m0, %3\n\t" DMA("") :: "v"(voff), "s"(srd), "s"(soff), "s"(lds), "i"(N) : "memory");
}
__device__ __forceinline__ v4u srd_of(const void* ptr, unsigned bytes) {
  const u64 a = (u64)ptr;
  return v4u{(unsigned)a, (unsigned)(a >> 32) & 0xffffu, bytes, 0x00020000u};
}
constexpr int fdiv6(int a) { return a >= 0 ? a / 6 : -((5 - a) / 6); }
constexpr int lag3(int w) { return fdiv6(3 - w); }
constexpr int lag6(int w) { return fdiv6(6 - w); }

constexpr int R0 = K_R0, A1 = K_A1, C1 = K_C1, B1 = 8 * R0 - 6 * A1 - C1;
static_assert(B1 >= A1 && C1 >= 0 && 2 * (B1 + 32 - R0) <= 16 * NC, "member 6 has no prologue; a wave keeps the scores of 16 slots");
struct Plan {
  bool mlp;
  int npro, n1;
  __device__ __forceinline__ Plan(int h, int m) {
    mlp = m == 7 && h < NMLP;
    npro = m < 6 ? NS_WQ : mlp ? NS_MLP : 0;
    n1 = h < NMLP ? (m < 6 ? A1 : m == 6 ? B1 : C1) : R0;
  }
  __device__ __forceinline__ int piece(int i, int b, int h, int m) const {
    if (b == 0 && i < n1 && h < NMLP) return m == 7 ? 6 * A1 + B1 + i : i < A1 ? 7 * i + m : 6 * A1 + i;
    const int rho = b ? i : i - n1 + R0;
    return rho * 8 + ((m + rho * K_ROT) & 7);
  }
  __device__ __forceinline__ int len0() const { return 2 * (n1 + 32 - R0); }
  __device__ __forceinline__ int ntot() const { return npro + 2 * len0() + 128; }
  __device__ __forceinline__ int wo_skip() const { const int r = ntot() % NSLOT; return r + NWO > NSLOT ? NSLOT - r : 0; }
};
__device__ __forceinline__ int piece_off(int pc, int b, int h) { return (b * NH + h) * SLAB + (((pc >> 3) * K_STR + h * K_HROT) & 31) * 98304 + (pc & 7) * 12288; }

struct Loader {
  LAS unsigned* ctl;
  unsigned lbase;
  int s, pos, limit, pub;
  unsigned next_addr;
  u64 stall = 0;
  unsigned t0 = 0;
  int due = 0, pace = 0;
  int now = 0, tend = 0, tnew = K_TEND, slast = 0, pmax = 1 << 30, last = 0, shift = 0;
  __device__ __forceinline__ void publish(int n) { pub = max(pub, n); __hip_atomic_store(ctl + C_LANDED, (unsigned)pub, RLX_WG); }
  __device__ __forceinline__ void credit(int last) {
    if (__builtin_expect(last >= limit, 0)) {
      stall -= __builtin_amdgcn_s_memrealtime();
#pragma nounroll
      for (unsigned n = 0; n < LDS_SPIN; ++n) {
        int v = (int)__hip_atomic_load((LAS unsigned*)(size_t)next_addr, RLX_WG);
        v = min(v, dppi<XOR1>(v)); v = min(v, dppi<XOR2>(v)); v = min(v, dppi<MIR8>(v));
        limit = rfl(v) + NSLOT;
        if (last < limit) break;
        if (n == 8) { asm volatile("s_waitcnt vmcnt(0)" ::: "memory"); publish(s); }
        __builtin_amdgcn_s_sleep(1);
      }
      stall += __builtin_amdgcn_s_memrealtime();
      CBAR();
    }
  }
  __device__ __forceinline__ unsigned dst() const { return lbase + pos * SLOTB; }
  __device__ __forceinline__ void advance() { ++s; pos = pos + 1 == NSLOT ? 0 : pos + 1; }
  template <int W> __device__ __forceinline__ void rows_w(int voff, v4u srd, int so) {
    int v;
    u64 t;
    asm volatile("ds_read_b32 %0, %1" : "=v"(v) : "v"(next_addr) : "memory");
    dma3<W - 1>(voff, srd, so, dst());
    asm volatile("s_waitcnt lgkmcnt(0)" : "+v"(v));
    tnew = __builtin_amdgcn_readlane(v, 8);
    v = min(v, dppi<XOR1>(v)); v = min(v, dppi<XOR2>(v));
    asm volatile("" : "+v"(v));
    if (lag3(W) == lag6(W)) publish(s + lag3(W));
    dma3<W - 1>(voff, srd, so + 3072, dst() + 3072);
    limit = rfl(min(v, dppi<MIR8>(v))) + NSLOT;
    if (lag3(W) != lag6(W)) publish(s + lag6(W));
    advance();
    dma3<W - 1>(voff, srd, so + 6144, dst());
    if (lag3(W) == lag6(W)) publish(s + lag3(W));
    dma3<W - 1>(voff, srd, so + 9216, dst() + 3072);
    asm volatile("s_memrealtime %0" : "=s"(t));
    if (lag3(W) != lag6(W)) publish(s + lag6(W));
    advance();
    asm volatile("s_waitcnt lgkmcnt(0)" : "+s"(t));
    now = (int)((unsigned)t - t0);
  }
  __device__ __forceinline__ void rows(int voff, v4u srd, int so) {
    credit(s + 1);
    if (__builtin_expect(now - last > K_STALL, 0)) { const int d = now - last - (pace >> 8); shift += d; due += d << 8; }
    if (__builtin_expect(tnew != tend, 0)) {
      tend = tnew;
      due = max(due, (now - K_LATE - K_KEEP) << 8);
      pace = min(max(((tend + shift) * 256 - due) * 2 / (slast - s), 0), pmax);
    }
#pragma nounroll
    for (unsigned n = 0; now < (due >> 8) && n < LDS_SPIN; ++n) now = clock();
    last = now;
    const int late = now - (due >> 8);
    __hip_atomic_store(ctl + C_LATE, (unsigned)late, RLX_WG);
    due += pace;
    if (late <= K_LATE) rows_w<INF>(voff, srd, so); else rows_w<K_WB>(voff, srd, so);
  }
  __device__ __forceinline__ int clock() const { return (int)((unsigned)__builtin_amdgcn_s_memrealtime() - t0); }
};

template <int NP> __device__ __forceinline__ v2f sum_producers(__amdgpu_buffer_rsrc_t rw, int base, unsigned tag, int lane) {
  const int off = base + (lane >> 2) * (DM * 8) + blockIdx.x * 64 + (lane & 3) * 16;
  const bool two = lane < 4 * (NP - 16);
  v4u a, c;
#pragma nounroll
  for (unsigned n = 0; n < GLB_SPIN; ++n) {
    CBAR();
    a = ld16(rw, off);
    c = ld16(rw, two ? off + 16 * (DM * 8) : off);
    const u64 bad = __builtin_amdgcn_ballot_w64(!(fresh(a, tag) && fresh(c, tag)));
    if (bad == 0) break;
    if (K_SL2 && __builtin_popcountll(bad) > 12) __builtin_amdgcn_s_sleep(K_SL2);
  }
  if (!two) c = v4u{0u, 0u, 0u, 0u};
  float s0 = fold32(val(a.x) + val(c.x), val(a.y) + val(c.y)), s1 = fold32(val(a.z) + val(c.z), val(a.w) + val(c.w));
  s0 = fold16(s0, s0); s1 = fold16(s1, s1);
  s0 += dpp<ROR8>(s0); s1 += dpp<ROR8>(s1);
  return v2f{bfr(s0 + dpp<ROR4>(s0)), bfr(s1 + dpp<ROR4>(s1))};
}

__device__ __forceinline__ void combine(const Params& p, LAS unsigned char* L, int lane, int b) {
  LAS unsigned* ctl = (LAS unsigned*)(L + L_CTL);
  const int h = K_XMAP ? blockIdx.x >> 3 : blockIdx.x & 31, l48 = min(lane, 47);
  const __amdgpu_buffer_rsrc_t rw = rsrc(p.ws, W_END);
  const unsigned tag = (unsigned)rfl((int)ctl[C_TAG]);
  const int off = W_OP + (b * NH + h) * 8 * 768 + l48 * 16;
  v4f g[8];
  v4u mv;
  unsigned miss = 0xffu;
#pragma unroll
  for (int k = 0; k < 8; ++k) g[k] = v4f{0.f, 0.f, 0.f, 0.f};
  if (K_D1) __builtin_amdgcn_s_sleep(K_D1);
#pragma nounroll
  for (unsigned n = 0; n < GLB_SPIN; ++n) {
    v4u t[8];
    CBAR();
    mv = ld16(rw, W_ML + (h * 8 + (lane & 7)) * 16);
#pragma unroll
    for (int k = 0; k < 8; ++k)
      if (miss >> k & 1) t[k] = ld16(rw, off + k * 768);
#pragma unroll
    for (int k = 0; k < 8; ++k)
      if (miss >> k & 1) {
        if (__all(fresh(t[k], tag))) { g[k] = v4f{val(t[k].x), val(t[k].y), val(t[k].z), val(t[k].w)}; miss ^= 1u << k; }
      }
    if (miss == 0u && __all(fresh(mv, tag))) break;
    if (K_SL1 && __builtin_popcount(miss) > K_FEW) __builtin_amdgcn_s_sleep(K_SL1);
  }
  const float Mk = val(b ? mv.z : mv.x);
  float M = Mk;
  M = fmaxf(M, dpp<XOR1>(M)); M = fmaxf(M, dpp<XOR2>(M)); M = fmaxf(M, dpp<MIR8>(M));
  float w = exp2f_(Mk - M), Ls = w * val(b ? mv.w : mv.y);
  Ls += dpp<XOR1>(Ls); Ls += dpp<XOR2>(Ls); Ls += dpp<MIR8>(Ls);
  w *= __builtin_amdgcn_rcpf(Ls);
  v4f o = {0.f, 0.f, 0.f, 0.f};
#pragma unroll
  for (int k = 0; k < 8; ++k) o += asf((unsigned)__builtin_amdgcn_readlane((int)asu(w), k)) * g[k];
  *(LAS v2u*)(L + L_OA + b * (DH * 2) + l48 * 8) = v2u{bfbits(o.x) | (bfbits(o.y) << 16), bfbits(o.z) | (bfbits(o.w) << 16)};
  lds_set(ctl + C_O + b, 1u);
}

__device__ __forceinline__ void run_loader(const Params& p, LAS unsigned char* L, int lane, u64 tentry) {
  LAS unsigned* ctl = (LAS unsigned*)(L + L_CTL);
  const int bid = blockIdx.x, h = K_XMAP ? bid >> 3 : bid & 31, m = K_XMAP ? bid & 7 : bid >> 5;
  const int r = lane >> 3, j = lane & 7;
  __builtin_amdgcn_s_setprio(3);
  STAMPS();
  Loader ld{ctl, (unsigned)(size_t)L, 0, 0, NSLOT, 0, (unsigned)(size_t)(r == 1 ? ctl + C_TEND : ctl + C_NEXT + (j < NC ? j : 0))};
  const int voff = lane * 16;
  if (__builtin_expect(m < 6, 1)) {
    const v4u rq = srd_of(p.wq, DM * NH * DH * 2);
    const int vo = r * (NH * DH * 2) + (j ^ (r & 2)) * 16, so = (m / 3) * 320 * (NH * DH * 2) + h * (DH * 2) + (m % 3) * 128;
#pragma unroll 2
    for (int g = 0; g < 6 * NS_WQ; ++g) dma1<63>(g & 1 ? vo ^ 64 : vo, rq, so + min(g, 39) * (8 * NH * DH * 2), ld.lbase + g * 1024);
    ld.s = ld.pos = NS_WQ;
  }
  asm volatile("s_waitcnt lgkmcnt(0)\n\ts_barrier" ::: "memory");
  STAMP(1);
  const Plan pl(h, m);
  ld.t0 = (unsigned)tentry;
  ld.slast = pl.ntot();
  if (pl.mlp) {
    const v4u rg = srd_of(p.wg, DM * FF * 2), ru = srd_of(p.wu, DM * FF * 2), rd = srd_of(p.wd, FF * DM * 2);
    const int vo = r * (FF * 2) + (j ^ (r & 2)) * 16;
    constexpr int W = K_WMLP;
    const int sd = h * 64 * (DM * 2);
    auto slot = [&](int pc0, int sg) {
#pragma unroll
      for (int q = 0; q < 6; ++q) {
        const int pc = pc0 + q;
        if (pc < 16 || sg >= 0) dma1<W - 1>(q & 1 ? vo ^ 64 : vo, pc & 4 ? ru : rg, (sg < 0 ? 72 : sg) * (8 * FF * 2) + ((pc >> 3) * 4 + (pc & 3)) * (8 * FF * 2) + h * 128, ld.dst() + q * 1024);
        else dma1<W - 1>(voff, rd, sd + (pc - 16) * 1024, ld.dst() + q * 1024);
      }
      ld.publish(ld.s + lag6(W));
      ld.advance();
    };
#pragma nounroll
    for (int t = 0; t < 6; ++t) {
#pragma unroll
      for (int u = 0; u < 4; ++u) slot(6 * u, 12 * t);
    }
#pragma unroll
    for (int u = 0; u < 3; ++u) { ld.credit(ld.s); slot(6 * u, -1); }
#pragma nounroll
    for (int t = 27; t < NS_MLP; ++t) {
      ld.credit(ld.s);
      dma3<W - 1>(voff, rd, sd + (6 * t - 160) * 1024, ld.dst());
      dma3<W - 1>(voff, rd, sd + (6 * t - 157) * 1024, ld.dst() + 3072);
      ld.publish(ld.s + lag6(W));
      ld.advance();
    }
  }
  STAMP(2);
  ld.now = ld.clock();
  ld.last = ld.now;
  ld.due = ld.now << 8;
  ld.tend = K_TEND;
  ld.pace = (K_TEND * 256 - ld.due) * 2 / (ld.slast - ld.s);
  ld.pmax = ld.pace + (ld.pace >> 3);
#pragma nounroll
  for (int ph = 0; ph < 4; ++ph) {
    const v4u rs = srd_of(ph & 1 ? p.v : p.k, KV_BYTES);
    const int b = ph >> 1, np = b ? 32 : pl.len0() / 2;
#pragma nounroll
    for (int i = 0; i < np; ++i) ld.rows(voff, rs, piece_off(pl.piece(i, b, h, m), b, h));
    if (ph == 0) STAMP(5); else if (ph == 1) STAMP(6); else if (ph == 2) STAMP(7);
  }
  STAMP(3);
  const v4u ro = srd_of(p.wo, NH * DH * DM * 2);
  int row = lane / 10, ch = lane - row * 10;
  ld.credit(ld.s + pl.wo_skip() + NWO - 1);
  if (pl.wo_skip()) ld.pos = 0;
#pragma nounroll
  for (int t = 0; t < NWO; ++t) {
#pragma unroll
    for (int q = 0; q < 6; ++q) {
      dma1<INF - 1>(row * (DM * 2) + ch * 16, ro, h * DH * (DM * 2) + m * 160, ld.dst() + q * 1024);
      if (q == 2 && lag3(INF) == lag6(INF)) ld.publish(ld.s + lag3(INF));
      if (q == 5 && lag3(INF) != lag6(INF)) ld.publish(ld.s + lag6(INF));
      row += 6; ch += 4;
      if (ch >= 10) { ch -= 10; row += 1; }
    }
    ld.advance();
  }
  asm volatile("s_waitcnt vmcnt(0)" ::: "memory");
  ld.publish(ld.s);
  STAMP(4);
  if (bid < NFIN) {
    const v2f y = sum_producers<NMLP>(rsrc(p.ws, W_END), W_YP, (unsigned)rfl((int)ctl[C_TAG]), lane);
    const float sg = *(const LAS float*)(L + L_G + lane * 4);
    const unsigned xn = *(const LAS unsigned*)(L + L_XP + (lane >> 5) * 1280 + bid * 16 + (lane & 3) * 4);
    *(LAS v2f*)(L + L_G + 256 + lane * 8) = v2f{bfr(siluf(bfr(lo16(xn) + bfr(y.x * sg)))), bfr(siluf(bfr(hi16(xn) + bfr(y.y * sg))))};
    lds_set(ctl + C_G, 1u);
  }
  st.t[0] = tentry;
  FLUSH(0, 8);
}

struct Early { v2u xr[5], wr[5]; unsigned e0, xo; };
__device__ __forceinline__ void early_loads(const Params& p, Early& e, int lane) {
#pragma unroll
  for (int i = 0; i < 5; ++i) e.xr[i] = ((const GAS v2u*)p.x)[(lane >> 5) * 160 + (lane & 31) + 32 * i];
  e.e0 = __hip_atomic_load((GAS unsigned*)(p.ws + W_EPOCH), RLX_AGENT);
}
__device__ __forceinline__ void run_helper(const Params& p, LAS unsigned char* L, int lane, Early& e) {
  LAS unsigned* ctl = (LAS unsigned*)(L + L_CTL);
  const int bid = blockIdx.x, h = K_XMAP ? bid >> 3 : bid & 31, m = K_XMAP ? bid & 7 : bid >> 5;
  const int j = lane & 7;
  const __amdgpu_buffer_rsrc_t rw = rsrc(p.ws, W_END);
  STAMPS();
  const int fb = lane >> 5, col = min(bid, NFIN - 1) * 8 + 2 * (lane & 3), l48 = min(lane, 47);
#pragma unroll
  for (int i = 0; i < 5; ++i) e.wr[i] = ((const GAS v2u*)p.wr)[(lane & 31) + 32 * i];
  e.xo = ((const GAS unsigned*)p.x)[(fb * DM + col) >> 1];
  __builtin_amdgcn_sched_barrier(0);
  layer_norm(e.xr, L, lane);
  const unsigned epoch = (unsigned)rfl((int)e.e0) + 1u, tag = tag_of(epoch);
  ctl[C_TAG] = tag;
  lds_set(ctl + C_XOK, 1u);
  STAMP(0);
  {
    lds_wait<2>(ctl + C_LANDED, K_QWAIT);
    const int off = W_QP + (h * 2 * DH + 4 * l48) * 8;
    v4u g[4];
#pragma nounroll
    for (unsigned n = 0; n < GLB_SPIN; ++n) {
      CBAR();
#pragma unroll
      for (int i = 0; i < 4; ++i) g[i] = ld16(rw, off + (i & 1) * 16 + (i >> 1) * (DH * 8));
      if (__all(fresh(g[0], tag) && fresh(g[1], tag) && fresh(g[2], tag) && fresh(g[3], tag))) break;
    }
#pragma unroll
    for (int b = 0; b < 2; ++b)
      *(LAS v2u*)(L + L_QB + b * (DH * 2) + l48 * 8) = v2u{bfbits(val(g[0][b]) + val(g[2][b])) | (bfbits(val(g[0][2 + b]) + val(g[2][2 + b])) << 16),
                                                            bfbits(val(g[1][b]) + val(g[3][b])) | (bfbits(val(g[1][2 + b]) + val(g[3][2 + b])) << 16)};
    lds_set(ctl + C_Q, 1u);
  }
  float rs = 0.f;
#pragma unroll
  for (int i = 0; i < 5; ++i) {
    const v2u xx = *(const LAS v2u*)(L + L_XP + fb * 1280 + ((lane & 31) + 32 * i) * 8);
    rs = dot2(e.wr[i].y, xx.y, dot2(e.wr[i].x, xx.x, rs));
  }
  rs += dpp<XOR1>(rs); rs += dpp<XOR2>(rs); rs += dpp<ROR4>(rs); rs += dpp<ROR8>(rs);
  *(LAS float*)(L + L_G + lane * 4) = bfr(1.0f / (1.0f + expf(-bfr(fold16(rs, rs)))));
  v4u po[2], pml;
  v2f st2[2];
#pragma unroll
  for (int b = 0; b < 2; ++b) {
    lds_wait_all<NC, 16>(ctl + C_ML, j, b + 1);
    v2f ml = *(const LAS v2f*)(L + L_MLW + b * 64 + j * 8);
    if (j >= NC) ml = v2f{-3.0e38f, 0.f};
    float M = ml.x;
    M = fmaxf(M, dpp<XOR1>(M)); M = fmaxf(M, dpp<XOR2>(M)); M = fmaxf(M, dpp<MIR8>(M));
    M = val(asu(M));
    const float w = exp2f_(ml.x - M);
    *(LAS float*)(L + L_F + b * 32 + j * 4) = w;
    lds_set(ctl + C_F, b + 1);
    float Ls = w * ml.y;
    Ls += dpp<XOR1>(Ls); Ls += dpp<XOR2>(Ls); Ls += dpp<MIR8>(Ls);
    st2[b] = v2f{M, Ls};
    if (b == 0) {
      lds_wait_all<NC, 16>(ctl + C_R, j, 1);
    } else {
      pml = pack4(st2[0].x, st2[0].y, st2[1].x, st2[1].y, tag);
      asm volatile("" : "+v"(pml), "+v"(po[0]));
      lds_wait_all<NC, 0>(ctl + C_R, j, 2);
    }
    STAMP(1);
    v4f o = {0.f, 0.f, 0.f, 0.f};
#pragma unroll
    for (int c = 0; c < NC; ++c) o += *(const LAS v4f*)(L + L_REC + c * 768 + l48 * 16);
    po[b] = pack4(o.x, o.y, o.z, o.w, tag);
    if (b == 0) lds_set(ctl + C_RFREE, 1u);
  }
  if (lane < 48) {
    st16(rw, W_OP + ((NH + h) * 8 + m) * 768 + lane * 16, po[1]);
    st16(rw, W_OP + (h * 8 + m) * 768 + lane * 16, po[0]);
  }
  if (lane == 0) st16(rw, W_ML + (h * 8 + m) * 16, pml);
  lds_set(ctl + C_PUB, 1u);
  STAMP(2);
  combine(p, L, lane, 1);
  STAMP(4);
  if (bid < NFIN) {
    lds_wait_all<5, 0>(ctl + C_T3, j);
    lds_wait<0>(ctl + C_G, 1);
    const v2f gt = *(const LAS v2f*)(L + L_G + 256 + lane * 8);
    STAMP(5);
    if (K_D2) __builtin_amdgcn_s_sleep(K_D2);
    const v2f sv = sum_producers<NH>(rw, W_SP, tag, lane);
    STAMP(6);
    const unsigned ov = bfbits(lo16(e.xo) + bfr(sv.x * gt.x)) | (bfbits(hi16(e.xo) + bfr(sv.y * gt.y)) << 16);
    if ((lane & 31) < 4) ((GAS unsigned*)p.out)[(fb * DM + col) >> 1] = ov;
    if (bid == 0 && lane == 0) __hip_atomic_store((GAS unsigned*)(p.ws + W_EPOCH), epoch, RLX_AGENT);
    STAMP(7);
  }
  st.t[3] = st.t[4];
  FLUSH(8, bid < NFIN ? 8 : 5);
}

__device__ __forceinline__ void run_consumer(const Params& p, LAS unsigned char* L, int lane, int c) {
  LAS unsigned* ctl = (LAS unsigned*)(L + L_CTL);
  const int bid = blockIdx.x, h = K_XMAP ? bid >> 3 : bid & 31, m = K_XMAP ? bid & 7 : bid >> 5;
  const int r = lane >> 3, j = lane & 7;
  const Plan pl(h, m);
  const __amdgpu_buffer_rsrc_t rw = rsrc(p.ws, W_END);
  STAMPS();
  if (pl.npro == 0 || (m < 6 && c >= 4)) lds_set(ctl + C_NEXT + c, pl.npro + c);
  lds_wait<1>(ctl + C_XOK, 1);
  const unsigned tag = (unsigned)rfl((int)ctl[C_TAG]);
  const int g = lane >> 4;
  const int bq = (lane >> 4) * 1024 + ((lane & 15) >> 2) * 128 + ((2 * c + ((lane & 3) >> 1)) ^ (2 * (r & 3))) * 16 + (lane & 1) * 8;
  if (m < 6) {
    if (c < 4) {
      lds_wait<0>(ctl + C_LANDED, NS_WQ);
      v4u bf[10];
      load_bt<10, 4096, 512>(L + bq, bf);
      lds_set(ctl + C_NEXT + c, NS_WQ + c);
      const v4f qv = mm2<10>(L + L_XP + (lane & 1) * 1280 + (m / 3) * 640 + (lane >> 4) * 16, bf);
      if (lane < 16) st8(rw, W_QP + ((h * 2 + m / 3) * DH + (m % 3) * 64 + 16 * c + lane) * 8, pack2(qv.x, qv.y, tag));
    }
  } else if (pl.mlp) {
    if (c < 4) {
      const LAS unsigned char* ap = L + L_XP + (lane & 1) * 1280 + g * 16;
      v4f ga = {0.f, 0.f, 0.f, 0.f}, ua = {0.f, 0.f, 0.f, 0.f};
#pragma nounroll
      for (int t = 0; t < 20; t += 2) {
        lds_wait<1>(ctl + C_LANDED, (8 * t + 21) / 6);
        v4u gf[2], uf[2];
        load_bt<2, 8192, 512>(L + (t < 18 ? t : t - 18) * 8192 + bq, gf);
        load_bt<2, 8192, 512>(L + (t < 18 ? t : t - 18) * 8192 + 4096 + bq, uf);
        lds_set(ctl + C_NEXT + c, (8 * t + 16) / 6);
        ga = mm2<2>(ap + t * 64, gf, ga);
        ua = mm2<2>(ap + t * 64, uf, ua);
      }
      if (lane < 32) *(LAS unsigned short*)(L + L_HM + g * 128 + (16 * c + (lane & 15)) * 2) = (unsigned short)bfbits(bfr(siluf(bfr(g ? ga[1] : ga[0]))) * bfr(g ? ua[1] : ua[0]));
      lds_set(ctl + C_HM + c, 1u);
    } else {
      lds_set(ctl + C_NEXT + c, 160 / 6);
    }
    lds_wait_all<4, 1>(ctl + C_HM, j);
    lds_wait<1>(ctl + C_LANDED, NS_MLP);
#pragma nounroll
    for (int n = c; n < DM / 16; n += NC) {
      v4u bf[2];
      load_bt<2, 32 * 1280, 4 * 1280>(L + (160 * 1024 - RINGB) + (8 * g + ((lane & 15) >> 2)) * 1280 + n * 32 + (lane & 3) * 8, bf);
      const v4f y = mm2<2>(L + L_HM + (lane & 1) * 128 + g * 16, bf);
      if (lane < 16) st8(rw, W_YP + (h * DM + n * 16 + lane) * 8, pack2(y[0], y[1], tag));
    }
    lds_set(ctl + C_NEXT + c, NS_MLP + c);
  }
  STAMP(0);
  const int ntot = pl.ntot();
  int s = pl.npro + c;
  auto take = [&](int i, int len) -> const LAS unsigned char* {
    lds_wait<1>(ctl + C_LANDED, s + 1);
    const LAS unsigned char* sp = L + s % NSLOT * SLOTB;
    s += i + NC < len ? NC : len - i + c;
    return sp;
  };
  auto fetch = [&](v4u (&kk)[6], int i, int len) {
    const LAS unsigned char* sp = take(i, len) + r * 384 + j * 16;
#pragma unroll
    for (int u = 0; u < 6; ++u) kk[u] = *(const LAS v4u*)(sp + (u / 3) * 3072 + (u % 3) * 128);
    lds_set(ctl + C_NEXT + c, s);
  };
  v4u e0[6], e1[6];
  fetch(e0, c, pl.len0());
  fetch(e1, c + NC, pl.len0());
  lds_wait<1>(ctl + C_Q, 1);
  STAMP(1);
  LAS unsigned char* pw = L + L_P + c * 512;
#pragma nounroll
  for (int b = 0; b < 2; ++b) {
    const int len = b ? 64 : pl.len0();
    v4u q[3];
#pragma unroll
    for (int u = 0; u < 3; ++u) q[u] = *(const LAS v4u*)(L + L_QB + (b * DH + (j + 8 * u) * 8) * 2);
    float sc[4] = {-3.0e38f, -3.0e38f, -3.0e38f, -3.0e38f};
    auto score = [&](const v4u (&kk)[6], int it) {
      float d0 = 0.f, d1 = 0.f;
#pragma unroll
      for (int u = 0; u < 3; ++u)
#pragma unroll
        for (int e = 0; e < 4; ++e) { d0 = dot2(kk[u][e], q[u][e], d0); d1 = dot2(kk[3 + u][e], q[u][e], d1); }
      d0 += dpp<XOR1>(d0); d1 += dpp<XOR1>(d1); d0 += dpp<XOR2>(d0); d1 += dpp<XOR2>(d1);
      const float d = (j & 4 ? d1 : d0) + dpp<MIR8>(j & 4 ? d0 : d1);
      const float t = bfr(bfr(d) * 0.07216878364870322f) * LOG2E;
#pragma unroll
      for (int k = 0; k < 4; ++k) sc[k] = it == 4 * k + (j & 3) ? t : sc[k];
    };
    int i = c, it = 0;
    if (b == 0) { score(e0, 0); score(e1, 1); i += 2 * NC; it = 2; }
#pragma nounroll
    for (; i < len; i += NC, ++it) {
      v4u kk[6];
      fetch(kk, i, len);
      score(kk, it);
    }
    float M = fmaxf(fmaxf(sc[0], sc[1]), fmaxf(sc[2], sc[3]));
    M = fmaxf(M, dpp<XOR1>(M)); M = fmaxf(M, dpp<XOR2>(M)); M = fmaxf(M, dpp<ROR4>(M)); M = fmaxf(M, dpp<ROR8>(M));
    M = max_rows(M);
    float ls = 0.f;
#pragma unroll
    for (int k = 0; k < 4; ++k) {
      const float pr = exp2f_(sc[k] - M);
      ls += pr;
      *(LAS unsigned short*)(pw + k * 128 + (j & 3) * 32 + (r + 8 * (j >> 2)) * 2) = (unsigned short)bfbits(pr);
    }
    ls = wave_sum(ls);
    *(LAS v2f*)(L + L_MLW + b * 64 + c * 8) = v2f{M, ls};
    lds_set(ctl + C_ML + c, b + 1);
    v4f acc[12];
#pragma unroll
    for (int t = 0; t < 12; ++t) acc[t] = v4f{0.f, 0.f, 0.f, 0.f};
    float f = 0.f;
#pragma nounroll
    for (i = c, it = 0; i < len; i += NC, ++it) {
      if (i + NC >= len) {
        lds_wait<1>(ctl + C_F, b + 1);
        if (b) lds_wait<1>(ctl + C_RFREE, 1);
        f = *(const LAS float*)(L + L_F + b * 32 + c * 4);
      }
      const LAS unsigned char* sp = take(i, len) + (lane >> 2) * 384 + (lane & 3) * 8;
      const v4s pa = *(const LAS v4s*)(pw + it * 32 + g * 8);
      v4s vf[12];
#pragma unroll
      for (int t = 0; t < 12; ++t) vf[t] = __builtin_amdgcn_ds_read_tr16_b64_v4i16((LAS v4s*)(sp + t * 32));
      lds_set(ctl + C_NEXT + c, s);
#pragma unroll
      for (int t = 0; t < 12; ++t) acc[t] = __builtin_amdgcn_mfma_f32_16x16x16bf16_1k(pa, vf[t], acc[t], 0, 0, 0);
    }
    if (b) STAMP(3); else STAMP(2);
#pragma unroll
    for (int k = 0; k < 3; ++k) {
      const float v = g & 2 ? (g & 1 ? acc[9 + k][0] : acc[6 + k][0]) : (g & 1 ? acc[3 + k][0] : acc[k][0]);
      *(LAS float*)(L + L_REC + c * 768 + (g * 48 + k * 16 + (lane & 15)) * 4) = v * f;
    }
    lds_set(ctl + C_R + c, b + 1);
  }
  STAMP(4);
  if (c == 5) {
    lds_wait<0>(ctl + C_PUB, 1);
    combine(p, L, lane, 0);
    return;
  }
  lds_wait<0>(ctl + C_LANDED, ntot + NWO);
  v4u bf[6];
  load_bt<6, 32 * 160, 4 * 160>(L + (ntot + pl.wo_skip()) % NSLOT * SLOTB + ((lane >> 4) * 8 + ((lane & 15) >> 2)) * 160 + c * 32 + (lane & 3) * 8, bf);
  STAMP(5);
  lds_wait_all<2, 0>(ctl + C_O, j);
  STAMP(6);
  const v4f sv = mm2<6>(L + L_OA + (lane & 1) * (DH * 2) + (lane >> 4) * 16, bf);
  if (lane < 16) st8(rw, W_SP + (h * DM + m * 80 + c * 16 + lane) * 8, pack2(sv.x, sv.y, tag));
  lds_set(ctl + C_T3 + c, 1u);
  STAMP(7);
  if (c == 0) FLUSH(16, 8);
  if (c == 4) FLUSH(24, 8);
}

__device__ __forceinline__ void run_pacer(const Params& p, LAS unsigned char* L, int lane, u64 tentry) {
  LAS unsigned* ctl = (LAS unsigned*)(L + L_CTL);
  const int bid = blockIdx.x;
  const __amdgpu_buffer_rsrc_t rw = rsrc(p.ws, W_END);
  auto until = [&](int t) {
    int now = 0;
#pragma nounroll
    for (unsigned n = 0; n < LDS_SPIN; ++n) {
      now = (int)((unsigned)__builtin_amdgcn_s_memrealtime() - (unsigned)tentry);
      if (now >= t) break;
      __builtin_amdgcn_s_sleep(3);
    }
    return now;
  };
  lds_wait<16>(ctl + C_XOK, 1);
  const unsigned tag = (unsigned)rfl((int)ctl[C_TAG]);
  float tend = K_TEND, base = K_BASE;
#pragma nounroll
  for (int c = 0; c < K_NCP; ++c) {
    const int t = K_CP0 + c * K_CP;
    if (until(t) > t + K_CPD / 2) continue;
    if ((bid & 96) == 0 && lane == 0) st8(rw, W_PC + c * 512 + ((bid & 31) | (bid >> 7) << 5) * 8, pack2((float)(int)__hip_atomic_load(ctl + C_LATE, RLX_WG), tend, tag));
    until(t + K_CPD);
    const v2u g = __builtin_amdgcn_raw_buffer_load_b64(rw, W_PC + c * 512 + lane * 8, 0, 16);
    const bool ok = __builtin_amdgcn_perm(g.y, g.x, 0x0c0c0400u) == (tag & 0xffffu);
    const float n = (float)__builtin_popcountll(__builtin_amdgcn_ballot_w64(ok));
    float tm = ok ? val(g.y) : 0.f;
    tm = fmaxf(tm, dpp<XOR1>(tm)); tm = fmaxf(tm, dpp<XOR2>(tm)); tm = fmaxf(tm, dpp<ROR4>(tm)); tm = fmaxf(tm, dpp<ROR8>(tm));
    tend = fmaxf(tend, max_rows(tm));
    const float lam = ok ? val(g.x) : 0.f, inv = 1.f / fmaxf(n, 1.f);
    const float late = fmaxf(wave_sum(lam) * inv - K_DEAD, K_XW * wave_sum(fmaxf(lam - K_L0, 0.f)) * inv - K_XD);
    const float step = fminf(K_STEP, late * fmaxf(K_GMIN, K_GAIN * (tend - t) / fmaxf(t - base, K_FLOOR)));
    if (step > K_MIN && n >= 48.f) { tend = floorf(tend + step); base = t + K_CPD; }
    lds_set(ctl + C_TEND, (unsigned)rfl((int)tend));
    if (bid == 0) { ((GAS float*)(p.ws + W_ST + (512 << 10)))[c * 2] = late; ((GAS float*)(p.ws + W_ST + (512 << 10)))[c * 2 + 1] = tend - K_TEND; }
  }
}

__global__ void __launch_bounds__(NTHR, 3) fused_decode(Params p) {
  extern __shared__ __attribute__((aligned(16))) unsigned char smem[];
  const u64 tentry = __builtin_amdgcn_s_memrealtime();
  LAS unsigned char* L = (LAS unsigned char*)smem;
  const int tid = threadIdx.x, lane = tid & 63, wave = rfl(tid >> 6);
  if (tid < 80) ((LAS unsigned*)(L + L_CTL))[tid] = tid == C_TEND ? K_TEND : 0u;
  Early e;
  if (wave == NC + 1) early_loads(p, e, lane);
  int role = wave;
  asm volatile("" : "+s"(role));
  if (role > 0) {
    asm volatile("s_waitcnt lgkmcnt(0)\n\ts_barrier" ::: "memory");
    if (wave <= NC) run_consumer(p, L, lane, wave - 1);
    else if (wave == NC + 1) run_helper(p, L, lane, e);
    else run_pacer(p, L, lane, tentry);
  } else {
    run_loader(p, L, lane, tentry);
  }
}

}

namespace {
__global__ void k_rep(Params p) {
  const u64 now = __builtin_amdgcn_s_memrealtime();
  const GAS u64* st = (const GAS u64*)(p.ws + W_ST);
  const int l = threadIdx.x;
  u64 t0 = ~0ull, mx = 0, sum = 0;
  for (int i = l; i < 256; i += 64) { const u64 d = st[i * 32]; t0 = d < t0 ? d : t0; }
  for (int o = 32; o; o >>= 1) { const u64 b = __shfl_xor(t0, o); t0 = b < t0 ? b : t0; }
  for (int i = l; i < NWGS; i += 64) { const u64 d = st[i * 32 + IDX] - t0; mx = d > mx ? d : mx; sum += d; }
  for (int o = 32; o; o >>= 1) { const u64 b = __shfl_xor(mx, o); mx = b > mx ? b : mx; sum += __shfl_xor(sum, o); }
  u64 v = MODE == 0 ? mx : MODE == 1 ? sum / NWGS : MODE == 2 ? now - t0 : (u64)(((const GAS float*)(p.ws + W_ST + (512 << 10)))[PIDX] + 100.f);
  if (v > 20000) v = 20000;
  while (__builtin_amdgcn_s_memrealtime() - now < 1000 + SCALE * v) __builtin_amdgcn_s_sleep(8);
}
}
extern "C" void kernel_launch(void* const* d_in, const int* in_sizes, int n_in, void* d_out, int out_size, void* d_ws,
                              size_t ws_size, hipStream_t stream) {
  Params p;
  p.x = (const unsigned short*)d_in[0];
  p.k = (const unsigned short*)d_in[1];
  p.v = (const unsigned short*)d_in[2];
  p.wq = (const unsigned short*)d_in[3];
  p.wo = (const unsigned short*)d_in[4];
  p.wr = (const unsigned short*)d_in[6];
  p.wg = (const unsigned short*)d_in[7];
  p.wu = (const unsigned short*)d_in[8];
  p.wd = (const unsigned short*)d_in[9];
  p.out = (unsigned short*)d_out;
  p.ws = (unsigned char*)d_ws;
  (void)hipFuncSetAttribute((const void*)fused_decode, hipFuncAttributeMaxDynamicSharedMemorySize, L_END);
  fused_decode<<<NWG, NTHR, L_END, stream>>>(p);
  k_rep<<<1, 64, 0, stream>>>(p);
}
```

```cpp
#define PIDX 1
#define K_STAMPS 1
#define IDX 1
#define MODE 1
#define SCALE 20
#define NWGS 256
#include <hip/hip_runtime.h>
#include <hip/hip_bf16.h>

namespace {

#define GAS __attribute__((address_space(1)))
#define LAS __attribute__((address_space(3)))
#define RLX_AGENT __ATOMIC_RELAXED, __HIP_MEMORY_SCOPE_AGENT
#define RLX_WG __ATOMIC_RELAXED, __HIP_MEMORY_SCOPE_WORKGROUP
typedef unsigned v4u __attribute__((ext_vector_type(4)));
typedef unsigned v2u __attribute__((ext_vector_type(2)));
typedef float v4f __attribute__((ext_vector_type(4)));
typedef float v2f __attribute__((ext_vector_type(2)));
typedef short v4s __attribute__((ext_vector_type(4)));
typedef __bf16 bf16x2 __attribute__((ext_vector_type(2)));
typedef __bf16 bf16x8 __attribute__((ext_vector_type(8)));
typedef unsigned long long u64;

#define K_INF 21
#define K_NSLOT 24
#define K_A1 10
#define K_C1 1
#define K_WMLP 33
#define K_HROT 1
#define K_XMAP 0
#define K_STR 21
#define K_ROT 1
#define K_FEW 2
#define K_SL1 8
#define K_SL2 8
#define K_TEND 5380
#define K_LATE 40
#define K_WB 33
#define K_D1 12
#define K_D2 12
#define K_NCP 14
#define K_CP0 1625
#define K_CP 275
#define K_CPD 125
#define K_L0 60.f
#define K_GMIN 3.f
#define K_STEP 200.f
#define K_KEEP 0
#define K_XW 2.f
#define K_XD 6.f
#define K_BASE 1200
#define K_FLOOR 600.f
#define K_MIN 8.f
#define K_STALL 160
#define K_GAIN 0.6f
#define K_DEAD 50.f
#define K_R0 9
#define K_QWAIT 22

constexpr int DM = 640, NH = 32, DH = 192, SK = 8192, FF = 1536;
constexpr int NWG = 256, NC = 6, NTHR = (NC + 3) * 64;
constexpr int INF = K_INF;
constexpr int NSLOT = K_NSLOT;
constexpr int SLOTB = 6144, RINGB = NSLOT * SLOTB, NWO = 5, NS_WQ = 7, NS_MLP = 40;
constexpr unsigned KV_BYTES = 2u * NH * SK * DH * 2u;
constexpr int SLAB = SK * DH * 2;
constexpr int NMLP = FF / 64;
constexpr int NFIN = DM / 8;
static_assert(NSLOT == 24, "the MLP images rely on where the ring wraps");
static_assert(INF >= 7 && INF <= 30 && K_WB <= 60, "vmcnt is 6 bits; the last K/V slots are published while wo is requested");

constexpr int L_XP = NSLOT * SLOTB;
constexpr int L_REC = L_XP + 2560;
constexpr int L_QB = L_REC + 4608;
constexpr int L_OA = L_QB + 768;
constexpr int L_HM = L_OA + 768;
constexpr int L_P = L_HM + 256;
constexpr int L_MLW = L_P + 3072;
constexpr int L_F = L_MLW + 128;
constexpr int L_G = L_F + 64;
constexpr int L_CTL = L_G + 768;
constexpr int L_END = L_CTL + 320;
static_assert(L_END <= 160 * 1024 && L_END > 80 * 1024, "one workgroup per CU");
enum { C_NEXT = 0, C_HM = 16, C_ML = 24, C_R = 32, C_T3 = 48, C_O = 56, C_LANDED = 64, C_XOK, C_TAG, C_Q, C_F, C_RFREE, C_G, C_PUB, C_LATE, C_TEND };

constexpr int W_EPOCH = 0;
constexpr int W_ML = 1024;
constexpr int W_OP = W_ML + NH * 8 * 16;
constexpr int W_SP = W_OP + 2 * NH * 8 * 768;
constexpr int W_YP = W_SP + NH * DM * 8;
constexpr int W_QP = W_YP + NMLP * DM * 8;
constexpr int W_PC = W_QP + NH * 2 * DH * 8;
constexpr int W_END = W_PC + 24 * 512;
constexpr int W_ST = 4 << 20;

constexpr unsigned LDS_SPIN = 1u << 20, GLB_SPIN = 1u << 16;
constexpr float LOG2E = 1.4426950408889634f;

struct Params {
  const unsigned short *x, *k, *v, *wq, *wo, *wr, *wg, *wu, *wd;
  unsigned short* out;
  unsigned char* ws;
};

__device__ __forceinline__ int rfl(int v) { return __builtin_amdgcn_readfirstlane(v); }
__device__ __forceinline__ float bfr(float x) { return (float)(__bf16)x; }
__device__ __forceinline__ unsigned bfbits(float x) { return (unsigned)__builtin_bit_cast(unsigned short, (__bf16)x); }
__device__ __forceinline__ float lo16(unsigned u) { return __builtin_bit_cast(float, u << 16); }
__device__ __forceinline__ float hi16(unsigned u) { return __builtin_bit_cast(float, u & 0xffff0000u); }
__device__ __forceinline__ float asf(unsigned u) { return __builtin_bit_cast(float, u); }
__device__ __forceinline__ unsigned asu(float f) { return __builtin_bit_cast(unsigned, f); }
__device__ __forceinline__ float dot2(unsigned a, unsigned b, float c) {
  return __builtin_amdgcn_fdot2_f32_bf16(__builtin_bit_cast(bf16x2, a), __builtin_bit_cast(bf16x2, b), c, false);
}
template <int CTRL> __device__ __forceinline__ float dpp(float x) {
  return __builtin_bit_cast(float, __builtin_amdgcn_mov_dpp(__builtin_bit_cast(int, x), CTRL, 0xf, 0xf, true));
}
template <int CTRL> __device__ __forceinline__ int dppi(int x) { return __builtin_amdgcn_mov_dpp(x, CTRL, 0xf, 0xf, true); }
constexpr int XOR1 = 0xB1, XOR2 = 0x4E, MIR8 = 0x141, ROR4 = 0x124, ROR8 = 0x128;
__device__ __forceinline__ float fold32(float a, float b) {
  auto s = __builtin_amdgcn_permlane32_swap(asu(a), asu(b), false, false);
  return asf(s[0]) + asf(s[1]);
}
__device__ __forceinline__ float fold16(float a, float b) {
  auto s = __builtin_amdgcn_permlane16_swap(asu(a), asu(b), false, false);
  return asf(s[0]) + asf(s[1]);
}
__device__ __forceinline__ float max_rows(float x) {
  auto s = __builtin_amdgcn_permlane16_swap(asu(x), asu(x), false, false);
  x = fmaxf(asf(s[0]), asf(s[1]));
  auto t = __builtin_amdgcn_permlane32_swap(asu(x), asu(x), false, false);
  return fmaxf(asf(t[0]), asf(t[1]));
}
__device__ __forceinline__ float sum_rows(float x) { x = fold16(x, x); return fold32(x, x); }
__device__ __forceinline__ float wave_sum(float x) {
  x += dpp<XOR1>(x); x += dpp<XOR2>(x); x += dpp<ROR4>(x); x += dpp<ROR8>(x);
  return sum_rows(x);
}
__device__ __forceinline__ float exp2f_(float x) { return __builtin_amdgcn_exp2f(x); }
__device__ __forceinline__ float siluf(float x) { return x / (1.0f + expf(-x)); }
#define CBAR() asm volatile("" ::: "memory")

__device__ __forceinline__ __amdgpu_buffer_rsrc_t rsrc(const void* p, unsigned bytes) {
  return __builtin_amdgcn_make_buffer_rsrc((void*)p, 0, bytes, 0x00020000);
}
__device__ __forceinline__ void lds_set(LAS unsigned* w, unsigned v) {
  CBAR();
  __hip_atomic_store(w, v, RLX_WG);
  CBAR();
}
template <int SLEEP> __device__ __forceinline__ void lds_wait(LAS unsigned* w, unsigned want) {
#pragma nounroll
  for (unsigned n = 0; n < LDS_SPIN; ++n) {
    if ((unsigned)rfl((int)__hip_atomic_load(w, RLX_WG)) >= want) break;
    if (SLEEP) __builtin_amdgcn_s_sleep(SLEEP);
  }
  CBAR();
}
template <int N, int SLEEP> __device__ __forceinline__ void lds_wait_all(LAS unsigned* w, int j, unsigned want = 1u) {
  LAS unsigned* a = w + (j < N ? j : 0);
#pragma nounroll
  for (unsigned n = 0; n < LDS_SPIN; ++n) {
    if (__all(__hip_atomic_load(a, RLX_WG) >= want)) break;
    if (SLEEP) __builtin_amdgcn_s_sleep(SLEEP);
  }
  CBAR();
}
__device__ __forceinline__ unsigned tag_of(unsigned e) { return ((e & 0xffu) | (((e >> 8) ^ e) & 0xffu) << 8) * 0x10001u; }
__device__ __forceinline__ v2u pack2(float a, float b, unsigned tag) {
  return v2u{__builtin_amdgcn_perm(asu(a), tag, 0x07060500u), __builtin_amdgcn_perm(asu(b), tag, 0x07060501u)};
}
__device__ __forceinline__ v4u pack4(float a, float b, float c, float d, unsigned tag) {
  const v2u x = pack2(a, b, tag), y = pack2(c, d, tag);
  return v4u{x.x, x.y, y.x, y.y};
}
__device__ __forceinline__ bool fresh(v4u g, unsigned tag) {
  return (__builtin_amdgcn_perm(g.y, g.x, 0x0c0c0400u) | __builtin_amdgcn_perm(g.w, g.z, 0x04000c0cu)) == tag;
}
__device__ __forceinline__ float val(unsigned w) { return asf(w & 0xffffff00u); }
__device__ __forceinline__ v4u ld16(__amdgpu_buffer_rsrc_t rw, int off) { return __builtin_amdgcn_raw_buffer_load_b128(rw, off, 0, 16); }
__device__ __forceinline__ void st16(__amdgpu_buffer_rsrc_t rw, int off, v4u v) { __builtin_amdgcn_raw_buffer_store_b128(v, rw, off, 0, 16); }
__device__ __forceinline__ void st8(__amdgpu_buffer_rsrc_t rw, int off, v2u v) { __builtin_amdgcn_raw_buffer_store_b64(v, rw, off, 0, 16); }

struct Stamps {
  u64 t[8];
  __device__ __forceinline__ void flush(const Params& p, int base, int n) {
#pragma unroll
    for (int i = 0; i < 8; ++i) if (i < n) ((GAS u64*)(p.ws + W_ST))[blockIdx.x * 32 + base + i] = t[i];
  }
};
#define STAMP(i) do { __builtin_amdgcn_sched_barrier(0); asm volatile("s_memrealtime %0\n\ts_waitcnt lgkmcnt(0)" : "=s"(st.t[i]) :: "memory"); __builtin_amdgcn_sched_barrier(0); } while (0)
#define STAMPS() Stamps st = {}
#define FLUSH(base, n) st.flush(p, base, n)

__device__ __forceinline__ void layer_norm(v2u (&xr)[5], LAS unsigned char* L, int lane) {
  v2f x[10];
#pragma unroll
  for (int i = 0; i < 5; ++i) { x[2 * i] = v2f{lo16(xr[i].x), hi16(xr[i].x)}; x[2 * i + 1] = v2f{lo16(xr[i].y), hi16(xr[i].y)}; }
  const float p0 = asf((unsigned)__builtin_amdgcn_readlane((int)asu(x[0].x), 0)), p1 = asf((unsigned)__builtin_amdgcn_readlane((int)asu(x[0].x), 32));
  const float piv = lane < 32 ? p0 : p1;
  v2f s1 = {0.f, 0.f}, s2 = {0.f, 0.f};
#pragma unroll
  for (int i = 0; i < 10; ++i) { const v2f d = x[i] - piv; s1 += d; s2 += d * d; }
  float a = s1.x + s1.y, q = s2.x + s2.y;
  a += dpp<XOR1>(a); q += dpp<XOR1>(q); a += dpp<XOR2>(a); q += dpp<XOR2>(q);
  a += dpp<ROR4>(a); q += dpp<ROR4>(q); a += dpp<ROR8>(a); q += dpp<ROR8>(q);
  a = fold16(a, a) * (1.0f / DM); q = fold16(q, q) * (1.0f / DM);
  const float mb = bfr(piv + a), var = bfr(fmaf(-a, a, q));
  const float den = bfr(__builtin_amdgcn_sqrtf(bfr(var + 1e-5f)));
  const float rcp = __builtin_amdgcn_rcpf(den);
#pragma unroll
  for (int i = 0; i < 5; ++i) {
    unsigned o[2];
#pragma unroll
    for (int e = 0; e < 2; ++e) {
      const v2f t = x[2 * i + e] - mb;
      const v2f d = {bfr(t.x), bfr(t.y)}, q0 = d * rcp;
      const v2f qq = (d - q0 * den) * rcp + q0;
      o[e] = bfbits(qq.x) | (bfbits(qq.y) << 16);
    }
    *(LAS v2u*)(L + L_XP + (lane >> 5) * 1280 + ((lane & 31) + 32 * i) * 8) = v2u{o[0], o[1]};
  }
}

template <int NK, int KSTEP, int HALF>
__device__ __forceinline__ void load_bt(const LAS unsigned char* b, v4u (&f)[NK]) {
#pragma unroll
  for (int ks = 0; ks < NK; ++ks) {
    const v2u lo = __builtin_bit_cast(v2u, __builtin_amdgcn_ds_read_tr16_b64_v4i16((LAS v4s*)(b + ks * KSTEP)));
    const v2u hi = __builtin_bit_cast(v2u, __builtin_amdgcn_ds_read_tr16_b64_v4i16((LAS v4s*)(b + ks * KSTEP + HALF)));
    f[ks] = v4u{lo.x, lo.y, hi.x, hi.y};
  }
}
template <int NK> __device__ __forceinline__ v4f mm2(const LAS unsigned char* a, const v4u (&f)[NK], v4f c = v4f{0.f, 0.f, 0.f, 0.f}) {
  v4u av[NK];
#pragma unroll
  for (int ks = 0; ks < NK; ++ks) av[ks] = *(const LAS v4u*)(a + ks * 64);
#pragma unroll
  for (int ks = 0; ks < NK; ++ks) c = __builtin_amdgcn_mfma_f32_16x16x32_bf16(__builtin_bit_cast(bf16x8, av[ks]), __builtin_bit_cast(bf16x8, f[ks]), c, 0, 0, 0);
  return c;
}

#define DMA(OFF) "s_waitcnt vmcnt(%4)\n\tbuffer_load_dwordx4 %0, %1, %2 offen " OFF "nt lds"
template <int N> __device__ __forceinline__ void dma3(int voff, v4u srd, int soff, unsigned lds) {
  asm volatile("s_mov_b32 m0, %3\n\t" DMA("") "\n\t" DMA("offset:1024 ") "\n\t" DMA("offset:2048 ")
               :: "v"(voff), "s"(srd), "s"(soff), "s"(lds), "i"(N) : "memory");
}
template <int N> __device__ __forceinline__ void dma1(int voff, v4u srd, int soff, unsigned lds) {
  asm volatile("s_mov_b32 m0, %3\n\t" DMA("") :: "v"(voff), "s"(srd), "s"(soff), "s"(lds), "i"(N) : "memory");
}
__device__ __forceinline__ v4u srd_of(const void* ptr, unsigned bytes) {
  const u64 a = (u64)ptr;
  return v4u{(unsigned)a, (unsigned)(a >> 32) & 0xffffu, bytes, 0x00020000u};
}
constexpr int fdiv6(int a) { return a >= 0 ? a / 6 : -((5 - a) / 6); }
constexpr int lag3(int w) { return fdiv6(3 - w); }
constexpr int lag6(int w) { return fdiv6(6 - w); }

constexpr int R0 = K_R0, A1 = K_A1, C1 = K_C1, B1 = 8 * R0 - 6 * A1 - C1;
static_assert(B1 >= A1 && C1 >= 0 && 2 * (B1 + 32 - R0) <= 16 * NC, "member 6 has no prologue; a wave keeps the scores of 16 slots");
struct Plan {
  bool mlp;
  int npro, n1;
  __device__ __forceinline__ Plan(int h, int m) {
    mlp = m == 7 && h < NMLP;
    npro = m < 6 ? NS_WQ : mlp ? NS_MLP : 0;
    n1 = h < NMLP ? (m < 6 ? A1 : m == 6 ? B1 : C1) : R0;
  }
  __device__ __forceinline__ int piece(int i, int b, int h, int m) const {
    if (b == 0 && i < n1 && h < NMLP) return m == 7 ? 6 * A1 + B1 + i : i < A1 ? 7 * i + m : 6 * A1 + i;
    const int rho = b ? i : i - n1 + R0;
    return rho * 8 + ((m + rho * K_ROT) & 7);
  }
  __device__ __forceinline__ int len0() const { return 2 * (n1 + 32 - R0); }
  __device__ __forceinline__ int ntot() const { return npro + 2 * len0() + 128; }
  __device__ __forceinline__ int wo_skip() const { const int r = ntot() % NSLOT; return r + NWO > NSLOT ? NSLOT - r : 0; }
};
__device__ __forceinline__ int piece_off(int pc, int b, int h) { return (b * NH + h) * SLAB + (((pc >> 3) * K_STR + h * K_HROT) & 31) * 98304 + (pc & 7) * 12288; }

struct Loader {
  LAS unsigned* ctl;
  unsigned lbase;
  int s, pos, limit, pub;
  unsigned next_addr;
  u64 stall = 0;
  unsigned t0 = 0;
  int due = 0, pace = 0;
  int now = 0, tend = 0, tnew = K_TEND, slast = 0, pmax = 1 << 30, last = 0, shift = 0;
  __device__ __forceinline__ void publish(int n) { pub = max(pub, n); __hip_atomic_store(ctl + C_LANDED, (unsigned)pub, RLX_WG); }
  __device__ __forceinline__ void credit(int last) {
    if (__builtin_expect(last >= limit, 0)) {
      stall -= __builtin_amdgcn_s_memrealtime();
#pragma nounroll
      for (unsigned n = 0; n < LDS_SPIN; ++n) {
        int v = (int)__hip_atomic_load((LAS unsigned*)(size_t)next_addr, RLX_WG);
        v = min(v, dppi<XOR1>(v)); v = min(v, dppi<XOR2>(v)); v = min(v, dppi<MIR8>(v));
        limit = rfl(v) + NSLOT;
        if (last < limit) break;
        if (n == 8) { asm volatile("s_waitcnt vmcnt(0)" ::: "memory"); publish(s); }
        __builtin_amdgcn_s_sleep(1);
      }
      stall += __builtin_amdgcn_s_memrealtime();
      CBAR();
    }
  }
  __device__ __forceinline__ unsigned dst() const { return lbase + pos * SLOTB; }
  __device__ __forceinline__ void advance() { ++s; pos = pos + 1 == NSLOT ? 0 : pos + 1; }
  template <int W> __device__ __forceinline__ void rows_w(int voff, v4u srd, int so) {
    int v;
    u64 t;
    asm volatile("ds_read_b32 %0, %1" : "=v"(v) : "v"(next_addr) : "memory");
    dma3<W - 1>(voff, srd, so, dst());
    asm volatile("s_waitcnt lgkmcnt(0)" : "+v"(v));
    tnew = __builtin_amdgcn_readlane(v, 8);
    v = min(v, dppi<XOR1>(v)); v = min(v, dppi<XOR2>(v));
    asm volatile("" : "+v"(v));
    if (lag3(W) == lag6(W)) publish(s + lag3(W));
    dma3<W - 1>(voff, srd, so + 3072, dst() + 3072);
    limit = rfl(min(v, dppi<MIR8>(v))) + NSLOT;
    if (lag3(W) != lag6(W)) publish(s + lag6(W));
    advance();
    dma3<W - 1>(voff, srd, so + 6144, dst());
    if (lag3(W) == lag6(W)) publish(s + lag3(W));
    dma3<W - 1>(voff, srd, so + 9216, dst() + 3072);
    asm volatile("s_memrealtime %0" : "=s"(t));
    if (lag3(W) != lag6(W)) publish(s + lag6(W));
    advance();
    asm volatile("s_waitcnt lgkmcnt(0)" : "+s"(t));
    now = (int)((unsigned)t - t0);
  }
  __device__ __forceinline__ void rows(int voff, v4u srd, int so) {
    credit(s + 1);
    if (__builtin_expect(now - last > K_STALL, 0)) { const int d = now - last - (pace >> 8); shift += d; due += d << 8; }
    if (__builtin_expect(tnew != tend, 0)) {
      tend = tnew;
      due = max(due, (now - K_LATE - K_KEEP) << 8);
      pace = min(max(((tend + shift) * 256 - due) * 2 / (slast - s), 0), pmax);
    }
#pragma nounroll
    for (unsigned n = 0; now < (due >> 8) && n < LDS_SPIN; ++n) now = clock();
    last = now;
    const int late = now - (due >> 8);
    __hip_atomic_store(ctl + C_LATE, (unsigned)late, RLX_WG);
    due += pace;
    if (late <= K_LATE) rows_w<INF>(voff, srd, so); else rows_w<K_WB>(voff, srd, so);
  }
  __device__ __forceinline__ int clock() const { return (int)((unsigned)__builtin_amdgcn_s_memrealtime() - t0); }
};

template <int NP> __device__ __forceinline__ v2f sum_producers(__amdgpu_buffer_rsrc_t rw, int base, unsigned tag, int lane) {
  const int off = base + (lane >> 2) * (DM * 8) + blockIdx.x * 64 + (lane & 3) * 16;
  const bool two = lane < 4 * (NP - 16);
  v4u a, c;
#pragma nounroll
  for (unsigned n = 0; n < GLB_SPIN; ++n) {
    CBAR();
    a = ld16(rw, off);
    c = ld16(rw, two ? off + 16 * (DM * 8) : off);
    const u64 bad = __builtin_amdgcn_ballot_w64(!(fresh(a, tag) && fresh(c, tag)));
    if (bad == 0) break;
    if (K_SL2 && __builtin_popcountll(bad) > 12) __builtin_amdgcn_s_sleep(K_SL2);
  }
  if (!two) c = v4u{0u, 0u, 0u, 0u};
  float s0 = fold32(val(a.x) + val(c.x), val(a.y) + val(c.y)), s1 = fold32(val(a.z) + val(c.z), val(a.w) + val(c.w));
  s0 = fold16(s0, s0); s1 = fold16(s1, s1);
  s0 += dpp<ROR8>(s0); s1 += dpp<ROR8>(s1);
  return v2f{bfr(s0 + dpp<ROR4>(s0)), bfr(s1 + dpp<ROR4>(s1))};
}

__device__ __forceinline__ void combine(const Params& p, LAS unsigned char* L, int lane, int b) {
  LAS unsigned* ctl = (LAS unsigned*)(L + L_CTL);
  const int h = K_XMAP ? blockIdx.x >> 3 : blockIdx.x & 31, l48 = min(lane, 47);
  const __amdgpu_buffer_rsrc_t rw = rsrc(p.ws, W_END);
  const unsigned tag = (unsigned)rfl((int)ctl[C_TAG]);
  const int off = W_OP + (b * NH + h) * 8 * 768 + l48 * 16;
  v4f g[8];
  v4u mv;
  unsigned miss = 0xffu;
#pragma unroll
  for (int k = 0; k < 8; ++k) g[k] = v4f{0.f, 0.f, 0.f, 0.f};
  if (K_D1) __builtin_amdgcn_s_sleep(K_D1);
#pragma nounroll
  for (unsigned n = 0; n < GLB_SPIN; ++n) {
    v4u t[8];
    CBAR();
    mv = ld16(rw, W_ML + (h * 8 + (lane & 7)) * 16);
#pragma unroll
    for (int k = 0; k < 8; ++k)
      if (miss >> k & 1) t[k] = ld16(rw, off + k * 768);
#pragma unroll
    for (int k = 0; k < 8; ++k)
      if (miss >> k & 1) {
        if (__all(fresh(t[k], tag))) { g[k] = v4f{val(t[k].x), val(t[k].y), val(t[k].z), val(t[k].w)}; miss ^= 1u << k; }
      }
    if (miss == 0u && __all(fresh(mv, tag))) break;
    if (K_SL1 && __builtin_popcount(miss) > K_FEW) __builtin_amdgcn_s_sleep(K_SL1);
  }
  const float Mk = val(b ? mv.z : mv.x);
  float M = Mk;
  M = fmaxf(M, dpp<XOR1>(M)); M = fmaxf(M, dpp<XOR2>(M)); M = fmaxf(M, dpp<MIR8>(M));
  float w = exp2f_(Mk - M), Ls = w * val(b ? mv.w : mv.y);
  Ls += dpp<XOR1>(Ls); Ls += dpp<XOR2>(Ls); Ls += dpp<MIR8>(Ls);
  w *= __builtin_amdgcn_rcpf(Ls);
  v4f o = {0.f, 0.f, 0.f, 0.f};
#pragma unroll
  for (int k = 0; k < 8; ++k) o += asf((unsigned)__builtin_amdgcn_readlane((int)asu(w), k)) * g[k];
  *(LAS v2u*)(L + L_OA + b * (DH * 2) + l48 * 8) = v2u{bfbits(o.x) | (bfbits(o.y) << 16), bfbits(o.z) | (bfbits(o.w) << 16)};
  lds_set(ctl + C_O + b, 1u);
}

__device__ __forceinline__ void run_loader(const Params& p, LAS unsigned char* L, int lane, u64 tentry) {
  LAS unsigned* ctl = (LAS unsigned*)(L + L_CTL);
  const int bid = blockIdx.x, h = K_XMAP ? bid >> 3 : bid & 31, m = K_XMAP ? bid & 7 : bid >> 5;
  const int r = lane >> 3, j = lane & 7;
  __builtin_amdgcn_s_setprio(3);
  STAMPS();
  Loader ld{ctl, (unsigned)(size_t)L, 0, 0, NSLOT, 0, (unsigned)(size_t)(r == 1 ? ctl + C_TEND : ctl + C_NEXT + (j < NC ? j : 0))};
  const int voff = lane * 16;
  if (__builtin_expect(m < 6, 1)) {
    const v4u rq = srd_of(p.wq, DM * NH * DH * 2);
    const int vo = r * (NH * DH * 2) + (j ^ (r & 2)) * 16, so = (m / 3) * 320 * (NH * DH * 2) + h * (DH * 2) + (m % 3) * 128;
#pragma unroll 2
    for (int g = 0; g < 6 * NS_WQ; ++g) dma1<63>(g & 1 ? vo ^ 64 : vo, rq, so + min(g, 39) * (8 * NH * DH * 2), ld.lbase + g * 1024);
    ld.s = ld.pos = NS_WQ;
  }
  asm volatile("s_waitcnt lgkmcnt(0)\n\ts_barrier" ::: "memory");
  STAMP(1);
  const Plan pl(h, m);
  ld.t0 = (unsigned)tentry;
  ld.slast = pl.ntot();
  if (pl.mlp) {
    const v4u rg = srd_of(p.wg, DM * FF * 2), ru = srd_of(p.wu, DM * FF * 2), rd = srd_of(p.wd, FF * DM * 2);
    const int vo = r * (FF * 2) + (j ^ (r & 2)) * 16;
    constexpr int W = K_WMLP;
    const int sd = h * 64 * (DM * 2);
    auto slot = [&](int pc0, int sg) {
#pragma unroll
      for (int q = 0; q < 6; ++q) {
        const int pc = pc0 + q;
        if (pc < 16 || sg >= 0) dma1<W - 1>(q & 1 ? vo ^ 64 : vo, pc & 4 ? ru : rg, (sg < 0 ? 72 : sg) * (8 * FF * 2) + ((pc >> 3) * 4 + (pc & 3)) * (8 * FF * 2) + h * 128, ld.dst() + q * 1024);
        else dma1<W - 1>(voff, rd, sd + (pc - 16) * 1024, ld.dst() + q * 1024);
      }
      ld.publish(ld.s + lag6(W));
      ld.advance();
    };
#pragma nounroll
    for (int t = 0; t < 6; ++t) {
#pragma unroll
      for (int u = 0; u < 4; ++u) slot(6 * u, 12 * t);
    }
#pragma unroll
    for (int u = 0; u < 3; ++u) { ld.credit(ld.s); slot(6 * u, -1); }
#pragma nounroll
    for (int t = 27; t < NS_MLP; ++t) {
      ld.credit(ld.s);
      dma3<W - 1>(voff, rd, sd + (6 * t - 160) * 1024, ld.dst());
      dma3<W - 1>(voff, rd, sd + (6 * t - 157) * 1024, ld.dst() + 3072);
      ld.publish(ld.s + lag6(W));
      ld.advance();
    }
  }
  STAMP(2);
  ld.now = ld.clock();
  ld.last = ld.now;
  ld.due = ld.now << 8;
  ld.tend = K_TEND;
  ld.pace = (K_TEND * 256 - ld.due) * 2 / (ld.slast - ld.s);
  ld.pmax = ld.pace + (ld.pace >> 3);
#pragma nounroll
  for (int ph = 0; ph < 4; ++ph) {
    const v4u rs = srd_of(ph & 1 ? p.v : p.k, KV_BYTES);
    const int b = ph >> 1, np = b ? 32 : pl.len0() / 2;
#pragma nounroll
    for (int i = 0; i < np; ++i) ld.rows(voff, rs, piece_off(pl.piece(i, b, h, m), b, h));
    if (ph == 0) STAMP(5); else if (ph == 1) STAMP(6); else if (ph == 2) STAMP(7);
  }
  STAMP(3);
  const v4u ro = srd_of(p.wo, NH * DH * DM * 2);
  int row = lane / 10, ch = lane - row * 10;
  ld.credit(ld.s + pl.wo_skip() + NWO - 1);
  if (pl.wo_skip()) ld.pos = 0;
#pragma nounroll
  for (int t = 0; t < NWO; ++t) {
#pragma unroll
    for (int q = 0; q < 6; ++q) {
      dma1<INF - 1>(row * (DM * 2) + ch * 16, ro, h * DH * (DM * 2) + m * 160, ld.dst() + q * 1024);
      if (q == 2 && lag3(INF) == lag6(INF)) ld.publish(ld.s + lag3(INF));
      if (q == 5 && lag3(INF) != lag6(INF)) ld.publish(ld.s + lag6(INF));
      row += 6; ch += 4;
      if (ch >= 10) { ch -= 10; row += 1; }
    }
    ld.advance();
  }
  asm volatile("s_waitcnt vmcnt(0)" ::: "memory");
  ld.publish(ld.s);
  STAMP(4);
  if (bid < NFIN) {
    const v2f y = sum_producers<NMLP>(rsrc(p.ws, W_END), W_YP, (unsigned)rfl((int)ctl[C_TAG]), lane);
    const float sg = *(const LAS float*)(L + L_G + lane * 4);
    const unsigned xn = *(const LAS unsigned*)(L + L_XP + (lane >> 5) * 1280 + bid * 16 + (lane & 3) * 4);
    *(LAS v2f*)(L + L_G + 256 + lane * 8) = v2f{bfr(siluf(bfr(lo16(xn) + bfr(y.x * sg)))), bfr(siluf(bfr(hi16(xn) + bfr(y.y * sg))))};
    lds_set(ctl + C_G, 1u);
  }
  st.t[0] = tentry; st.t[1] = tentry + ld.shift;
  FLUSH(0, 8);
}

struct Early { v2u xr[5], wr[5]; unsigned e0, xo; };
__device__ __forceinline__ void early_loads(const Params& p, Early& e, int lane) {
#pragma unroll
  for (int i = 0; i < 5; ++i) e.xr[i] = ((const GAS v2u*)p.x)[(lane >> 5) * 160 + (lane & 31) + 32 * i];
  e.e0 = __hip_atomic_load((GAS unsigned*)(p.ws + W_EPOCH), RLX_AGENT);
}
__device__ __forceinline__ void run_helper(const Params& p, LAS unsigned char* L, int lane, Early& e) {
  LAS unsigned* ctl = (LAS unsigned*)(L + L_CTL);
  const int bid = blockIdx.x, h = K_XMAP ? bid >> 3 : bid & 31, m = K_XMAP ? bid & 7 : bid >> 5;
  const int j = lane & 7;
  const __amdgpu_buffer_rsrc_t rw = rsrc(p.ws, W_END);
  STAMPS();
  const int fb = lane >> 5, col = min(bid, NFIN - 1) * 8 + 2 * (lane & 3), l48 = min(lane, 47);
#pragma unroll
  for (int i = 0; i < 5; ++i) e.wr[i] = ((const GAS v2u*)p.wr)[(lane & 31) + 32 * i];
  e.xo = ((const GAS unsigned*)p.x)[(fb * DM + col) >> 1];
  __builtin_amdgcn_sched_barrier(0);
  layer_norm(e.xr, L, lane);
  const unsigned epoch = (unsigned)rfl((int)e.e0) + 1u, tag = tag_of(epoch);
  ctl[C_TAG] = tag;
  lds_set(ctl + C_XOK, 1u);
  STAMP(0);
  {
    lds_wait<2>(ctl + C_LANDED, K_QWAIT);
    const int off = W_QP + (h * 2 * DH + 4 * l48) * 8;
    v4u g[4];
#pragma nounroll
    for (unsigned n = 0; n < GLB_SPIN; ++n) {
      CBAR();
#pragma unroll
      for (int i = 0; i < 4; ++i) g[i] = ld16(rw, off + (i & 1) * 16 + (i >> 1) * (DH * 8));
      if (__all(fresh(g[0], tag) && fresh(g[1], tag) && fresh(g[2], tag) && fresh(g[3], tag))) break;
    }
#pragma unroll
    for (int b = 0; b < 2; ++b)
      *(LAS v2u*)(L + L_QB + b * (DH * 2) + l48 * 8) = v2u{bfbits(val(g[0][b]) + val(g[2][b])) | (bfbits(val(g[0][2 + b]) + val(g[2][2 + b])) << 16),
                                                            bfbits(val(g[1][b]) + val(g[3][b])) | (bfbits(val(g[1][2 + b]) + val(g[3][2 + b])) << 16)};
    lds_set(ctl + C_Q, 1u);
  }
  float rs = 0.f;
#pragma unroll
  for (int i = 0; i < 5; ++i) {
    const v2u xx = *(const LAS v2u*)(L + L_XP + fb * 1280 + ((lane & 31) + 32 * i) * 8);
    rs = dot2(e.wr[i].y, xx.y, dot2(e.wr[i].x, xx.x, rs));
  }
  rs += dpp<XOR1>(rs); rs += dpp<XOR2>(rs); rs += dpp<ROR4>(rs); rs += dpp<ROR8>(rs);
  *(LAS float*)(L + L_G + lane * 4) = bfr(1.0f / (1.0f + expf(-bfr(fold16(rs, rs)))));
  v4u po[2], pml;
  v2f st2[2];
#pragma unroll
  for (int b = 0; b < 2; ++b) {
    lds_wait_all<NC, 16>(ctl + C_ML, j, b + 1);
    v2f ml = *(const LAS v2f*)(L + L_MLW + b * 64 + j * 8);
    if (j >= NC) ml = v2f{-3.0e38f, 0.f};
    float M = ml.x;
    M = fmaxf(M, dpp<XOR1>(M)); M = fmaxf(M, dpp<XOR2>(M)); M = fmaxf(M, dpp<MIR8>(M));
    M = val(asu(M));
    const float w = exp2f_(ml.x - M);
    *(LAS float*)(L + L_F + b * 32 + j * 4) = w;
    lds_set(ctl + C_F, b + 1);
    float Ls = w * ml.y;
    Ls += dpp<XOR1>(Ls); Ls += dpp<XOR2>(Ls); Ls += dpp<MIR8>(Ls);
    st2[b] = v2f{M, Ls};
    if (b == 0) {
      lds_wait_all<NC, 16>(ctl + C_R, j, 1);
    } else {
      pml = pack4(st2[0].x, st2[0].y, st2[1].x, st2[1].y, tag);
      asm volatile("" : "+v"(pml), "+v"(po[0]));
      lds_wait_all<NC, 0>(ctl + C_R, j, 2);
    }
    STAMP(1);
    v4f o = {0.f, 0.f, 0.f, 0.f};
#pragma unroll
    for (int c = 0; c < NC; ++c) o += *(const LAS v4f*)(L + L_REC + c * 768 + l48 * 16);
    po[b] = pack4(o.x, o.y, o.z, o.w, tag);
    if (b == 0) lds_set(ctl + C_RFREE, 1u);
  }
  if (lane < 48) {
    st16(rw, W_OP + ((NH + h) * 8 + m) * 768 + lane * 16, po[1]);
    st16(rw, W_OP + (h * 8 + m) * 768 + lane * 16, po[0]);
  }
  if (lane == 0) st16(rw, W_ML + (h * 8 + m) * 16, pml);
  lds_set(ctl + C_PUB, 1u);
  STAMP(2);
  combine(p, L, lane, 1);
  STAMP(4);
  if (bid < NFIN) {
    lds_wait_all<5, 0>(ctl + C_T3, j);
    lds_wait<0>(ctl + C_G, 1);
    const v2f gt = *(const LAS v2f*)(L + L_G + 256 + lane * 8);
    STAMP(5);
    if (K_D2) __builtin_amdgcn_s_sleep(K_D2);
    const v2f sv = sum_producers<NH>(rw, W_SP, tag, lane);
    STAMP(6);
    const unsigned ov = bfbits(lo16(e.xo) + bfr(sv.x * gt.x)) | (bfbits(hi16(e.xo) + bfr(sv.y * gt.y)) << 16);
    if ((lane & 31) < 4) ((GAS unsigned*)p.out)[(fb * DM + col) >> 1] = ov;
    if (bid == 0 && lane == 0) __hip_atomic_store((GAS unsigned*)(p.ws + W_EPOCH), epoch, RLX_AGENT);
    STAMP(7);
  }
  st.t[3] = st.t[4];
  FLUSH(8, bid < NFIN ? 8 : 5);
}

__device__ __forceinline__ void run_consumer(const Params& p, LAS unsigned char* L, int lane, int c) {
  LAS unsigned* ctl = (LAS unsigned*)(L + L_CTL);
  const int bid = blockIdx.x, h = K_XMAP ? bid >> 3 : bid & 31, m = K_XMAP ? bid & 7 : bid >> 5;
  const int r = lane >> 3, j = lane & 7;
  const Plan pl(h, m);
  const __amdgpu_buffer_rsrc_t rw = rsrc(p.ws, W_END);
  STAMPS();
  if (pl.npro == 0 || (m < 6 && c >= 4)) lds_set(ctl + C_NEXT + c, pl.npro + c);
  lds_wait<1>(ctl + C_XOK, 1);
  const unsigned tag = (unsigned)rfl((int)ctl[C_TAG]);
  const int g = lane >> 4;
  const int bq = (lane >> 4) * 1024 + ((lane & 15) >> 2) * 128 + ((2 * c + ((lane & 3) >> 1)) ^ (2 * (r & 3))) * 16 + (lane & 1) * 8;
  if (m < 6) {
    if (c < 4) {
      lds_wait<0>(ctl + C_LANDED, NS_WQ);
      v4u bf[10];
      load_bt<10, 4096, 512>(L + bq, bf);
      lds_set(ctl + C_NEXT + c, NS_WQ + c);
      const v4f qv = mm2<10>(L + L_XP + (lane & 1) * 1280 + (m / 3) * 640 + (lane >> 4) * 16, bf);
      if (lane < 16) st8(rw, W_QP + ((h * 2 + m / 3) * DH + (m % 3) * 64 + 16 * c + lane) * 8, pack2(qv.x, qv.y, tag));
    }
  } else if (pl.mlp) {
    if (c < 4) {
      const LAS unsigned char* ap = L + L_XP + (lane & 1) * 1280 + g * 16;
      v4f ga = {0.f, 0.f, 0.f, 0.f}, ua = {0.f, 0.f, 0.f, 0.f};
#pragma nounroll
      for (int t = 0; t < 20; t += 2) {
        lds_wait<1>(ctl + C_LANDED, (8 * t + 21) / 6);
        v4u gf[2], uf[2];
        load_bt<2, 8192, 512>(L + (t < 18 ? t : t - 18) * 8192 + bq, gf);
        load_bt<2, 8192, 512>(L + (t < 18 ? t : t - 18) * 8192 + 4096 + bq, uf);
        lds_set(ctl + C_NEXT + c, (8 * t + 16) / 6);
        ga = mm2<2>(ap + t * 64, gf, ga);
        ua = mm2<2>(ap + t * 64, uf, ua);
      }
      if (lane < 32) *(LAS unsigned short*)(L + L_HM + g * 128 + (16 * c + (lane & 15)) * 2) = (unsigned short)bfbits(bfr(siluf(bfr(g ? ga[1] : ga[0]))) * bfr(g ? ua[1] : ua[0]));
      lds_set(ctl + C_HM + c, 1u);
    } else {
      lds_set(ctl + C_NEXT + c, 160 / 6);
    }
    lds_wait_all<4, 1>(ctl + C_HM, j);
    lds_wait<1>(ctl + C_LANDED, NS_MLP);
#pragma nounroll
    for (int n = c; n < DM / 16; n += NC) {
      v4u bf[2];
      load_bt<2, 32 * 1280, 4 * 1280>(L + (160 * 1024 - RINGB) + (8 * g + ((lane & 15) >> 2)) * 1280 + n * 32 + (lane & 3) * 8, bf);
      const v4f y = mm2<2>(L + L_HM + (lane & 1) * 128 + g * 16, bf);
      if (lane < 16) st8(rw, W_YP + (h * DM + n * 16 + lane) * 8, pack2(y[0], y[1], tag));
    }
    lds_set(ctl + C_NEXT + c, NS_MLP + c);
  }
  STAMP(0);
  const int ntot = pl.ntot();
  int s = pl.npro + c;
  auto take = [&](int i, int len) -> const LAS unsigned char* {
    lds_wait<1>(ctl + C_LANDED, s + 1);
    const LAS unsigned char* sp = L + s % NSLOT * SLOTB;
    s += i + NC < len ? NC : len - i + c;
    return sp;
  };
  auto fetch = [&](v4u (&kk)[6], int i, int len) {
    const LAS unsigned char* sp = take(i, len) + r * 384 + j * 16;
#pragma unroll
    for (int u = 0; u < 6; ++u) kk[u] = *(const LAS v4u*)(sp + (u / 3) * 3072 + (u % 3) * 128);
    lds_set(ctl + C_NEXT + c, s);
  };
  v4u e0[6], e1[6];
  fetch(e0, c, pl.len0());
  fetch(e1, c + NC, pl.len0());
  lds_wait<1>(ctl + C_Q, 1);
  STAMP(1);
  LAS unsigned char* pw = L + L_P + c * 512;
#pragma nounroll
  for (int b = 0; b < 2; ++b) {
    const int len = b ? 64 : pl.len0();
    v4u q[3];
#pragma unroll
    for (int u = 0; u < 3; ++u) q[u] = *(const LAS v4u*)(L + L_QB + (b * DH + (j + 8 * u) * 8) * 2);
    float sc[4] = {-3.0e38f, -3.0e38f, -3.0e38f, -3.0e38f};
    auto score = [&](const v4u (&kk)[6], int it) {
      float d0 = 0.f, d1 = 0.f;
#pragma unroll
      for (int u = 0; u < 3; ++u)
#pragma unroll
        for (int e = 0; e < 4; ++e) { d0 = dot2(kk[u][e], q[u][e], d0); d1 = dot2(kk[3 + u][e], q[u][e], d1); }
      d0 += dpp<XOR1>(d0); d1 += dpp<XOR1>(d1); d0 += dpp<XOR2>(d0); d1 += dpp<XOR2>(d1);
      const float d = (j & 4 ? d1 : d0) + dpp<MIR8>(j & 4 ? d0 : d1);
      const float t = bfr(bfr(d) * 0.07216878364870322f) * LOG2E;
#pragma unroll
      for (int k = 0; k < 4; ++k) sc[k] = it == 4 * k + (j & 3) ? t : sc[k];
    };
    int i = c, it = 0;
    if (b == 0) { score(e0, 0); score(e1, 1); i += 2 * NC; it = 2; }
#pragma nounroll
    for (; i < len; i += NC, ++it) {
      v4u kk[6];
      fetch(kk, i, len);
      score(kk, it);
    }
    float M = fmaxf(fmaxf(sc[0], sc[1]), fmaxf(sc[2], sc[3]));
    M = fmaxf(M, dpp<XOR1>(M)); M = fmaxf(M, dpp<XOR2>(M)); M = fmaxf(M, dpp<ROR4>(M)); M = fmaxf(M, dpp<ROR8>(M));
    M = max_rows(M);
    float ls = 0.f;
#pragma unroll
    for (int k = 0; k < 4; ++k) {
      const float pr = exp2f_(sc[k] - M);
      ls += pr;
      *(LAS unsigned short*)(pw + k * 128 + (j & 3) * 32 + (r + 8 * (j >> 2)) * 2) = (unsigned short)bfbits(pr);
    }
    ls = wave_sum(ls);
    *(LAS v2f*)(L + L_MLW + b * 64 + c * 8) = v2f{M, ls};
    lds_set(ctl + C_ML + c, b + 1);
    v4f acc[12];
#pragma unroll
    for (int t = 0; t < 12; ++t) acc[t] = v4f{0.f, 0.f, 0.f, 0.f};
    float f = 0.f;
#pragma nounroll
    for (i = c, it = 0; i < len; i += NC, ++it) {
      if (i + NC >= len) {
        lds_wait<1>(ctl + C_F, b + 1);
        if (b) lds_wait<1>(ctl + C_RFREE, 1);
        f = *(const LAS float*)(L + L_F + b * 32 + c * 4);
      }
      const LAS unsigned char* sp = take(i, len) + (lane >> 2) * 384 + (lane & 3) * 8;
      const v4s pa = *(const LAS v4s*)(pw + it * 32 + g * 8);
      v4s vf[12];
#pragma unroll
      for (int t = 0; t < 12; ++t) vf[t] = __builtin_amdgcn_ds_read_tr16_b64_v4i16((LAS v4s*)(sp + t * 32));
      lds_set(ctl + C_NEXT + c, s);
#pragma unroll
      for (int t = 0; t < 12; ++t) acc[t] = __builtin_amdgcn_mfma_f32_16x16x16bf16_1k(pa, vf[t], acc[t], 0, 0, 0);
    }
    if (b) STAMP(3); else STAMP(2);
#pragma unroll
    for (int k = 0; k < 3; ++k) {
      const float v = g & 2 ? (g & 1 ? acc[9 + k][0] : acc[6 + k][0]) : (g & 1 ? acc[3 + k][0] : acc[k][0]);
      *(LAS float*)(L + L_REC + c * 768 + (g * 48 + k * 16 + (lane & 15)) * 4) = v * f;
    }
    lds_set(ctl + C_R + c, b + 1);
  }
  STAMP(4);
  if (c == 5) {
    lds_wait<0>(ctl + C_PUB, 1);
    combine(p, L, lane, 0);
    return;
  }
  lds_wait<0>(ctl + C_LANDED, ntot + NWO);
  v4u bf[6];
  load_bt<6, 32 * 160, 4 * 160>(L + (ntot + pl.wo_skip()) % NSLOT * SLOTB + ((lane >> 4) * 8 + ((lane & 15) >> 2)) * 160 + c * 32 + (lane & 3) * 8, bf);
  STAMP(5);
  lds_wait_all<2, 0>(ctl + C_O, j);
  STAMP(6);
  const v4f sv = mm2<6>(L + L_OA + (lane & 1) * (DH * 2) + (lane >> 4) * 16, bf);
  if (lane < 16) st8(rw, W_SP + (h * DM + m * 80 + c * 16 + lane) * 8, pack2(sv.x, sv.y, tag));
  lds_set(ctl + C_T3 + c, 1u);
  STAMP(7);
  if (c == 0) FLUSH(16, 8);
  if (c == 4) FLUSH(24, 8);
}

__device__ __forceinline__ void run_pacer(const Params& p, LAS unsigned char* L, int lane, u64 tentry) {
  LAS unsigned* ctl = (LAS unsigned*)(L + L_CTL);
  const int bid = blockIdx.x;
  const __amdgpu_buffer_rsrc_t rw = rsrc(p.ws, W_END);
  auto until = [&](int t) {
    int now = 0;
#pragma nounroll
    for (unsigned n = 0; n < LDS_SPIN; ++n) {
      now = (int)((unsigned)__builtin_amdgcn_s_memrealtime() - (unsigned)tentry);
      if (now >= t) break;
      __builtin_amdgcn_s_sleep(3);
    }
    return now;
  };
  lds_wait<16>(ctl + C_XOK, 1);
  const unsigned tag = (unsigned)rfl((int)ctl[C_TAG]);
  float tend = K_TEND + 1, base = K_BASE;
  until(K_BASE);
  lds_set(ctl + C_TEND, K_TEND + 1);
#pragma nounroll
  for (int c = 0; c < K_NCP; ++c) {
    const int t = K_CP0 + c * K_CP;
    if (until(t) > t + K_CPD / 2) continue;
    if ((bid & 96) == 0 && lane == 0) st8(rw, W_PC + c * 512 + ((bid & 31) | (bid >> 7) << 5) * 8, pack2((float)(int)__hip_atomic_load(ctl + C_LATE, RLX_WG), tend, tag));
    until(t + K_CPD);
    const v2u g = __builtin_amdgcn_raw_buffer_load_b64(rw, W_PC + c * 512 + lane * 8, 0, 16);
    const bool ok = __builtin_amdgcn_perm(g.y, g.x, 0x0c0c0400u) == (tag & 0xffffu);
    const float n = (float)__builtin_popcountll(__builtin_amdgcn_ballot_w64(ok));
    float tm = ok ? val(g.y) : 0.f;
    tm = fmaxf(tm, dpp<XOR1>(tm)); tm = fmaxf(tm, dpp<XOR2>(tm)); tm = fmaxf(tm, dpp<ROR4>(tm)); tm = fmaxf(tm, dpp<ROR8>(tm));
    tend = fmaxf(tend, max_rows(tm));
    const float lam = ok ? val(g.x) : 0.f, inv = 1.f / fmaxf(n, 1.f);
    const float late = fmaxf(wave_sum(lam) * inv - K_DEAD, K_XW * wave_sum(fmaxf(lam - K_L0, 0.f)) * inv - K_XD);
    const float step = fminf(K_STEP, late * fmaxf(K_GMIN, K_GAIN * (tend - t) / fmaxf(t - base, K_FLOOR)));
    if (step > K_MIN && n >= 48.f) { tend = floorf(tend + step); base = t + K_CPD; }
    lds_set(ctl + C_TEND, (unsigned)rfl((int)tend));
    if (bid == 0) { ((GAS float*)(p.ws + W_ST + (512 << 10)))[c * 2] = late; ((GAS float*)(p.ws + W_ST + (512 << 10)))[c * 2 + 1] = tend - K_TEND; }
  }
}

__global__ void __launch_bounds__(NTHR, 3) fused_decode(Params p) {
  extern __shared__ __attribute__((aligned(16))) unsigned char smem[];
  const u64 tentry = __builtin_amdgcn_s_memrealtime();
  LAS unsigned char* L = (LAS unsigned char*)smem;
  const int tid = threadIdx.x, lane = tid & 63, wave = rfl(tid >> 6);
  if (tid < 80) ((LAS unsigned*)(L + L_CTL))[tid] = tid == C_TEND ? K_TEND : 0u;
  Early e;
  if (wave == NC + 1) early_loads(p, e, lane);
  int role = wave;
  asm volatile("" : "+s"(role));
  if (role > 0) {
    asm volatile("s_waitcnt lgkmcnt(0)\n\ts_barrier" ::: "memory");
    if (wave <= NC) run_consumer(p, L, lane, wave - 1);
    else if (wave == NC + 1) run_helper(p, L, lane, e);
    else run_pacer(p, L, lane, tentry);
  } else {
    run_loader(p, L, lane, tentry);
  }
}

}

namespace {
__global__ void k_rep(Params p) {
  const u64 now = __builtin_amdgcn_s_memrealtime();
  const GAS u64* st = (const GAS u64*)(p.ws + W_ST);
  const int l = threadIdx.x;
  u64 t0 = ~0ull, mx = 0, sum = 0;
  for (int i = l; i < 256; i += 64) { const u64 d = st[i * 32]; t0 = d < t0 ? d : t0; }
  for (int o = 32; o; o >>= 1) { const u64 b = __shfl_xor(t0, o); t0 = b < t0 ? b : t0; }
  for (int i = l; i < NWGS; i += 64) { const u64 d = st[i * 32 + IDX] - t0; mx = d > mx ? d : mx; sum += d; }
  for (int o = 32; o; o >>= 1) { const u64 b = __shfl_xor(mx, o); mx = b > mx ? b : mx; sum += __shfl_xor(sum, o); }
  u64 v = MODE == 0 ? mx : MODE == 1 ? sum / NWGS : MODE == 2 ? now - t0 : (u64)(((const GAS float*)(p.ws + W_ST + (512 << 10)))[PIDX] + 100.f);
  if (v > 20000) v = 20000;
  while (__builtin_amdgcn_s_memrealtime() - now < 1000 + SCALE * v) __builtin_amdgcn_s_sleep(8);
}
}
extern "C" void kernel_launch(void* const* d_in, const int* in_sizes, int n_in, void* d_out, int out_size, void* d_ws,
                              size_t ws_size, hipStream_t stream) {
  Params p;
  p.x = (const unsigned short*)d_in[0];
  p.k = (const unsigned short*)d_in[1];
  p.v = (const unsigned short*)d_in[2];
  p.wq = (const unsigned short*)d_in[3];
  p.wo = (const unsigned short*)d_in[4];
  p.wr = (const unsigned short*)d_in[6];
  p.wg = (const unsigned short*)d_in[7];
  p.wu = (const unsigned short*)d_in[8];
  p.wd = (const unsigned short*)d_in[9];
  p.out = (unsigned short*)d_out;
  p.ws = (unsigned char*)d_ws;
  (void)hipFuncSetAttribute((const void*)fused_decode, hipFuncAttributeMaxDynamicSharedMemorySize, L_END);
  fused_decode<<<NWG, NTHR, L_END, stream>>>(p);
  k_rep<<<1, 64, 0, stream>>>(p);
}
```

```cpp
#define PIDX 2
#define K_STAMPS 1
#define IDX 2
#define MODE 1
#define SCALE 10
#define NWGS 256
#include <hip/hip_runtime.h>
#include <hip/hip_bf16.h>

namespace {

#define GAS __attribute__((address_space(1)))
#define LAS __attribute__((address_space(3)))
#define RLX_AGENT __ATOMIC_RELAXED, __HIP_MEMORY_SCOPE_AGENT
#define RLX_WG __ATOMIC_RELAXED, __HIP_MEMORY_SCOPE_WORKGROUP
typedef unsigned v4u __attribute__((ext_vector_type(4)));
typedef unsigned v2u __attribute__((ext_vector_type(2)));
typedef float v4f __attribute__((ext_vector_type(4)));
typedef float v2f __attribute__((ext_vector_type(2)));
typedef short v4s __attribute__((ext_vector_type(4)));
typedef __bf16 bf16x2 __attribute__((ext_vector_type(2)));
typedef __bf16 bf16x8 __attribute__((ext_vector_type(8)));
typedef unsigned long long u64;

#define K_INF 21
#define K_NSLOT 24
#define K_A1 10
#define K_C1 1
#define K_WMLP 33
#define K_HROT 1
#define K_XMAP 0
#define K_STR 21
#define K_ROT 1
#define K_FEW 2
#define K_SL1 8
#define K_SL2 8
#define K_TEND 5380
#define K_LATE 40
#define K_WB 33
#define K_D1 12
#define K_D2 12
#define K_NCP 14
#define K_CP0 1625
#define K_CP 275
#define K_CPD 125
#define K_L0 60.f
#define K_GMIN 3.f
#define K_STEP 200.f
#define K_KEEP 0
#define K_XW 2.f
#define K_XD 6.f
#define K_BASE 1200
#define K_FLOOR 600.f
#define K_MIN 8.f
#define K_STALL 160
#define K_GAIN 0.6f
#define K_DEAD 50.f
#define K_R0 9
#define K_TOUCH 1
#define K_TMASK 7
#define K_T1 300
#define K_TC2 3
#define K_TC3 8
#define K_TC4 12
#define K_EARLY0 0
#define K_STAG 0
#define K_CL 9
#define K_GL 5.f
#define K_NEND 0
#define K_LATE2 15
#define K_WWO K_INF
#define K_NS 32
#define K_YLATE 2
#define K_QWAIT 22

constexpr int DM = 640, NH = 32, DH = 192, SK = 8192, FF = 1536;
constexpr int NWG = 256, NC = 6, NTHR = (NC + 3) * 64;
constexpr int INF = K_INF;
constexpr int NSLOT = K_NSLOT;
constexpr int SLOTB = 6144, RINGB = NSLOT * SLOTB, NWO = 5, NS_WQ = 7, NS_MLP = 40;
constexpr unsigned KV_BYTES = 2u * NH * SK * DH * 2u;
constexpr int SLAB = SK * DH * 2;
constexpr int NMLP = FF / 64;
constexpr int NFIN = DM / 8;
static_assert(NSLOT == 24, "the MLP images rely on where the ring wraps");
static_assert(INF >= 7 && INF <= 30 && K_WB <= 60, "vmcnt is 6 bits; the last K/V slots are published while wo is requested");

constexpr int L_XP = NSLOT * SLOTB;
constexpr int L_REC = L_XP + 2560;
constexpr int L_QB = L_REC + 4608;
constexpr int L_OA = L_QB + 768;
constexpr int L_HM = L_OA + 768;
constexpr int L_P = L_HM + 256;
constexpr int L_MLW = L_P + 3072;
constexpr int L_F = L_MLW + 128;
constexpr int L_G = L_F + 64;
constexpr int L_CTL = L_G + 768;
constexpr int L_END = L_CTL + 320;
static_assert(L_END <= 160 * 1024 && L_END > 80 * 1024, "one workgroup per CU");
enum { C_NEXT = 0, C_HM = 16, C_ML = 24, C_R = 32, C_T3 = 48, C_O = 56, C_LANDED = 64, C_XOK, C_TAG, C_Q, C_F, C_RFREE, C_G, C_PUB, C_LATE, C_TEND };

constexpr int W_EPOCH = 0;
constexpr int W_ML = 1024;
constexpr int W_OP = W_ML + 2 * NH * 8 * 16;
constexpr int W_SP = W_OP + 2 * NH * 8 * 768;
constexpr int W_YP = W_SP + NH * DM * 8;
constexpr int W_QP = W_YP + NMLP * DM * 8;
constexpr int W_PC = W_QP + NH * 2 * DH * 8;
constexpr int W_END = W_PC + 24 * 512;
constexpr int W_ST = 4 << 20;

constexpr unsigned LDS_SPIN = 1u << 20, GLB_SPIN = 1u << 16;
constexpr float LOG2E = 1.4426950408889634f;

struct Params {
  const unsigned short *x, *k, *v, *wq, *wo, *wr, *wg, *wu, *wd;
  unsigned short* out;
  unsigned char* ws;
};

__device__ __forceinline__ int rfl(int v) { return __builtin_amdgcn_readfirstlane(v); }
__device__ __forceinline__ float bfr(float x) { return (float)(__bf16)x; }
__device__ __forceinline__ unsigned bfbits(float x) { return (unsigned)__builtin_bit_cast(unsigned short, (__bf16)x); }
__device__ __forceinline__ float lo16(unsigned u) { return __builtin_bit_cast(float, u << 16); }
__device__ __forceinline__ float hi16(unsigned u) { return __builtin_bit_cast(float, u & 0xffff0000u); }
__device__ __forceinline__ float asf(unsigned u) { return __builtin_bit_cast(float, u); }
__device__ __forceinline__ unsigned asu(float f) { return __builtin_bit_cast(unsigned, f); }
__device__ __forceinline__ float dot2(unsigned a, unsigned b, float c) {
  return __builtin_amdgcn_fdot2_f32_bf16(__builtin_bit_cast(bf16x2, a), __builtin_bit_cast(bf16x2, b), c, false);
}
template <int CTRL> __device__ __forceinline__ float dpp(float x) {
  return __builtin_bit_cast(float, __builtin_amdgcn_mov_dpp(__builtin_bit_cast(int, x), CTRL, 0xf, 0xf, true));
}
template <int CTRL> __device__ __forceinline__ int dppi(int x) { return __builtin_amdgcn_mov_dpp(x, CTRL, 0xf, 0xf, true); }
constexpr int XOR1 = 0xB1, XOR2 = 0x4E, MIR8 = 0x141, ROR4 = 0x124, ROR8 = 0x128;
__device__ __forceinline__ float fold32(float a, float b) {
  auto s = __builtin_amdgcn_permlane32_swap(asu(a), asu(b), false, false);
  return asf(s[0]) + asf(s[1]);
}
__device__ __forceinline__ float fold16(float a, float b) {
  auto s = __builtin_amdgcn_permlane16_swap(asu(a), asu(b), false, false);
  return asf(s[0]) + asf(s[1]);
}
__device__ __forceinline__ float max_rows(float x) {
  auto s = __builtin_amdgcn_permlane16_swap(asu(x), asu(x), false, false);
  x = fmaxf(asf(s[0]), asf(s[1]));
  auto t = __builtin_amdgcn_permlane32_swap(asu(x), asu(x), false, false);
  return fmaxf(asf(t[0]), asf(t[1]));
}
__device__ __forceinline__ float sum_rows(float x) { x = fold16(x, x); return fold32(x, x); }
__device__ __forceinline__ float wave_sum(float x) {
  x += dpp<XOR1>(x); x += dpp<XOR2>(x); x += dpp<ROR4>(x); x += dpp<ROR8>(x);
  return sum_rows(x);
}
__device__ __forceinline__ float exp2f_(float x) { return __builtin_amdgcn_exp2f(x); }
__device__ __forceinline__ float siluf(float x) { return x / (1.0f + expf(-x)); }
#define CBAR() asm volatile("" ::: "memory")

__device__ __forceinline__ __amdgpu_buffer_rsrc_t rsrc(const void* p, unsigned bytes) {
  return __builtin_amdgcn_make_buffer_rsrc((void*)p, 0, bytes, 0x00020000);
}
__device__ __forceinline__ void lds_set(LAS unsigned* w, unsigned v) {
  CBAR();
  __hip_atomic_store(w, v, RLX_WG);
  CBAR();
}
template <int SLEEP> __device__ __forceinline__ void lds_wait(LAS unsigned* w, unsigned want) {
#pragma nounroll
  for (unsigned n = 0; n < LDS_SPIN; ++n) {
    if ((unsigned)rfl((int)__hip_atomic_load(w, RLX_WG)) >= want) break;
    if (SLEEP) __builtin_amdgcn_s_sleep(SLEEP);
  }
  CBAR();
}
template <int N, int SLEEP> __device__ __forceinline__ void lds_wait_all(LAS unsigned* w, int j, unsigned want = 1u) {
  LAS unsigned* a = w + (j < N ? j : 0);
#pragma nounroll
  for (unsigned n = 0; n < LDS_SPIN; ++n) {
    if (__all(__hip_atomic_load(a, RLX_WG) >= want)) break;
    if (SLEEP) __builtin_amdgcn_s_sleep(SLEEP);
  }
  CBAR();
}
__device__ __forceinline__ unsigned tag_of(unsigned e) { return ((e & 0xffu) | (((e >> 8) ^ e) & 0xffu) << 8) * 0x10001u; }
__device__ __forceinline__ v2u pack2(float a, float b, unsigned tag) {
  return v2u{__builtin_amdgcn_perm(asu(a), tag, 0x07060500u), __builtin_amdgcn_perm(asu(b), tag, 0x07060501u)};
}
__device__ __forceinline__ v4u pack4(float a, float b, float c, float d, unsigned tag) {
  const v2u x = pack2(a, b, tag), y = pack2(c, d, tag);
  return v4u{x.x, x.y, y.x, y.y};
}
__device__ __forceinline__ bool fresh(v4u g, unsigned tag) {
  return (__builtin_amdgcn_perm(g.y, g.x, 0x0c0c0400u) | __builtin_amdgcn_perm(g.w, g.z, 0x04000c0cu)) == tag;
}
__device__ __forceinline__ float val(unsigned w) { return asf(w & 0xffffff00u); }
__device__ __forceinline__ v4u ld16(__amdgpu_buffer_rsrc_t rw, int off) { return __builtin_amdgcn_raw_buffer_load_b128(rw, off, 0, 16); }
__device__ __forceinline__ void st16(__amdgpu_buffer_rsrc_t rw, int off, v4u v) { __builtin_amdgcn_raw_buffer_store_b128(v, rw, off, 0, 16); }
__device__ __forceinline__ void st8(__amdgpu_buffer_rsrc_t rw, int off, v2u v) { __builtin_amdgcn_raw_buffer_store_b64(v, rw, off, 0, 16); }

struct Stamps {
  u64 t[8];
  __device__ __forceinline__ void flush(const Params& p, int base, int n) {
#pragma unroll
    for (int i = 0; i < 8; ++i) if (i < n) ((GAS u64*)(p.ws + W_ST))[blockIdx.x * 32 + base + i] = t[i];
  }
};
#define STAMP(i) do { __builtin_amdgcn_sched_barrier(0); asm volatile("s_memrealtime %0\n\ts_waitcnt lgkmcnt(0)" : "=s"(st.t[i]) :: "memory"); __builtin_amdgcn_sched_barrier(0); } while (0)
#define STAMPS() Stamps st = {}
#define FLUSH(base, n) st.flush(p, base, n)

__device__ __forceinline__ void layer_norm(v2u (&xr)[5], LAS unsigned char* L, int lane) {
  v2f x[10];
#pragma unroll
  for (int i = 0; i < 5; ++i) { x[2 * i] = v2f{lo16(xr[i].x), hi16(xr[i].x)}; x[2 * i + 1] = v2f{lo16(xr[i].y), hi16(xr[i].y)}; }
  const float p0 = asf((unsigned)__builtin_amdgcn_readlane((int)asu(x[0].x), 0)), p1 = asf((unsigned)__builtin_amdgcn_readlane((int)asu(x[0].x), 32));
  const float piv = lane < 32 ? p0 : p1;
  v2f s1 = {0.f, 0.f}, s2 = {0.f, 0.f};
#pragma unroll
  for (int i = 0; i < 10; ++i) { const v2f d = x[i] - piv; s1 += d; s2 += d * d; }
  float a = s1.x + s1.y, q = s2.x + s2.y;
  a += dpp<XOR1>(a); q += dpp<XOR1>(q); a += dpp<XOR2>(a); q += dpp<XOR2>(q);
  a += dpp<ROR4>(a); q += dpp<ROR4>(q); a += dpp<ROR8>(a); q += dpp<ROR8>(q);
  a = fold16(a, a) * (1.0f / DM); q = fold16(q, q) * (1.0f / DM);
  const float mb = bfr(piv + a), var = bfr(fmaf(-a, a, q));
  const float den = bfr(__builtin_amdgcn_sqrtf(bfr(var + 1e-5f)));
  const float rcp = __builtin_amdgcn_rcpf(den);
#pragma unroll
  for (int i = 0; i < 5; ++i) {
    unsigned o[2];
#pragma unroll
    for (int e = 0; e < 2; ++e) {
      const v2f t = x[2 * i + e] - mb;
      const v2f d = {bfr(t.x), bfr(t.y)}, q0 = d * rcp;
      const v2f qq = (d - q0 * den) * rcp + q0;
      o[e] = bfbits(qq.x) | (bfbits(qq.y) << 16);
    }
    *(LAS v2u*)(L + L_XP + (lane >> 5) * 1280 + ((lane & 31) + 32 * i) * 8) = v2u{o[0], o[1]};
  }
}

template <int NK, int KSTEP, int HALF>
__device__ __forceinline__ void load_bt(const LAS unsigned char* b, v4u (&f)[NK]) {
#pragma unroll
  for (int ks = 0; ks < NK; ++ks) {
    const v2u lo = __builtin_bit_cast(v2u, __builtin_amdgcn_ds_read_tr16_b64_v4i16((LAS v4s*)(b + ks * KSTEP)));
    const v2u hi = __builtin_bit_cast(v2u, __builtin_amdgcn_ds_read_tr16_b64_v4i16((LAS v4s*)(b + ks * KSTEP + HALF)));
    f[ks] = v4u{lo.x, lo.y, hi.x, hi.y};
  }
}
template <int NK> __device__ __forceinline__ v4f mm2(const LAS unsigned char* a, const v4u (&f)[NK], v4f c = v4f{0.f, 0.f, 0.f, 0.f}) {
  v4u av[NK];
#pragma unroll
  for (int ks = 0; ks < NK; ++ks) av[ks] = *(const LAS v4u*)(a + ks * 64);
#pragma unroll
  for (int ks = 0; ks < NK; ++ks) c = __builtin_amdgcn_mfma_f32_16x16x32_bf16(__builtin_bit_cast(bf16x8, av[ks]), __builtin_bit_cast(bf16x8, f[ks]), c, 0, 0, 0);
  return c;
}

#define DMA(OFF) "s_waitcnt vmcnt(%4)\n\tbuffer_load_dwordx4 %0, %1, %2 offen " OFF "nt lds"
template <int N> __device__ __forceinline__ void dma3(int voff, v4u srd, int soff, unsigned lds) {
  asm volatile("s_mov_b32 m0, %3\n\t" DMA("") "\n\t" DMA("offset:1024 ") "\n\t" DMA("offset:2048 ")
               :: "v"(voff), "s"(srd), "s"(soff), "s"(lds), "i"(N) : "memory");
}
template <int N> __device__ __forceinline__ void dma1(int voff, v4u srd, int soff, unsigned lds) {
  asm volatile("s_mov_b32 m0, %3\n\t" DMA("") :: "v"(voff), "s"(srd), "s"(soff), "s"(lds), "i"(N) : "memory");
}
__device__ __forceinline__ v4u srd_of(const void* ptr, unsigned bytes) {
  const u64 a = (u64)ptr;
  return v4u{(unsigned)a, (unsigned)(a >> 32) & 0xffffu, bytes, 0x00020000u};
}
constexpr int fdiv6(int a) { return a >= 0 ? a / 6 : -((5 - a) / 6); }
constexpr int lag3(int w) { return fdiv6(3 - w); }
constexpr int lag6(int w) { return fdiv6(6 - w); }

constexpr int R0 = K_R0, A1 = K_A1, C1 = K_C1, B1 = 8 * R0 - 6 * A1 - C1;
static_assert(B1 >= A1 && C1 >= 0 && 2 * (B1 + 32 - R0) <= 16 * NC, "member 6 has no prologue; a wave keeps the scores of 16 slots");
struct Plan {
  bool mlp;
  int npro, n1;
  __device__ __forceinline__ Plan(int h, int m) {
    mlp = m == 7 && h < NMLP;
    npro = m < 6 ? NS_WQ : mlp ? NS_MLP : 0;
    n1 = h < NMLP ? (m < 6 ? A1 : m == 6 ? B1 : C1) : R0;
  }
  __device__ __forceinline__ int piece(int i, int b, int h, int m) const {
    if (b == 0 && i < n1 && h < NMLP) return m == 7 ? 6 * A1 + B1 + i : i < A1 ? 7 * i + m : 6 * A1 + i;
    const int rho = b ? i : i - n1 + R0;
    return rho * 8 + ((m + rho * K_ROT) & 7);
  }
  __device__ __forceinline__ int len0() const { return 2 * (n1 + 32 - R0); }
  __device__ __forceinline__ int ntot() const { return npro + 2 * len0() + 128; }
  __device__ __forceinline__ int wo_skip() const { const int r = ntot() % NSLOT; return r + NWO > NSLOT ? NSLOT - r : 0; }
};
__device__ __forceinline__ int piece_off(int pc, int b, int h) { return (b * NH + h) * SLAB + (((pc >> 3) * K_STR + h * K_HROT) & 31) * 98304 + (pc & 7) * 12288; }

struct Loader {
  LAS unsigned* ctl;
  unsigned lbase;
  int s, pos, limit, pub;
  unsigned next_addr;
  u64 stall = 0;
  unsigned t0 = 0;
  int due = 0, pace = 0;
  int now = 0, tend = 0, tnew = K_TEND, slast = 0, pmax = 1 << 30, last = 0, shift = 0;
  __device__ __forceinline__ void publish(int n) { pub = max(pub, n); __hip_atomic_store(ctl + C_LANDED, (unsigned)pub, RLX_WG); }
  __device__ __forceinline__ void credit(int last) {
    if (__builtin_expect(last >= limit, 0)) {
#pragma nounroll
      for (unsigned n = 0; n < LDS_SPIN; ++n) {
        int v = (int)__hip_atomic_load((LAS unsigned*)(size_t)next_addr, RLX_WG);
        v = min(v, dppi<XOR1>(v)); v = min(v, dppi<XOR2>(v)); v = min(v, dppi<MIR8>(v));
        limit = rfl(v) + NSLOT;
        if (last < limit) break;
        if (n == 8) { asm volatile("s_waitcnt vmcnt(0)" ::: "memory"); publish(s); }
        __builtin_amdgcn_s_sleep(1);
      }
      CBAR();
    }
  }
  __device__ __forceinline__ unsigned dst() const { return lbase + pos * SLOTB; }
  __device__ __forceinline__ void advance() { ++s; pos = pos + 1 == NSLOT ? 0 : pos + 1; }
  template <int W> __device__ __forceinline__ void rows_w(int voff, v4u srd, int so) {
    int v;
    u64 t;
    asm volatile("ds_read_b32 %0, %1" : "=v"(v) : "v"(next_addr) : "memory");
    dma3<W - 1>(voff, srd, so, dst());
    asm volatile("s_waitcnt lgkmcnt(0)" : "+v"(v));
    tnew = __builtin_amdgcn_readlane(v, 8);
    v = min(v, dppi<XOR1>(v)); v = min(v, dppi<XOR2>(v));
    asm volatile("" : "+v"(v));
    if (lag3(W) == lag6(W)) publish(s + lag3(W));
    dma3<W - 1>(voff, srd, so + 3072, dst() + 3072);
    limit = rfl(min(v, dppi<MIR8>(v))) + NSLOT;
    if (lag3(W) != lag6(W)) publish(s + lag6(W));
    advance();
    dma3<W - 1>(voff, srd, so + 6144, dst());
    if (lag3(W) == lag6(W)) publish(s + lag3(W));
    dma3<W - 1>(voff, srd, so + 9216, dst() + 3072);
    asm volatile("s_memrealtime %0" : "=s"(t));
    if (lag3(W) != lag6(W)) publish(s + lag6(W));
    advance();
    asm volatile("s_waitcnt lgkmcnt(0)" : "+s"(t));
    now = (int)((unsigned)t - t0);
  }
  __device__ __forceinline__ void rows(int voff, v4u srd, int so) {
    credit(s + 1);
    if (__builtin_expect(now - last > K_STALL, 0)) { const int d = now - last - (pace >> 8); shift += d; due += d << 8; stall = last; }
    if (__builtin_expect(tnew != tend, 0)) {
      tend = tnew;
      due = max(due, (now - K_LATE - K_KEEP) << 8);
      pace = min(max(((tend + shift) * 256 - due) * 2 / (slast - s), 0), pmax);
    }
#pragma nounroll
    for (unsigned n = 0; now < (due >> 8) && n < LDS_SPIN; ++n) now = clock();
    last = now;
    const int late = now - (due >> 8);
    __hip_atomic_store(ctl + C_LATE, (unsigned)late, RLX_WG);
    due += pace;
    if (late <= (s + K_NEND < slast ? K_LATE : K_LATE2)) rows_w<INF>(voff, srd, so); else rows_w<K_WB>(voff, srd, so);
  }
  __device__ __forceinline__ int clock() const { return (int)((unsigned)__builtin_amdgcn_s_memrealtime() - t0); }
};

template <int NP> __device__ __forceinline__ v2f sum_producers(__amdgpu_buffer_rsrc_t rw, int base, unsigned tag, int lane) {
  const int off = base + (lane >> 2) * (DM * 8) + blockIdx.x * 64 + (lane & 3) * 16;
  const bool two = lane < 4 * (NP - 16);
  v4u a, c;
#pragma nounroll
  for (unsigned n = 0; n < GLB_SPIN; ++n) {
    CBAR();
    a = ld16(rw, off);
    c = ld16(rw, two ? off + 16 * (DM * 8) : off);
    const u64 bad = __builtin_amdgcn_ballot_w64(!(fresh(a, tag) && fresh(c, tag)));
    if (bad == 0) break;
    if (K_SL2 && __builtin_popcountll(bad) > 12) __builtin_amdgcn_s_sleep(K_SL2);
  }
  if (!two) c = v4u{0u, 0u, 0u, 0u};
  float s0 = fold32(val(a.x) + val(c.x), val(a.y) + val(c.y)), s1 = fold32(val(a.z) + val(c.z), val(a.w) + val(c.w));
  s0 = fold16(s0, s0); s1 = fold16(s1, s1);
  s0 += dpp<ROR8>(s0); s1 += dpp<ROR8>(s1);
  return v2f{bfr(s0 + dpp<ROR4>(s0)), bfr(s1 + dpp<ROR4>(s1))};
}

__device__ __forceinline__ void combine(const Params& p, LAS unsigned char* L, int lane, int b) {
  LAS unsigned* ctl = (LAS unsigned*)(L + L_CTL);
  const int h = K_XMAP ? blockIdx.x >> 3 : blockIdx.x & 31, l48 = min(lane, 47);
  const __amdgpu_buffer_rsrc_t rw = rsrc(p.ws, W_END);
  const unsigned tag = (unsigned)rfl((int)ctl[C_TAG]);
  const int off = W_OP + (b * NH + h) * 8 * 768 + l48 * 16;
  v4f g[8];
  v4u mv;
  unsigned miss = 0xffu;
#pragma unroll
  for (int k = 0; k < 8; ++k) g[k] = v4f{0.f, 0.f, 0.f, 0.f};
  if (K_D1) __builtin_amdgcn_s_sleep(K_D1);
#pragma nounroll
  for (unsigned n = 0; n < GLB_SPIN; ++n) {
    v4u t[8];
    CBAR();
    mv = ld16(rw, W_ML + (((K_EARLY0 ? b : 1) * NH + h) * 8 + (lane & 7)) * 16);
#pragma unroll
    for (int k = 0; k < 8; ++k)
      if (miss >> k & 1) t[k] = ld16(rw, off + k * 768);
#pragma unroll
    for (int k = 0; k < 8; ++k)
      if (miss >> k & 1) {
        if (__all(fresh(t[k], tag))) { g[k] = v4f{val(t[k].x), val(t[k].y), val(t[k].z), val(t[k].w)}; miss ^= 1u << k; }
      }
    if (miss == 0u && __all(fresh(mv, tag))) break;
    if (K_SL1 && __builtin_popcount(miss) > K_FEW) __builtin_amdgcn_s_sleep(K_SL1);
  }
  const float Mk = val(b ? mv.z : mv.x);
  float M = Mk;
  M = fmaxf(M, dpp<XOR1>(M)); M = fmaxf(M, dpp<XOR2>(M)); M = fmaxf(M, dpp<MIR8>(M));
  float w = exp2f_(Mk - M), Ls = w * val(b ? mv.w : mv.y);
  Ls += dpp<XOR1>(Ls); Ls += dpp<XOR2>(Ls); Ls += dpp<MIR8>(Ls);
  w *= __builtin_amdgcn_rcpf(Ls);
  v4f o = {0.f, 0.f, 0.f, 0.f};
#pragma unroll
  for (int k = 0; k < 8; ++k) o += asf((unsigned)__builtin_amdgcn_readlane((int)asu(w), k)) * g[k];
  *(LAS v2u*)(L + L_OA + b * (DH * 2) + l48 * 8) = v2u{bfbits(o.x) | (bfbits(o.y) << 16), bfbits(o.z) | (bfbits(o.w) << 16)};
  lds_set(ctl + C_O + b, 1u);
}

__device__ __forceinline__ void run_loader(const Params& p, LAS unsigned char* L, int lane, u64 tentry) {
  LAS unsigned* ctl = (LAS unsigned*)(L + L_CTL);
  const int bid = blockIdx.x, h = K_XMAP ? bid >> 3 : bid & 31, m = K_XMAP ? bid & 7 : bid >> 5;
  const int r = lane >> 3, j = lane & 7;
  __builtin_amdgcn_s_setprio(3);
  STAMPS();
  Loader ld{ctl, (unsigned)(size_t)L, 0, 0, NSLOT, 0, (unsigned)(size_t)(r == 1 ? ctl + C_TEND : ctl + C_NEXT + (j < NC ? j : 0))};
  const int voff = lane * 16;
  if (__builtin_expect(m < 6, 1)) {
    const v4u rq = srd_of(p.wq, DM * NH * DH * 2);
    const int vo = r * (NH * DH * 2) + (j ^ (r & 2)) * 16, so = (m / 3) * 320 * (NH * DH * 2) + h * (DH * 2) + (m % 3) * 128;
#pragma unroll 2
    for (int g = 0; g < 6 * NS_WQ; ++g) dma1<63>(g & 1 ? vo ^ 64 : vo, rq, so + min(g, 39) * (8 * NH * DH * 2), ld.lbase + g * 1024);
    ld.s = ld.pos = NS_WQ;
  }
  asm volatile("s_waitcnt lgkmcnt(0)\n\ts_barrier" ::: "memory");
  STAMP(1);
  const Plan pl(h, m);
  ld.t0 = (unsigned)tentry;
  ld.slast = pl.ntot();
  if (pl.mlp) {
    const v4u rg = srd_of(p.wg, DM * FF * 2), ru = srd_of(p.wu, DM * FF * 2), rd = srd_of(p.wd, FF * DM * 2);
    const int vo = r * (FF * 2) + (j ^ (r & 2)) * 16;
    constexpr int W = K_WMLP;
    const int sd = h * 64 * (DM * 2);
    auto slot = [&](int pc0, int sg) {
#pragma unroll
      for (int q = 0; q < 6; ++q) {
        const int pc = pc0 + q;
        if (pc < 16 || sg >= 0) dma1<W - 1>(q & 1 ? vo ^ 64 : vo, pc & 4 ? ru : rg, (sg < 0 ? 72 : sg) * (8 * FF * 2) + ((pc >> 3) * 4 + (pc & 3)) * (8 * FF * 2) + h * 128, ld.dst() + q * 1024);
        else dma1<W - 1>(voff, rd, sd + (pc - 16) * 1024, ld.dst() + q * 1024);
      }
      ld.publish(ld.s + lag6(W));
      ld.advance();
    };
#pragma nounroll
    for (int t = 0; t < 6; ++t) {
#pragma unroll
      for (int u = 0; u < 4; ++u) slot(6 * u, 12 * t);
    }
#pragma unroll
    for (int u = 0; u < 3; ++u) { ld.credit(ld.s); slot(6 * u, -1); }
#pragma nounroll
    for (int t = 27; t < NS_MLP; ++t) {
      ld.credit(ld.s);
      dma3<W - 1>(voff, rd, sd + (6 * t - 160) * 1024, ld.dst());
      dma3<W - 1>(voff, rd, sd + (6 * t - 157) * 1024, ld.dst() + 3072);
      ld.publish(ld.s + lag6(W));
      ld.advance();
    }
  }
  STAMP(2);
  ld.now = ld.clock();
  ld.last = ld.now;
  ld.due = ld.now << 8;
  ld.tend = K_TEND;
  ld.shift = K_STAG * (2 * h - 31) / 62;
  ld.pace = ((K_TEND + ld.shift) * 256 - ld.due) * 2 / (ld.slast - ld.s);
  ld.pmax = ld.pace + (ld.pace >> 3);
#pragma nounroll
  for (int ph = 0; ph < 4; ++ph) {
    const v4u rs = srd_of(ph & 1 ? p.v : p.k, KV_BYTES);
    const int b = ph >> 1, np = b ? 32 : pl.len0() / 2;
#pragma nounroll
    for (int i = 0; i < np; ++i) ld.rows(voff, rs, piece_off(pl.piece(i, b, h, m), b, h));
    if (ph == 0) STAMP(5); else if (ph == 1) STAMP(6); else if (ph == 2) STAMP(7);
  }
  STAMP(3);
  const v4u ro = srd_of(p.wo, NH * DH * DM * 2);
  int row = lane / 10, ch = lane - row * 10;
  ld.credit(ld.s + pl.wo_skip() + NWO - 1);
  if (pl.wo_skip()) ld.pos = 0;
#pragma nounroll
  for (int t = 0; t < NWO; ++t) {
#pragma unroll
    for (int q = 0; q < 6; ++q) {
      dma1<K_WWO - 1>(row * (DM * 2) + ch * 16, ro, h * DH * (DM * 2) + m * 160, ld.dst() + q * 1024);
      if (q == 2 && lag3(K_WWO) == lag6(K_WWO)) ld.publish(ld.s + lag3(K_WWO));
      if (q == 5 && lag3(K_WWO) != lag6(K_WWO)) ld.publish(ld.s + lag6(K_WWO));
      row += 6; ch += 4;
      if (ch >= 10) { ch -= 10; row += 1; }
    }
    ld.advance();
  }
  if (K_WWO > 48) { asm volatile("s_waitcnt vmcnt(48)" ::: "memory"); ld.publish(ld.s - 8); }
  if (K_WWO > 42) { asm volatile("s_waitcnt vmcnt(42)" ::: "memory"); ld.publish(ld.s - 7); }
  if (K_WWO > 36) { asm volatile("s_waitcnt vmcnt(36)" ::: "memory"); ld.publish(ld.s - 6); }
  if (K_WWO > 30) { asm volatile("s_waitcnt vmcnt(30)" ::: "memory"); ld.publish(ld.s - 5); }
  asm volatile("s_waitcnt vmcnt(0)" ::: "memory");
  ld.publish(ld.s);
  STAMP(4);
  if (bid < NFIN) {
    const v2f y = sum_producers<NMLP>(rsrc(p.ws, W_END), W_YP, (unsigned)rfl((int)ctl[C_TAG]), lane);
    const float sg = *(const LAS float*)(L + L_G + lane * 4);
    const unsigned xn = *(const LAS unsigned*)(L + L_XP + (lane >> 5) * 1280 + bid * 16 + (lane & 3) * 4);
    *(LAS v2f*)(L + L_G + 256 + lane * 8) = v2f{bfr(siluf(bfr(lo16(xn) + bfr(y.x * sg)))), bfr(siluf(bfr(hi16(xn) + bfr(y.y * sg))))};
    lds_set(ctl + C_G, 1u);
  }
  st.t[2] = tentry + ld.stall; st.t[0] = tentry; st.t[1] = tentry + ld.shift - K_STAG * (2 * h - 31) / 62;
  FLUSH(0, 8);
}

struct Early { v2u xr[5], wr[5]; unsigned e0, xo; };
__device__ __forceinline__ void early_loads(const Params& p, Early& e, int lane) {
#pragma unroll
  for (int i = 0; i < 5; ++i) e.xr[i] = ((const GAS v2u*)p.x)[(lane >> 5) * 160 + (lane & 31) + 32 * i];
  e.e0 = __hip_atomic_load((GAS unsigned*)(p.ws + W_EPOCH), RLX_AGENT);
}
__device__ __forceinline__ void run_helper(const Params& p, LAS unsigned char* L, int lane, Early& e) {
  LAS unsigned* ctl = (LAS unsigned*)(L + L_CTL);
  const int bid = blockIdx.x, h = K_XMAP ? bid >> 3 : bid & 31, m = K_XMAP ? bid & 7 : bid >> 5;
  const int j = lane & 7;
  const __amdgpu_buffer_rsrc_t rw = rsrc(p.ws, W_END);
  STAMPS();
  const int fb = lane >> 5, col = min(bid, NFIN - 1) * 8 + 2 * (lane & 3), l48 = min(lane, 47);
#pragma unroll
  for (int i = 0; i < 5; ++i) e.wr[i] = ((const GAS v2u*)p.wr)[(lane & 31) + 32 * i];
  e.xo = ((const GAS unsigned*)p.x)[(fb * DM + col) >> 1];
  __builtin_amdgcn_sched_barrier(0);
  layer_norm(e.xr, L, lane);
  const unsigned epoch = (unsigned)rfl((int)e.e0) + 1u, tag = tag_of(epoch);
  ctl[C_TAG] = tag;
  lds_set(ctl + C_XOK, 1u);
  STAMP(0);
  {
    lds_wait<2>(ctl + C_LANDED, K_QWAIT);
    const int off = W_QP + (h * 2 * DH + 4 * l48) * 8;
    v4u g[4];
#pragma nounroll
    for (unsigned n = 0; n < GLB_SPIN; ++n) {
      CBAR();
#pragma unroll
      for (int i = 0; i < 4; ++i) g[i] = ld16(rw, off + (i & 1) * 16 + (i >> 1) * (DH * 8));
      if (__all(fresh(g[0], tag) && fresh(g[1], tag) && fresh(g[2], tag) && fresh(g[3], tag))) break;
    }
#pragma unroll
    for (int b = 0; b < 2; ++b)
      *(LAS v2u*)(L + L_QB + b * (DH * 2) + l48 * 8) = v2u{bfbits(val(g[0][b]) + val(g[2][b])) | (bfbits(val(g[0][2 + b]) + val(g[2][2 + b])) << 16),
                                                            bfbits(val(g[1][b]) + val(g[3][b])) | (bfbits(val(g[1][2 + b]) + val(g[3][2 + b])) << 16)};
    lds_set(ctl + C_Q, 1u);
  }
  float rs = 0.f;
#pragma unroll
  for (int i = 0; i < 5; ++i) {
    const v2u xx = *(const LAS v2u*)(L + L_XP + fb * 1280 + ((lane & 31) + 32 * i) * 8);
    rs = dot2(e.wr[i].y, xx.y, dot2(e.wr[i].x, xx.x, rs));
  }
  rs += dpp<XOR1>(rs); rs += dpp<XOR2>(rs); rs += dpp<ROR4>(rs); rs += dpp<ROR8>(rs);
  *(LAS float*)(L + L_G + lane * 4) = bfr(1.0f / (1.0f + expf(-bfr(fold16(rs, rs)))));
  v4u po[2], pml;
  v2f st2[2];
#pragma unroll
  for (int b = 0; b < 2; ++b) {
    lds_wait_all<NC, 16>(ctl + C_ML, j, b + 1);
    v2f ml = *(const LAS v2f*)(L + L_MLW + b * 64 + j * 8);
    if (j >= NC) ml = v2f{-3.0e38f, 0.f};
    float M = ml.x;
    M = fmaxf(M, dpp<XOR1>(M)); M = fmaxf(M, dpp<XOR2>(M)); M = fmaxf(M, dpp<MIR8>(M));
    M = val(asu(M));
    const float w = exp2f_(ml.x - M);
    *(LAS float*)(L + L_F + b * 32 + j * 4) = w;
    lds_set(ctl + C_F, b + 1);
    float Ls = w * ml.y;
    Ls += dpp<XOR1>(Ls); Ls += dpp<XOR2>(Ls); Ls += dpp<MIR8>(Ls);
    st2[b] = v2f{M, Ls};
    if (b == 0) {
      lds_wait_all<NC, 16>(ctl + C_R, j, 1);
    } else {
      if (K_EARLY0) combine(p, L, lane, 0);
      pml = pack4(st2[0].x, st2[0].y, st2[1].x, st2[1].y, tag);
      asm volatile("" : "+v"(pml), "+v"(po[0]));
      lds_wait_all<NC, 0>(ctl + C_R, j, 2);
    }
    STAMP(1);
    v4f o = {0.f, 0.f, 0.f, 0.f};
#pragma unroll
    for (int c = 0; c < NC; ++c) o += *(const LAS v4f*)(L + L_REC + c * 768 + l48 * 16);
    po[b] = pack4(o.x, o.y, o.z, o.w, tag);
    if (b == 0) {
      lds_set(ctl + C_RFREE, 1u);
      if (K_EARLY0) {
        if (lane < 48) st16(rw, W_OP + (h * 8 + m) * 768 + lane * 16, po[0]);
        if (lane == 0) st16(rw, W_ML + (h * 8 + m) * 16, pack4(M, Ls, M, Ls, tag));
      }
    }
  }
  if (lane < 48) {
    st16(rw, W_OP + ((NH + h) * 8 + m) * 768 + lane * 16, po[1]);
    if (!K_EARLY0) st16(rw, W_OP + (h * 8 + m) * 768 + lane * 16, po[0]);
  }
  if (lane == 0) st16(rw, W_ML + ((NH + h) * 8 + m) * 16, pml);
  lds_set(ctl + C_PUB, 1u);
  STAMP(2);
  combine(p, L, lane, 1);
  STAMP(4);
  if (bid < NFIN) {
    lds_wait_all<5, 0>(ctl + C_T3, j);
    lds_wait<0>(ctl + C_G, 1);
    const v2f gt = *(const LAS v2f*)(L + L_G + 256 + lane * 8);
    STAMP(5);
    if (K_D2) __builtin_amdgcn_s_sleep(K_D2);
    const v2f sv = sum_producers<NH>(rw, W_SP, tag, lane);
    STAMP(6);
    const unsigned ov = bfbits(lo16(e.xo) + bfr(sv.x * gt.x)) | (bfbits(hi16(e.xo) + bfr(sv.y * gt.y)) << 16);
    if ((lane & 31) < 4) ((GAS unsigned*)p.out)[(fb * DM + col) >> 1] = ov;
    if (bid == 0 && lane == 0) __hip_atomic_store((GAS unsigned*)(p.ws + W_EPOCH), epoch, RLX_AGENT);
    STAMP(7);
  }
  st.t[3] = st.t[4];
  FLUSH(8, bid < NFIN ? 8 : 5);
}

__device__ __forceinline__ void run_consumer(const Params& p, LAS unsigned char* L, int lane, int c) {
  LAS unsigned* ctl = (LAS unsigned*)(L + L_CTL);
  const int bid = blockIdx.x, h = K_XMAP ? bid >> 3 : bid & 31, m = K_XMAP ? bid & 7 : bid >> 5;
  const int r = lane >> 3, j = lane & 7;
  const Plan pl(h, m);
  const __amdgpu_buffer_rsrc_t rw = rsrc(p.ws, W_END);
  STAMPS();
  if (pl.npro == 0 || (m < 6 && c >= 4)) lds_set(ctl + C_NEXT + c, pl.npro + c);
  lds_wait<1>(ctl + C_XOK, 1);
  const unsigned tag = (unsigned)rfl((int)ctl[C_TAG]);
  const int g = lane >> 4;
  const int bq = (lane >> 4) * 1024 + ((lane & 15) >> 2) * 128 + ((2 * c + ((lane & 3) >> 1)) ^ (2 * (r & 3))) * 16 + (lane & 1) * 8;
  v2u yp[7] = {};
  auto put_y = [&]() {
#pragma unroll
    for (int k = 0; k < 7; ++k)
      if (lane < 16 && c + NC * k < DM / 16) st8(rw, W_YP + (h * DM + (c + NC * k) * 16 + lane) * 8, yp[k]);
  };
  if (m < 6) {
    if (c < 4) {
      lds_wait<0>(ctl + C_LANDED, NS_WQ);
      v4u bf[10];
      load_bt<10, 4096, 512>(L + bq, bf);
      lds_set(ctl + C_NEXT + c, NS_WQ + c);
      const v4f qv = mm2<10>(L + L_XP + (lane & 1) * 1280 + (m / 3) * 640 + (lane >> 4) * 16, bf);
      if (lane < 16) st8(rw, W_QP + ((h * 2 + m / 3) * DH + (m % 3) * 64 + 16 * c + lane) * 8, pack2(qv.x, qv.y, tag));
    }
  } else if (pl.mlp) {
    if (c < 4) {
      const LAS unsigned char* ap = L + L_XP + (lane & 1) * 1280 + g * 16;
      v4f ga = {0.f, 0.f, 0.f, 0.f}, ua = {0.f, 0.f, 0.f, 0.f};
#pragma nounroll
      for (int t = 0; t < 20; t += 2) {
        lds_wait<1>(ctl + C_LANDED, (8 * t + 21) / 6);
        v4u gf[2], uf[2];
        load_bt<2, 8192, 512>(L + (t < 18 ? t : t - 18) * 8192 + bq, gf);
        load_bt<2, 8192, 512>(L + (t < 18 ? t : t - 18) * 8192 + 4096 + bq, uf);
        lds_set(ctl + C_NEXT + c, (8 * t + 16) / 6);
        ga = mm2<2>(ap + t * 64, gf, ga);
        ua = mm2<2>(ap + t * 64, uf, ua);
      }
      if (lane < 32) *(LAS unsigned short*)(L + L_HM + g * 128 + (16 * c + (lane & 15)) * 2) = (unsigned short)bfbits(bfr(siluf(bfr(g ? ga[1] : ga[0]))) * bfr(g ? ua[1] : ua[0]));
      lds_set(ctl + C_HM + c, 1u);
    } else {
      lds_set(ctl + C_NEXT + c, 160 / 6);
    }
    lds_wait_all<4, 1>(ctl + C_HM, j);
    lds_wait<1>(ctl + C_LANDED, NS_MLP);
#pragma unroll
    for (int k = 0; k < 7; ++k) {
      const int n = min(c + NC * k, DM / 16 - 1);
      v4u bf[2];
      load_bt<2, 32 * 1280, 4 * 1280>(L + (160 * 1024 - RINGB) + (8 * g + ((lane & 15) >> 2)) * 1280 + n * 32 + (lane & 3) * 8, bf);
      const v4f y = mm2<2>(L + L_HM + (lane & 1) * 128 + g * 16, bf);
      yp[k] = pack2(y[0], y[1], tag);
    }
    if (!K_YLATE) put_y();
    lds_set(ctl + C_NEXT + c, NS_MLP + c);
  }
  STAMP(0);
  const int ntot = pl.ntot();
  int s = pl.npro + c;
  auto take = [&](int i, int len) -> const LAS unsigned char* {
    lds_wait<1>(ctl + C_LANDED, s + 1);
    const LAS unsigned char* sp = L + s % NSLOT * SLOTB;
    s += i + NC < len ? NC : len - i + c;
    return sp;
  };
  auto fetch = [&](v4u (&kk)[6], int i, int len) {
    const LAS unsigned char* sp = take(i, len) + r * 384 + j * 16;
#pragma unroll
    for (int u = 0; u < 6; ++u) kk[u] = *(const LAS v4u*)(sp + (u / 3) * 3072 + (u % 3) * 128);
    lds_set(ctl + C_NEXT + c, s);
  };
  v4u e0[6], e1[6];
  fetch(e0, c, pl.len0());
  fetch(e1, c + NC, pl.len0());
  lds_wait<1>(ctl + C_Q, 1);
  STAMP(1);
  LAS unsigned char* pw = L + L_P + c * 512;
#pragma nounroll
  for (int b = 0; b < 2; ++b) {
    const int len = b ? 64 : pl.len0();
    v4u q[3];
#pragma unroll
    for (int u = 0; u < 3; ++u) q[u] = *(const LAS v4u*)(L + L_QB + (b * DH + (j + 8 * u) * 8) * 2);
    float sc[4] = {-3.0e38f, -3.0e38f, -3.0e38f, -3.0e38f};
    auto score = [&](const v4u (&kk)[6], int it) {
      float d0 = 0.f, d1 = 0.f;
#pragma unroll
      for (int u = 0; u < 3; ++u)
#pragma unroll
        for (int e = 0; e < 4; ++e) { d0 = dot2(kk[u][e], q[u][e], d0); d1 = dot2(kk[3 + u][e], q[u][e], d1); }
      d0 += dpp<XOR1>(d0); d1 += dpp<XOR1>(d1); d0 += dpp<XOR2>(d0); d1 += dpp<XOR2>(d1);
      const float d = (j & 4 ? d1 : d0) + dpp<MIR8>(j & 4 ? d0 : d1);
      const float t = bfr(bfr(d) * 0.07216878364870322f) * LOG2E;
#pragma unroll
      for (int k = 0; k < 4; ++k) sc[k] = it == 4 * k + (j & 3) ? t : sc[k];
    };
    int i = c, it = 0;
    if (b == 0) { score(e0, 0); score(e1, 1); i += 2 * NC; it = 2; }
#pragma nounroll
    for (; i < len; i += NC, ++it) {
      v4u kk[6];
      fetch(kk, i, len);
      score(kk, it);
    }
    float M = fmaxf(fmaxf(sc[0], sc[1]), fmaxf(sc[2], sc[3]));
    M = fmaxf(M, dpp<XOR1>(M)); M = fmaxf(M, dpp<XOR2>(M)); M = fmaxf(M, dpp<ROR4>(M)); M = fmaxf(M, dpp<ROR8>(M));
    M = max_rows(M);
    float ls = 0.f;
#pragma unroll
    for (int k = 0; k < 4; ++k) {
      const float pr = exp2f_(sc[k] - M);
      ls += pr;
      *(LAS unsigned short*)(pw + k * 128 + (j & 3) * 32 + (r + 8 * (j >> 2)) * 2) = (unsigned short)bfbits(pr);
    }
    ls = wave_sum(ls);
    *(LAS v2f*)(L + L_MLW + b * 64 + c * 8) = v2f{M, ls};
    lds_set(ctl + C_ML + c, b + 1);
    v4f acc[12];
#pragma unroll
    for (int t = 0; t < 12; ++t) acc[t] = v4f{0.f, 0.f, 0.f, 0.f};
    float f = 0.f;
#pragma nounroll
    for (i = c, it = 0; i < len; i += NC, ++it) {
      if (K_YLATE && b && pl.mlp && i + K_YLATE * NC >= len && i + (K_YLATE - 1) * NC < len) put_y();
      if (i + NC >= len) {
        lds_wait<1>(ctl + C_F, b + 1);
        if (b) lds_wait<1>(ctl + C_RFREE, 1);
        f = *(const LAS float*)(L + L_F + b * 32 + c * 4);
      }
      const LAS unsigned char* sp = take(i, len) + (lane >> 2) * 384 + (lane & 3) * 8;
      const v4s pa = *(const LAS v4s*)(pw + it * 32 + g * 8);
      v4s vf[12];
#pragma unroll
      for (int t = 0; t < 12; ++t) vf[t] = __builtin_amdgcn_ds_read_tr16_b64_v4i16((LAS v4s*)(sp + t * 32));
      lds_set(ctl + C_NEXT + c, s);
#pragma unroll
      for (int t = 0; t < 12; ++t) acc[t] = __builtin_amdgcn_mfma_f32_16x16x16bf16_1k(pa, vf[t], acc[t], 0, 0, 0);
    }
    if (b) STAMP(3); else STAMP(2);
#pragma unroll
    for (int k = 0; k < 3; ++k) {
      const float v = g & 2 ? (g & 1 ? acc[9 + k][0] : acc[6 + k][0]) : (g & 1 ? acc[3 + k][0] : acc[k][0]);
      *(LAS float*)(L + L_REC + c * 768 + (g * 48 + k * 16 + (lane & 15)) * 4) = v * f;
    }
    lds_set(ctl + C_R + c, b + 1);
  }
  STAMP(4);
  if (c == 5) {
    if (K_EARLY0) return;
    lds_wait<0>(ctl + C_PUB, 1);
    combine(p, L, lane, 0);
    return;
  }
  lds_wait<0>(ctl + C_LANDED, ntot + NWO);
  v4u bf[6];
  load_bt<6, 32 * 160, 4 * 160>(L + (ntot + pl.wo_skip()) % NSLOT * SLOTB + ((lane >> 4) * 8 + ((lane & 15) >> 2)) * 160 + c * 32 + (lane & 3) * 8, bf);
  STAMP(5);
  lds_wait_all<2, 0>(ctl + C_O, j);
  STAMP(6);
  const v4f sv = mm2<6>(L + L_OA + (lane & 1) * (DH * 2) + (lane >> 4) * 16, bf);
  if (lane < 16) st8(rw, W_SP + (h * DM + m * 80 + c * 16 + lane) * 8, pack2(sv.x, sv.y, tag));
  lds_set(ctl + C_T3 + c, 1u);
  STAMP(7);
  if (c == 0) FLUSH(16, 8);
  if (c == 4) FLUSH(24, 8);
}

__device__ __forceinline__ void run_pacer(const Params& p, LAS unsigned char* L, int lane, u64 tentry) {
  LAS unsigned* ctl = (LAS unsigned*)(L + L_CTL);
  const int bid = blockIdx.x;
  const __amdgpu_buffer_rsrc_t rw = rsrc(p.ws, W_END);
  auto until = [&](int t) {
    int now = 0;
#pragma nounroll
    for (unsigned n = 0; n < LDS_SPIN; ++n) {
      now = (int)((unsigned)__builtin_amdgcn_s_memrealtime() - (unsigned)tentry);
      if (now >= t) break;
      __builtin_amdgcn_s_sleep(3);
    }
    return now;
  };
  lds_wait<16>(ctl + C_XOK, 1);
  const unsigned tag = (unsigned)rfl((int)ctl[C_TAG]);
  const int h = K_XMAP ? bid >> 3 : bid & 31;
  auto touch = [&](int ph) {
    const GAS unsigned char* a = ph < 4 ? (const GAS unsigned char*)(ph & 1 ? p.v : p.k) + (size_t)((ph >> 1) * NH + h) * SLAB + min(lane << 20, SLAB - 128)
                                        : (const GAS unsigned char*)p.wo + (size_t)h * (DH * DM * 2) + (lane & 1) * (DH * DM * 2 - 128);
    if (K_TOUCH && lane < 4 && ((bid >> 5) & K_TMASK) == 0) { const unsigned v = __builtin_nontemporal_load((const GAS unsigned*)a); asm volatile("" :: "v"(v)); }
  };
  until(K_BASE - K_T1);
  touch(1);
  float tend = K_TEND + 1, base = K_BASE;
  until(K_BASE);
  lds_set(ctl + C_TEND, K_TEND + 1);
#pragma nounroll
  for (int c = 0; c < K_NCP; ++c) {
    const int t = K_CP0 + c * K_CP;
    if (c == K_TC2) touch(2);
    if (c == K_TC3) touch(3);
    if (c == K_TC4) touch(4);
    if (until(t) > t + K_CPD / 2) continue;
    if ((bid & 96) == 0 && ((bid & 31) | (bid >> 7) << 5) < K_NS && lane == 0) st8(rw, W_PC + c * 512 + ((bid & 31) | (bid >> 7) << 5) * 8, pack2((float)(int)__hip_atomic_load(ctl + C_LATE, RLX_WG), tend, tag));
    until(t + K_CPD);
    const v2u g = __builtin_amdgcn_raw_buffer_load_b64(rw, W_PC + c * 512 + lane * 8, 0, 16);
    const bool ok = lane < K_NS && __builtin_amdgcn_perm(g.y, g.x, 0x0c0c0400u) == (tag & 0xffffu) && val(g.x) < 4096.f && val(g.y) > K_TEND && val(g.y) < K_TEND + 4096;
    const float n = (float)__builtin_popcountll(__builtin_amdgcn_ballot_w64(ok));
    float tm = ok ? val(g.y) : 0.f;
    tm = fmaxf(tm, dpp<XOR1>(tm)); tm = fmaxf(tm, dpp<XOR2>(tm)); tm = fmaxf(tm, dpp<ROR4>(tm)); tm = fmaxf(tm, dpp<ROR8>(tm));
    tend = fmaxf(tend, max_rows(tm));
    const float lam = ok ? val(g.x) : 0.f, inv = 1.f / fmaxf(n, 1.f);
    const float late = fmaxf(wave_sum(lam) * inv - K_DEAD, K_XW * wave_sum(fmaxf(lam - K_L0, 0.f)) * inv - K_XD);
    const float step = fminf(K_STEP, late * fmaxf(c >= K_CL ? K_GL : K_GMIN, K_GAIN * (tend - t) / fmaxf(t - base, K_FLOOR)));
    if (step > K_MIN && n >= K_NS * 0.75f) { tend = floorf(tend + step); base = t + K_CPD; }
    lds_set(ctl + C_TEND, (unsigned)rfl((int)tend));
    if (bid == 0) { ((GAS float*)(p.ws + W_ST + (512 << 10)))[c * 2] = late; ((GAS float*)(p.ws + W_ST + (512 << 10)))[c * 2 + 1] = tend - K_TEND; }
  }
}

__global__ void __launch_bounds__(NTHR, 3) fused_decode(Params p) {
  extern __shared__ __attribute__((aligned(16))) unsigned char smem[];
  const u64 tentry = __builtin_amdgcn_s_memrealtime();
  LAS unsigned char* L = (LAS unsigned char*)smem;
  const int tid = threadIdx.x, lane = tid & 63, wave = rfl(tid >> 6);
  if (tid < 80) ((LAS unsigned*)(L + L_CTL))[tid] = tid == C_TEND ? K_TEND : 0u;
  Early e;
  if (wave == NC + 1) early_loads(p, e, lane);
  int role = wave;
  asm volatile("" : "+s"(role));
  if (role > 0) {
    asm volatile("s_waitcnt lgkmcnt(0)\n\ts_barrier" ::: "memory");
    if (wave <= NC) run_consumer(p, L, lane, wave - 1);
    else if (wave == NC + 1) run_helper(p, L, lane, e);
    else run_pacer(p, L, lane, tentry);
  } else {
    run_loader(p, L, lane, tentry);
  }
}

}

namespace {
__global__ void k_rep(Params p) {
  const u64 now = __builtin_amdgcn_s_memrealtime();
  const GAS u64* st = (const GAS u64*)(p.ws + W_ST);
  const int l = threadIdx.x;
  u64 t0 = ~0ull, mx = 0, sum = 0;
  for (int i = l; i < 256; i += 64) { const u64 d = st[i * 32]; t0 = d < t0 ? d : t0; }
  for (int o = 32; o; o >>= 1) { const u64 b = __shfl_xor(t0, o); t0 = b < t0 ? b : t0; }
  for (int i = l; i < NWGS; i += 64) { const u64 d = st[i * 32 + IDX] - t0; mx = d > mx ? d : mx; sum += d; }
  for (int o = 32; o; o >>= 1) { const u64 b = __shfl_xor(mx, o); mx = b > mx ? b : mx; sum += __shfl_xor(sum, o); }
  u64 v = MODE == 0 ? mx : MODE == 1 ? sum / NWGS : MODE == 2 ? now - t0 : (u64)(((const GAS float*)(p.ws + W_ST + (512 << 10)))[PIDX] + 100.f);
  if (v > 20000) v = 20000;
  while (__builtin_amdgcn_s_memrealtime() - now < 1000 + SCALE * v) __builtin_amdgcn_s_sleep(8);
}
}
extern "C" void kernel_launch(void* const* d_in, const int* in_sizes, int n_in, void* d_out, int out_size, void* d_ws,
                              size_t ws_size, hipStream_t stream) {
  Params p;
  p.x = (const unsigned short*)d_in[0];
  p.k = (const unsigned short*)d_in[1];
  p.v = (const unsigned short*)d_in[2];
  p.wq = (const unsigned short*)d_in[3];
  p.wo = (const unsigned short*)d_in[4];
  p.wr = (const unsigned short*)d_in[6];
  p.wg = (const unsigned short*)d_in[7];
  p.wu = (const unsigned short*)d_in[8];
  p.wd = (const unsigned short*)d_in[9];
  p.out = (unsigned short*)d_out;
  p.ws = (unsigned char*)d_ws;
  (void)hipFuncSetAttribute((const void*)fused_decode, hipFuncAttributeMaxDynamicSharedMemorySize, L_END);
  fused_decode<<<NWG, NTHR, L_END, stream>>>(p);
  k_rep<<<1, 64, 0, stream>>>(p);
}
```

```cpp
#define PIDX 5
#define K_STAMPS 1
#define IDX 5
#define MODE 1
#define SCALE 10
#define NWGS 256
#include <hip/hip_runtime.h>
#include <hip/hip_bf16.h>

namespace {

#define GAS __attribute__((address_space(1)))
#define LAS __attribute__((address_space(3)))
#define RLX_AGENT __ATOMIC_RELAXED, __HIP_MEMORY_SCOPE_AGENT
#define RLX_WG __ATOMIC_RELAXED, __HIP_MEMORY_SCOPE_WORKGROUP
typedef unsigned v4u __attribute__((ext_vector_type(4)));
typedef unsigned v2u __attribute__((ext_vector_type(2)));
typedef float v4f __attribute__((ext_vector_type(4)));
typedef float v2f __attribute__((ext_vector_type(2)));
typedef short v4s __attribute__((ext_vector_type(4)));
typedef __bf16 bf16x2 __attribute__((ext_vector_type(2)));
typedef __bf16 bf16x8 __attribute__((ext_vector_type(8)));
typedef unsigned long long u64;

#define K_INF 21
#define K_NSLOT 24
#define K_A1 10
#define K_C1 1
#define K_WMLP 33
#define K_HROT 1
#define K_XMAP 0
#define K_STR 21
#define K_ROT 1
#define K_FEW 2
#define K_SL1 8
#define K_SL2 8
#define K_TEND 5380
#define K_LATE 40
#define K_WB 33
#define K_D1 12
#define K_D2 12
#define K_NCP 14
#define K_CP0 1625
#define K_CP 275
#define K_CPD 125
#define K_L0 60.f
#define K_GMIN 3.f
#define K_STEP 200.f
#define K_KEEP 0
#define K_XW 2.f
#define K_XD 6.f
#define K_BASE 1200
#define K_FLOOR 600.f
#define K_MIN 8.f
#define K_STALL 160
#define K_GAIN 0.6f
#define K_DEAD 50.f
#define K_R0 9
#define K_TOUCH 1
#define K_T1 300
#define K_TC2 3
#define K_TC3 8
#define K_TC4 12
#define K_EARLY0 0
#define K_CL 9
#define K_GL 5.f
#define K_NEND 0
#define K_LATE2 15
#define K_WWO K_INF
#define K_NS 32
#define K_YLATE 2
#define K_QWAIT 22

constexpr int DM = 640, NH = 32, DH = 192, SK = 8192, FF = 1536;
constexpr int NWG = 256, NC = 6, NTHR = (NC + 3) * 64;
constexpr int INF = K_INF;
constexpr int NSLOT = K_NSLOT;
constexpr int SLOTB = 6144, RINGB = NSLOT * SLOTB, NWO = 5, NS_WQ = 7, NS_MLP = 40;
constexpr unsigned KV_BYTES = 2u * NH * SK * DH * 2u;
constexpr int SLAB = SK * DH * 2;
constexpr int NMLP = FF / 64;
constexpr int NFIN = DM / 8;
static_assert(NSLOT == 24, "the MLP images rely on where the ring wraps");
static_assert(INF >= 7 && INF <= 30 && K_WB <= 60, "vmcnt is 6 bits; the last K/V slots are published while wo is requested");

constexpr int L_XP = NSLOT * SLOTB;
constexpr int L_REC = L_XP + 2560;
constexpr int L_QB = L_REC + 4608;
constexpr int L_OA = L_QB + 768;
constexpr int L_HM = L_OA + 768;
constexpr int L_P = L_HM + 256;
constexpr int L_MLW = L_P + 3072;
constexpr int L_F = L_MLW + 128;
constexpr int L_G = L_F + 64;
constexpr int L_CTL = L_G + 768;
constexpr int L_END = L_CTL + 320;
static_assert(L_END <= 160 * 1024 && L_END > 80 * 1024, "one workgroup per CU");
enum { C_NEXT = 0, C_HM = 16, C_ML = 24, C_R = 32, C_T3 = 48, C_O = 56, C_LANDED = 64, C_XOK, C_TAG, C_Q, C_F, C_RFREE, C_G, C_PUB, C_LATE, C_TEND };

constexpr int W_EPOCH = 0;
constexpr int W_ML = 1024;
constexpr int W_OP = W_ML + 2 * NH * 8 * 16;
constexpr int W_SP = W_OP + 2 * NH * 8 * 768;
constexpr int W_YP = W_SP + NH * DM * 8;
constexpr int W_QP = W_YP + NMLP * DM * 8;
constexpr int W_PC = W_QP + NH * 2 * DH * 8;
constexpr int W_END = W_PC + 24 * 512;
constexpr int W_ST = 4 << 20;

constexpr unsigned LDS_SPIN = 1u << 20, GLB_SPIN = 1u << 16;
constexpr float LOG2E = 1.4426950408889634f;

struct Params {
  const unsigned short *x, *k, *v, *wq, *wo, *wr, *wg, *wu, *wd;
  unsigned short* out;
  unsigned char* ws;
};

__device__ __forceinline__ int rfl(int v) { return __builtin_amdgcn_readfirstlane(v); }
__device__ __forceinline__ float bfr(float x) { return (float)(__bf16)x; }
__device__ __forceinline__ unsigned bfbits(float x) { return (unsigned)__builtin_bit_cast(unsigned short, (__bf16)x); }
__device__ __forceinline__ float lo16(unsigned u) { return __builtin_bit_cast(float, u << 16); }
__device__ __forceinline__ float hi16(unsigned u) { return __builtin_bit_cast(float, u & 0xffff0000u); }
__device__ __forceinline__ float asf(unsigned u) { return __builtin_bit_cast(float, u); }
__device__ __forceinline__ unsigned asu(float f) { return __builtin_bit_cast(unsigned, f); }
__device__ __forceinline__ float dot2(unsigned a, unsigned b, float c) {
  return __builtin_amdgcn_fdot2_f32_bf16(__builtin_bit_cast(bf16x2, a), __builtin_bit_cast(bf16x2, b), c, false);
}
template <int CTRL> __device__ __forceinline__ float dpp(float x) {
  return __builtin_bit_cast(float, __builtin_amdgcn_mov_dpp(__builtin_bit_cast(int, x), CTRL, 0xf, 0xf, true));
}
template <int CTRL> __device__ __forceinline__ int dppi(int x) { return __builtin_amdgcn_mov_dpp(x, CTRL, 0xf, 0xf, true); }
constexpr int XOR1 = 0xB1, XOR2 = 0x4E, MIR8 = 0x141, ROR4 = 0x124, ROR8 = 0x128;
__device__ __forceinline__ float fold32(float a, float b) {
  auto s = __builtin_amdgcn_permlane32_swap(asu(a), asu(b), false, false);
  return asf(s[0]) + asf(s[1]);
}
__device__ __forceinline__ float fold16(float a, float b) {
  auto s = __builtin_amdgcn_permlane16_swap(asu(a), asu(b), false, false);
  return asf(s[0]) + asf(s[1]);
}
__device__ __forceinline__ float max_rows(float x) {
  auto s = __builtin_amdgcn_permlane16_swap(asu(x), asu(x), false, false);
  x = fmaxf(asf(s[0]), asf(s[1]));
  auto t = __builtin_amdgcn_permlane32_swap(asu(x), asu(x), false, false);
  return fmaxf(asf(t[0]), asf(t[1]));
}
__device__ __forceinline__ float sum_rows(float x) { x = fold16(x, x); return fold32(x, x); }
__device__ __forceinline__ float wave_sum(float x) {
  x += dpp<XOR1>(x); x += dpp<XOR2>(x); x += dpp<ROR4>(x); x += dpp<ROR8>(x);
  return sum_rows(x);
}
__device__ __forceinline__ float exp2f_(float x) { return __builtin_amdgcn_exp2f(x); }
__device__ __forceinline__ float siluf(float x) { return x / (1.0f + expf(-x)); }
#define CBAR() asm volatile("" ::: "memory")

__device__ __forceinline__ __amdgpu_buffer_rsrc_t rsrc(const void* p, unsigned bytes) {
  return __builtin_amdgcn_make_buffer_rsrc((void*)p, 0, bytes, 0x00020000);
}
__device__ __forceinline__ void lds_set(LAS unsigned* w, unsigned v) {
  CBAR();
  __hip_atomic_store(w, v, RLX_WG);
  CBAR();
}
template <int SLEEP> __device__ __forceinline__ void lds_wait(LAS unsigned* w, unsigned want) {
#pragma nounroll
  for (unsigned n = 0; n < LDS_SPIN; ++n) {
    if ((unsigned)rfl((int)__hip_atomic_load(w, RLX_WG)) >= want) break;
    if (SLEEP) __builtin_amdgcn_s_sleep(SLEEP);
  }
  CBAR();
}
template <int N, int SLEEP> __device__ __forceinline__ void lds_wait_all(LAS unsigned* w, int j, unsigned want = 1u) {
  LAS unsigned* a = w + (j < N ? j : 0);
#pragma nounroll
  for (unsigned n = 0; n < LDS_SPIN; ++n) {
    if (__all(__hip_atomic_load(a, RLX_WG) >= want)) break;
    if (SLEEP) __builtin_amdgcn_s_sleep(SLEEP);
  }
  CBAR();
}
__device__ __forceinline__ unsigned tag_of(unsigned e) { return ((e & 0xffu) | (((e >> 8) ^ e) & 0xffu) << 8) * 0x10001u; }
__device__ __forceinline__ v2u pack2(float a, float b, unsigned tag) {
  return v2u{__builtin_amdgcn_perm(asu(a), tag, 0x07060500u), __builtin_amdgcn_perm(asu(b), tag, 0x07060501u)};
}
__device__ __forceinline__ v4u pack4(float a, float b, float c, float d, unsigned tag) {
  const v2u x = pack2(a, b, tag), y = pack2(c, d, tag);
  return v4u{x.x, x.y, y.x, y.y};
}
__device__ __forceinline__ bool fresh(v4u g, unsigned tag) {
  return (__builtin_amdgcn_perm(g.y, g.x, 0x0c0c0400u) | __builtin_amdgcn_perm(g.w, g.z, 0x04000c0cu)) == tag;
}
__device__ __forceinline__ float val(unsigned w) { return asf(w & 0xffffff00u); }
__device__ __forceinline__ v4u ld16(__amdgpu_buffer_rsrc_t rw, int off) { return __builtin_amdgcn_raw_buffer_load_b128(rw, off, 0, 16); }
__device__ __forceinline__ void st16(__amdgpu_buffer_rsrc_t rw, int off, v4u v) { __builtin_amdgcn_raw_buffer_store_b128(v, rw, off, 0, 16); }
__device__ __forceinline__ void st8(__amdgpu_buffer_rsrc_t rw, int off, v2u v) { __builtin_amdgcn_raw_buffer_store_b64(v, rw, off, 0, 16); }

struct Stamps {
  u64 t[8];
  __device__ __forceinline__ void flush(const Params& p, int base, int n) {
#pragma unroll
    for (int i = 0; i < 8; ++i) if (i < n) ((GAS u64*)(p.ws + W_ST))[blockIdx.x * 32 + base + i] = t[i];
  }
};
#define STAMP(i) do { __builtin_amdgcn_sched_barrier(0); asm volatile("s_memrealtime %0\n\ts_waitcnt lgkmcnt(0)" : "=s"(st.t[i]) :: "memory"); __builtin_amdgcn_sched_barrier(0); } while (0)
#define STAMPS() Stamps st = {}
#define FLUSH(base, n) st.flush(p, base, n)

__device__ __forceinline__ void layer_norm(v2u (&xr)[5], LAS unsigned char* L, int lane) {
  v2f x[10];
#pragma unroll
  for (int i = 0; i < 5; ++i) { x[2 * i] = v2f{lo16(xr[i].x), hi16(xr[i].x)}; x[2 * i + 1] = v2f{lo16(xr[i].y), hi16(xr[i].y)}; }
  const float p0 = asf((unsigned)__builtin_amdgcn_readlane((int)asu(x[0].x), 0)), p1 = asf((unsigned)__builtin_amdgcn_readlane((int)asu(x[0].x), 32));
  const float piv = lane < 32 ? p0 : p1;
  v2f s1 = {0.f, 0.f}, s2 = {0.f, 0.f};
#pragma unroll
  for (int i = 0; i < 10; ++i) { const v2f d = x[i] - piv; s1 += d; s2 += d * d; }
  float a = s1.x + s1.y, q = s2.x + s2.y;
  a += dpp<XOR1>(a); q += dpp<XOR1>(q); a += dpp<XOR2>(a); q += dpp<XOR2>(q);
  a += dpp<ROR4>(a); q += dpp<ROR4>(q); a += dpp<ROR8>(a); q += dpp<ROR8>(q);
  a = fold16(a, a) * (1.0f / DM); q = fold16(q, q) * (1.0f / DM);
  const float mb = bfr(piv + a), var = bfr(fmaf(-a, a, q));
  const float den = bfr(__builtin_amdgcn_sqrtf(bfr(var + 1e-5f)));
  const float rcp = __builtin_amdgcn_rcpf(den);
#pragma unroll
  for (int i = 0; i < 5; ++i) {
    unsigned o[2];
#pragma unroll
    for (int e = 0; e < 2; ++e) {
      const v2f t = x[2 * i + e] - mb;
      const v2f d = {bfr(t.x), bfr(t.y)}, q0 = d * rcp;
      const v2f qq = (d - q0 * den) * rcp + q0;
      o[e] = bfbits(qq.x) | (bfbits(qq.y) << 16);
    }
    *(LAS v2u*)(L + L_XP + (lane >> 5) * 1280 + ((lane & 31) + 32 * i) * 8) = v2u{o[0], o[1]};
  }
}

template <int NK, int KSTEP, int HALF>
__device__ __forceinline__ void load_bt(const LAS unsigned char* b, v4u (&f)[NK]) {
#pragma unroll
  for (int ks = 0; ks < NK; ++ks) {
    const v2u lo = __builtin_bit_cast(v2u, __builtin_amdgcn_ds_read_tr16_b64_v4i16((LAS v4s*)(b + ks * KSTEP)));
    const v2u hi = __builtin_bit_cast(v2u, __builtin_amdgcn_ds_read_tr16_b64_v4i16((LAS v4s*)(b + ks * KSTEP + HALF)));
    f[ks] = v4u{lo.x, lo.y, hi.x, hi.y};
  }
}
template <int NK> __device__ __forceinline__ v4f mm2(const LAS unsigned char* a, const v4u (&f)[NK], v4f c = v4f{0.f, 0.f, 0.f, 0.f}) {
  v4u av[NK];
#pragma unroll
  for (int ks = 0; ks < NK; ++ks) av[ks] = *(const LAS v4u*)(a + ks * 64);
#pragma unroll
  for (int ks = 0; ks < NK; ++ks) c = __builtin_amdgcn_mfma_f32_16x16x32_bf16(__builtin_bit_cast(bf16x8, av[ks]), __builtin_bit_cast(bf16x8, f[ks]), c, 0, 0, 0);
  return c;
}

#define DMA(OFF) "s_waitcnt vmcnt(%4)\n\tbuffer_load_dwordx4 %0, %1, %2 offen " OFF "nt lds"
template <int N> __device__ __forceinline__ void dma3(int voff, v4u srd, int soff, unsigned lds) {
  asm volatile("s_mov_b32 m0, %3\n\t" DMA("") "\n\t" DMA("offset:1024 ") "\n\t" DMA("offset:2048 ")
               :: "v"(voff), "s"(srd), "s"(soff), "s"(lds), "i"(N) : "memory");
}
template <int N> __device__ __forceinline__ void dma1(int voff, v4u srd, int soff, unsigned lds) {
  asm volatile("s_mov_b32 m0, %3\n\t" DMA("") :: "v"(voff), "s"(srd), "s"(soff), "s"(lds), "i"(N) : "memory");
}
__device__ __forceinline__ v4u srd_of(const void* ptr, unsigned bytes) {
  const u64 a = (u64)ptr;
  return v4u{(unsigned)a, (unsigned)(a >> 32) & 0xffffu, bytes, 0x00020000u};
}
constexpr int fdiv6(int a) { return a >= 0 ? a / 6 : -((5 - a) / 6); }
constexpr int lag3(int w) { return fdiv6(3 - w); }
constexpr int lag6(int w) { return fdiv6(6 - w); }

constexpr int R0 = K_R0, A1 = K_A1, C1 = K_C1, B1 = 8 * R0 - 6 * A1 - C1;
static_assert(B1 >= A1 && C1 >= 0 && 2 * (B1 + 32 - R0) <= 16 * NC, "member 6 has no prologue; a wave keeps the scores of 16 slots");
struct Plan {
  bool mlp;
  int npro, n1;
  __device__ __forceinline__ Plan(int h, int m) {
    mlp = m == 7 && h < NMLP;
    npro = m < 6 ? NS_WQ : mlp ? NS_MLP : 0;
    n1 = h < NMLP ? (m < 6 ? A1 : m == 6 ? B1 : C1) : R0;
  }
  __device__ __forceinline__ int piece(int i, int b, int h, int m) const {
    if (b == 0 && i < n1 && h < NMLP) return m == 7 ? 6 * A1 + B1 + i : i < A1 ? 7 * i + m : 6 * A1 + i;
    const int rho = b ? i : i - n1 + R0;
    return rho * 8 + ((m + rho * K_ROT) & 7);
  }
  __device__ __forceinline__ int len0() const { return 2 * (n1 + 32 - R0); }
  __device__ __forceinline__ int ntot() const { return npro + 2 * len0() + 128; }
  __device__ __forceinline__ int wo_skip() const { const int r = ntot() % NSLOT; return r + NWO > NSLOT ? NSLOT - r : 0; }
};
__device__ __forceinline__ int piece_off(int pc, int b, int h) { return (b * NH + h) * SLAB + (((pc >> 3) * K_STR + h * K_HROT) & 31) * 98304 + (pc & 7) * 12288; }

struct Loader {
  LAS unsigned* ctl;
  unsigned lbase;
  int s, pos, limit, pub;
  unsigned next_addr;
  u64 stall = 0;
  unsigned t0 = 0;
  int due = 0, pace = 0;
  int now = 0, tend = 0, tnew = K_TEND << 12,
      slast = 0, pmax = 1 << 30, last = 0, shift = 0;
  __device__ __forceinline__ void publish(int n) { pub = max(pub, n); __hip_atomic_store(ctl + C_LANDED, (unsigned)pub, RLX_WG); }
  __device__ __forceinline__ void credit(int last) {
    if (__builtin_expect(last >= limit, 0)) {
#pragma nounroll
      for (unsigned n = 0; n < LDS_SPIN; ++n) {
        int v = (int)__hip_atomic_load((LAS unsigned*)(size_t)next_addr, RLX_WG);
        v = min(v, dppi<XOR1>(v)); v = min(v, dppi<XOR2>(v)); v = min(v, dppi<MIR8>(v));
        limit = rfl(v) + NSLOT;
        if (last < limit) break;
        if (n == 8) { asm volatile("s_waitcnt vmcnt(0)" ::: "memory"); publish(s); }
        __builtin_amdgcn_s_sleep(1);
      }
      CBAR();
    }
  }
  __device__ __forceinline__ unsigned dst() const { return lbase + pos * SLOTB; }
  __device__ __forceinline__ void advance() { ++s; pos = pos + 1 == NSLOT ? 0 : pos + 1; }
  template <int W> __device__ __forceinline__ void rows_w(int voff, v4u srd, int so) {
    int v;
    u64 t;
    asm volatile("ds_read_b32 %0, %1" : "=v"(v) : "v"(next_addr) : "memory");
    dma3<W - 1>(voff, srd, so, dst());
    asm volatile("s_waitcnt lgkmcnt(0)" : "+v"(v));
    tnew = __builtin_amdgcn_readlane(v, 8);
    v = min(v, dppi<XOR1>(v)); v = min(v, dppi<XOR2>(v));
    asm volatile("" : "+v"(v));
    if (lag3(W) == lag6(W)) publish(s + lag3(W));
    dma3<W - 1>(voff, srd, so + 3072, dst() + 3072);
    limit = rfl(min(v, dppi<MIR8>(v))) + NSLOT;
    if (lag3(W) != lag6(W)) publish(s + lag6(W));
    advance();
    dma3<W - 1>(voff, srd, so + 6144, dst());
    if (lag3(W) == lag6(W)) publish(s + lag3(W));
    dma3<W - 1>(voff, srd, so + 9216, dst() + 3072);
    asm volatile("s_memrealtime %0" : "=s"(t));
    if (lag3(W) != lag6(W)) publish(s + lag6(W));
    advance();
    asm volatile("s_waitcnt lgkmcnt(0)" : "+s"(t));
    now = (int)((unsigned)t - t0);
  }
  __device__ __forceinline__ void rows(int voff, v4u srd, int so) {
    credit(s + 1);
    if (__builtin_expect(now - last > K_STALL, 0)) { const int d = now - last - (pace >> 8); shift += d; due += d << 8; stall = last; }
    if (__builtin_expect(tnew != tend, 0)) {
      tend = tnew;
      due = max(due, (now - K_LATE - K_KEEP) << 8);
      pace = min(max((((tend >> 12) + shift - (tend & 0xfff)) * 256 - due) * 2 / (slast - s), 0), pmax);
    }
#pragma nounroll
    for (unsigned n = 0; now < (due >> 8) && n < LDS_SPIN; ++n) now = clock();
    last = now;
    const int late = now - (due >> 8);
    __hip_atomic_store(ctl + C_LATE, (unsigned)(min(late, 0xffff) | min(shift, 0xfff) << 16), RLX_WG);
    due += pace;
    if (late <= (s + K_NEND < slast ? K_LATE : K_LATE2)) rows_w<INF>(voff, srd, so); else rows_w<K_WB>(voff, srd, so);
  }
  __device__ __forceinline__ int clock() const { return (int)((unsigned)__builtin_amdgcn_s_memrealtime() - t0); }
};

template <int NP> __device__ __forceinline__ v2f sum_producers(__amdgpu_buffer_rsrc_t rw, int base, unsigned tag, int lane) {
  const int off = base + (lane >> 2) * (DM * 8) + blockIdx.x * 64 + (lane & 3) * 16;
  const bool two = lane < 4 * (NP - 16);
  v4u a, c;
#pragma nounroll
  for (unsigned n = 0; n < GLB_SPIN; ++n) {
    CBAR();
    a = ld16(rw, off);
    c = ld16(rw, two ? off + 16 * (DM * 8) : off);
    const u64 bad = __builtin_amdgcn_ballot_w64(!(fresh(a, tag) && fresh(c, tag)));
    if (bad == 0) break;
    if (K_SL2 && __builtin_popcountll(bad) > 12) __builtin_amdgcn_s_sleep(K_SL2);
  }
  if (!two) c = v4u{0u, 0u, 0u, 0u};
  float s0 = fold32(val(a.x) + val(c.x), val(a.y) + val(c.y)), s1 = fold32(val(a.z) + val(c.z), val(a.w) + val(c.w));
  s0 = fold16(s0, s0); s1 = fold16(s1, s1);
  s0 += dpp<ROR8>(s0); s1 += dpp<ROR8>(s1);
  return v2f{bfr(s0 + dpp<ROR4>(s0)), bfr(s1 + dpp<ROR4>(s1))};
}

__device__ __forceinline__ void combine(const Params& p, LAS unsigned char* L, int lane, int b) {
  LAS unsigned* ctl = (LAS unsigned*)(L + L_CTL);
  const int h = K_XMAP ? blockIdx.x >> 3 : blockIdx.x & 31, l48 = min(lane, 47);
  const __amdgpu_buffer_rsrc_t rw = rsrc(p.ws, W_END);
  const unsigned tag = (unsigned)rfl((int)ctl[C_TAG]);
  const int off = W_OP + (b * NH + h) * 8 * 768 + l48 * 16;
  v4f g[8];
  v4u mv;
  unsigned miss = 0xffu;
#pragma unroll
  for (int k = 0; k < 8; ++k) g[k] = v4f{0.f, 0.f, 0.f, 0.f};
  if (K_D1) __builtin_amdgcn_s_sleep(K_D1);
#pragma nounroll
  for (unsigned n = 0; n < GLB_SPIN; ++n) {
    v4u t[8];
    CBAR();
    mv = ld16(rw, W_ML + (((K_EARLY0 ? b : 1) * NH + h) * 8 + (lane & 7)) * 16);
#pragma unroll
    for (int k = 0; k < 8; ++k)
      if (miss >> k & 1) t[k] = ld16(rw, off + k * 768);
#pragma unroll
    for (int k = 0; k < 8; ++k)
      if (miss >> k & 1) {
        if (__all(fresh(t[k], tag))) { g[k] = v4f{val(t[k].x), val(t[k].y), val(t[k].z), val(t[k].w)}; miss ^= 1u << k; }
      }
    if (miss == 0u && __all(fresh(mv, tag))) break;
    if (K_SL1 && __builtin_popcount(miss) > K_FEW) __builtin_amdgcn_s_sleep(K_SL1);
  }
  const float Mk = val(b ? mv.z : mv.x);
  float M = Mk;
  M = fmaxf(M, dpp<XOR1>(M)); M = fmaxf(M, dpp<XOR2>(M)); M = fmaxf(M, dpp<MIR8>(M));
  float w = exp2f_(Mk - M), Ls = w * val(b ? mv.w : mv.y);
  Ls += dpp<XOR1>(Ls); Ls += dpp<XOR2>(Ls); Ls += dpp<MIR8>(Ls);
  w *= __builtin_amdgcn_rcpf(Ls);
  v4f o = {0.f, 0.f, 0.f, 0.f};
#pragma unroll
  for (int k = 0; k < 8; ++k) o += asf((unsigned)__builtin_amdgcn_readlane((int)asu(w), k)) * g[k];
  *(LAS v2u*)(L + L_OA + b * (DH * 2) + l48 * 8) = v2u{bfbits(o.x) | (bfbits(o.y) << 16), bfbits(o.z) | (bfbits(o.w) << 16)};
  lds_set(ctl + C_O + b, 1u);
}

__device__ __forceinline__ void run_loader(const Params& p, LAS unsigned char* L, int lane, u64 tentry) {
  LAS unsigned* ctl = (LAS unsigned*)(L + L_CTL);
  const int bid = blockIdx.x, h = K_XMAP ? bid >> 3 : bid & 31, m = K_XMAP ? bid & 7 : bid >> 5;
  const int r = lane >> 3, j = lane & 7;
  __builtin_amdgcn_s_setprio(3);
  STAMPS();
  Loader ld{ctl, (unsigned)(size_t)L, 0, 0, NSLOT, 0, (unsigned)(size_t)(r == 1 ? ctl + C_TEND : ctl + C_NEXT + (j < NC ? j : 0))};
  const int voff = lane * 16;
  if (__builtin_expect(m < 6, 1)) {
    const v4u rq = srd_of(p.wq, DM * NH * DH * 2);
    const int vo = r * (NH * DH * 2) + (j ^ (r & 2)) * 16, so = (m / 3) * 320 * (NH * DH * 2) + h * (DH * 2) + (m % 3) * 128;
#pragma unroll 2
    for (int g = 0; g < 6 * NS_WQ; ++g) dma1<63>(g & 1 ? vo ^ 64 : vo, rq, so + min(g, 39) * (8 * NH * DH * 2), ld.lbase + g * 1024);
    ld.s = ld.pos = NS_WQ;
  }
  asm volatile("s_waitcnt lgkmcnt(0)\n\ts_barrier" ::: "memory");
  STAMP(1);
  const Plan pl(h, m);
  ld.t0 = (unsigned)tentry;
  ld.slast = pl.ntot();
  if (pl.mlp) {
    const v4u rg = srd_of(p.wg, DM * FF * 2), ru = srd_of(p.wu, DM * FF * 2), rd = srd_of(p.wd, FF * DM * 2);
    const int vo = r * (FF * 2) + (j ^ (r & 2)) * 16;
    constexpr int W = K_WMLP;
    const int sd = h * 64 * (DM * 2);
    auto slot = [&](int pc0, int sg) {
#pragma unroll
      for (int q = 0; q < 6; ++q) {
        const int pc = pc0 + q;
        if (pc < 16 || sg >= 0) dma1<W - 1>(q & 1 ? vo ^ 64 : vo, pc & 4 ? ru : rg, (sg < 0 ? 72 : sg) * (8 * FF * 2) + ((pc >> 3) * 4 + (pc & 3)) * (8 * FF * 2) + h * 128, ld.dst() + q * 1024);
        else dma1<W - 1>(voff, rd, sd + (pc - 16) * 1024, ld.dst() + q * 1024);
      }
      ld.publish(ld.s + lag6(W));
      ld.advance();
    };
#pragma nounroll
    for (int t = 0; t < 6; ++t) {
#pragma unroll
      for (int u = 0; u < 4; ++u) slot(6 * u, 12 * t);
    }
#pragma unroll
    for (int u = 0; u < 3; ++u) { ld.credit(ld.s); slot(6 * u, -1); }
#pragma nounroll
    for (int t = 27; t < NS_MLP; ++t) {
      ld.credit(ld.s);
      dma3<W - 1>(voff, rd, sd + (6 * t - 160) * 1024, ld.dst());
      dma3<W - 1>(voff, rd, sd + (6 * t - 157) * 1024, ld.dst() + 3072);
      ld.publish(ld.s + lag6(W));
      ld.advance();
    }
  }
  STAMP(2);
  ld.now = ld.clock();
  ld.last = ld.now;
  ld.due = ld.now << 8;
  ld.tend = K_TEND << 12;
  ld.pace = (K_TEND * 256 - ld.due) * 2 / (ld.slast - ld.s);
  ld.pmax = ld.pace + (ld.pace >> 3);
#pragma nounroll
  for (int ph = 0; ph < 4; ++ph) {
    const v4u rs = srd_of(ph & 1 ? p.v : p.k, KV_BYTES);
    const int b = ph >> 1, np = b ? 32 : pl.len0() / 2;
#pragma nounroll
    for (int i = 0; i < np; ++i) ld.rows(voff, rs, piece_off(pl.piece(i, b, h, m), b, h));
    if (ph == 0) STAMP(5); else if (ph == 1) STAMP(6); else if (ph == 2) STAMP(7);
  }
  STAMP(3);
  const v4u ro = srd_of(p.wo, NH * DH * DM * 2);
  int row = lane / 10, ch = lane - row * 10;
  ld.credit(ld.s + pl.wo_skip() + NWO - 1);
  if (pl.wo_skip()) ld.pos = 0;
#pragma nounroll
  for (int t = 0; t < NWO; ++t) {
#pragma unroll
    for (int q = 0; q < 6; ++q) {
      dma1<K_WWO - 1>(row * (DM * 2) + ch * 16, ro, h * DH * (DM * 2) + m * 160, ld.dst() + q * 1024);
      if (q == 2 && lag3(K_WWO) == lag6(K_WWO)) ld.publish(ld.s + lag3(K_WWO));
      if (q == 5 && lag3(K_WWO) != lag6(K_WWO)) ld.publish(ld.s + lag6(K_WWO));
      row += 6; ch += 4;
      if (ch >= 10) { ch -= 10; row += 1; }
    }
    ld.advance();
  }
  if (K_WWO > 48) { asm volatile("s_waitcnt vmcnt(48)" ::: "memory"); ld.publish(ld.s - 8); }
  if (K_WWO > 42) { asm volatile("s_waitcnt vmcnt(42)" ::: "memory"); ld.publish(ld.s - 7); }
  if (K_WWO > 36) { asm volatile("s_waitcnt vmcnt(36)" ::: "memory"); ld.publish(ld.s - 6); }
  if (K_WWO > 30) { asm volatile("s_waitcnt vmcnt(30)" ::: "memory"); ld.publish(ld.s - 5); }
  asm volatile("s_waitcnt vmcnt(0)" ::: "memory");
  ld.publish(ld.s);
  STAMP(4);
  if (bid < NFIN) {
    const v2f y = sum_producers<NMLP>(rsrc(p.ws, W_END), W_YP, (unsigned)rfl((int)ctl[C_TAG]), lane);
    const float sg = *(const LAS float*)(L + L_G + lane * 4);
    const unsigned xn = *(const LAS unsigned*)(L + L_XP + (lane >> 5) * 1280 + bid * 16 + (lane & 3) * 4);
    *(LAS v2f*)(L + L_G + 256 + lane * 8) = v2f{bfr(siluf(bfr(lo16(xn) + bfr(y.x * sg)))), bfr(siluf(bfr(hi16(xn) + bfr(y.y * sg))))};
    lds_set(ctl + C_G, 1u);
  }
  st.t[2] = tentry + ld.stall; st.t[0] = tentry; st.t[1] = tentry + ld.shift;
  FLUSH(0, 8);
}

struct Early { v2u xr[5], wr[5]; unsigned e0, xo; };
__device__ __forceinline__ void early_loads(const Params& p, Early& e, int lane) {
#pragma unroll
  for (int i = 0; i < 5; ++i) e.xr[i] = ((const GAS v2u*)p.x)[(lane >> 5) * 160 + (lane & 31) + 32 * i];
  e.e0 = __hip_atomic_load((GAS unsigned*)(p.ws + W_EPOCH), RLX_AGENT);
}
__device__ __forceinline__ void run_helper(const Params& p, LAS unsigned char* L, int lane, Early& e) {
  LAS unsigned* ctl = (LAS unsigned*)(L + L_CTL);
  const int bid = blockIdx.x, h = K_XMAP ? bid >> 3 : bid & 31, m = K_XMAP ? bid & 7 : bid >> 5;
  const int j = lane & 7;
  const __amdgpu_buffer_rsrc_t rw = rsrc(p.ws, W_END);
  STAMPS();
  const int fb = lane >> 5, col = min(bid, NFIN - 1) * 8 + 2 * (lane & 3), l48 = min(lane, 47);
#pragma unroll
  for (int i = 0; i < 5; ++i) e.wr[i] = ((const GAS v2u*)p.wr)[(lane & 31) + 32 * i];
  e.xo = ((const GAS unsigned*)p.x)[(fb * DM + col) >> 1];
  __builtin_amdgcn_sched_barrier(0);
  layer_norm(e.xr, L, lane);
  const unsigned epoch = (unsigned)rfl((int)e.e0) + 1u, tag = tag_of(epoch);
  ctl[C_TAG] = tag;
  lds_set(ctl + C_XOK, 1u);
  STAMP(0);
  {
    lds_wait<2>(ctl + C_LANDED, K_QWAIT);
    const int off = W_QP + (h * 2 * DH + 4 * l48) * 8;
    v4u g[4];
#pragma nounroll
    for (unsigned n = 0; n < GLB_SPIN; ++n) {
      CBAR();
#pragma unroll
      for (int i = 0; i < 4; ++i) g[i] = ld16(rw, off + (i & 1) * 16 + (i >> 1) * (DH * 8));
      if (__all(fresh(g[0], tag) && fresh(g[1], tag) && fresh(g[2], tag) && fresh(g[3], tag))) break;
    }
#pragma unroll
    for (int b = 0; b < 2; ++b)
      *(LAS v2u*)(L + L_QB + b * (DH * 2) + l48 * 8) = v2u{bfbits(val(g[0][b]) + val(g[2][b])) | (bfbits(val(g[0][2 + b]) + val(g[2][2 + b])) << 16),
                                                            bfbits(val(g[1][b]) + val(g[3][b])) | (bfbits(val(g[1][2 + b]) + val(g[3][2 + b])) << 16)};
    lds_set(ctl + C_Q, 1u);
  }
  float rs = 0.f;
#pragma unroll
  for (int i = 0; i < 5; ++i) {
    const v2u xx = *(const LAS v2u*)(L + L_XP + fb * 1280 + ((lane & 31) + 32 * i) * 8);
    rs = dot2(e.wr[i].y, xx.y, dot2(e.wr[i].x, xx.x, rs));
  }
  rs += dpp<XOR1>(rs); rs += dpp<XOR2>(rs); rs += dpp<ROR4>(rs); rs += dpp<ROR8>(rs);
  *(LAS float*)(L + L_G + lane * 4) = bfr(1.0f / (1.0f + expf(-bfr(fold16(rs, rs)))));
  v4u po[2], pml;
  v2f st2[2];
#pragma unroll
  for (int b = 0; b < 2; ++b) {
    lds_wait_all<NC, 16>(ctl + C_ML, j, b + 1);
    v2f ml = *(const LAS v2f*)(L + L_MLW + b * 64 + j * 8);
    if (j >= NC) ml = v2f{-3.0e38f, 0.f};
    float M = ml.x;
    M = fmaxf(M, dpp<XOR1>(M)); M = fmaxf(M, dpp<XOR2>(M)); M = fmaxf(M, dpp<MIR8>(M));
    M = val(asu(M));
    const float w = exp2f_(ml.x - M);
    *(LAS float*)(L + L_F + b * 32 + j * 4) = w;
    lds_set(ctl + C_F, b + 1);
    float Ls = w * ml.y;
    Ls += dpp<XOR1>(Ls); Ls += dpp<XOR2>(Ls); Ls += dpp<MIR8>(Ls);
    st2[b] = v2f{M, Ls};
    if (b == 0) {
      lds_wait_all<NC, 16>(ctl + C_R, j, 1);
    } else {
      if (K_EARLY0) combine(p, L, lane, 0);
      pml = pack4(st2[0].x, st2[0].y, st2[1].x, st2[1].y, tag);
      asm volatile("" : "+v"(pml), "+v"(po[0]));
      lds_wait_all<NC, 0>(ctl + C_R, j, 2);
    }
    STAMP(1);
    v4f o = {0.f, 0.f, 0.f, 0.f};
#pragma unroll
    for (int c = 0; c < NC; ++c) o += *(const LAS v4f*)(L + L_REC + c * 768 + l48 * 16);
    po[b] = pack4(o.x, o.y, o.z, o.w, tag);
    if (b == 0) {
      lds_set(ctl + C_RFREE, 1u);
      if (K_EARLY0) {
        if (lane < 48) st16(rw, W_OP + (h * 8 + m) * 768 + lane * 16, po[0]);
        if (lane == 0) st16(rw, W_ML + (h * 8 + m) * 16, pack4(M, Ls, M, Ls, tag));
      }
    }
  }
  if (lane < 48) {
    st16(rw, W_OP + ((NH + h) * 8 + m) * 768 + lane * 16, po[1]);
    if (!K_EARLY0) st16(rw, W_OP + (h * 8 + m) * 768 + lane * 16, po[0]);
  }
  if (lane == 0) st16(rw, W_ML + ((NH + h) * 8 + m) * 16, pml);
  lds_set(ctl + C_PUB, 1u);
  STAMP(2);
  combine(p, L, lane, 1);
  STAMP(4);
  if (bid < NFIN) {
    lds_wait_all<5, 0>(ctl + C_T3, j);
    lds_wait<0>(ctl + C_G, 1);
    const v2f gt = *(const LAS v2f*)(L + L_G + 256 + lane * 8);
    STAMP(5);
    if (K_D2) __builtin_amdgcn_s_sleep(K_D2);
    const v2f sv = sum_producers<NH>(rw, W_SP, tag, lane);
    STAMP(6);
    const unsigned ov = bfbits(lo16(e.xo) + bfr(sv.x * gt.x)) | (bfbits(hi16(e.xo) + bfr(sv.y * gt.y)) << 16);
    if ((lane & 31) < 4) ((GAS unsigned*)p.out)[(fb * DM + col) >> 1] = ov;
    if (bid == 0 && lane == 0) __hip_atomic_store((GAS unsigned*)(p.ws + W_EPOCH), epoch, RLX_AGENT);
    STAMP(7);
  }
  st.t[3] = st.t[4];
  FLUSH(8, bid < NFIN ? 8 : 5);
}

__device__ __forceinline__ void run_consumer(const Params& p, LAS unsigned char* L, int lane, int c) {
  LAS unsigned* ctl = (LAS unsigned*)(L + L_CTL);
  const int bid = blockIdx.x, h = K_XMAP ? bid >> 3 : bid & 31, m = K_XMAP ? bid & 7 : bid >> 5;
  const int r = lane >> 3, j = lane & 7;
  const Plan pl(h, m);
  const __amdgpu_buffer_rsrc_t rw = rsrc(p.ws, W_END);
  STAMPS();
  if (pl.npro == 0 || (m < 6 && c >= 4)) lds_set(ctl + C_NEXT + c, pl.npro + c);
  lds_wait<1>(ctl + C_XOK, 1);
  const unsigned tag = (unsigned)rfl((int)ctl[C_TAG]);
  const int g = lane >> 4;
  const int bq = (lane >> 4) * 1024 + ((lane & 15) >> 2) * 128 + ((2 * c + ((lane & 3) >> 1)) ^ (2 * (r & 3))) * 16 + (lane & 1) * 8;
  v2u yp[7] = {};
  auto put_y = [&]() {
#pragma unroll
    for (int k = 0; k < 7; ++k)
      if (lane < 16 && c + NC * k < DM / 16) st8(rw, W_YP + (h * DM + (c + NC * k) * 16 + lane) * 8, yp[k]);
  };
  if (m < 6) {
    if (c < 4) {
      lds_wait<0>(ctl + C_LANDED, NS_WQ);
      v4u bf[10];
      load_bt<10, 4096, 512>(L + bq, bf);
      lds_set(ctl + C_NEXT + c, NS_WQ + c);
      const v4f qv = mm2<10>(L + L_XP + (lane & 1) * 1280 + (m / 3) * 640 + (lane >> 4) * 16, bf);
      if (lane < 16) st8(rw, W_QP + ((h * 2 + m / 3) * DH + (m % 3) * 64 + 16 * c + lane) * 8, pack2(qv.x, qv.y, tag));
    }
  } else if (pl.mlp) {
    if (c < 4) {
      const LAS unsigned char* ap = L + L_XP + (lane & 1) * 1280 + g * 16;
      v4f ga = {0.f, 0.f, 0.f, 0.f}, ua = {0.f, 0.f, 0.f, 0.f};
#pragma nounroll
      for (int t = 0; t < 20; t += 2) {
        lds_wait<1>(ctl + C_LANDED, (8 * t + 21) / 6);
        v4u gf[2], uf[2];
        load_bt<2, 8192, 512>(L + (t < 18 ? t : t - 18) * 8192 + bq, gf);
        load_bt<2, 8192, 512>(L + (t < 18 ? t : t - 18) * 8192 + 4096 + bq, uf);
        lds_set(ctl + C_NEXT + c, (8 * t + 16) / 6);
        ga = mm2<2>(ap + t * 64, gf, ga);
        ua = mm2<2>(ap + t * 64, uf, ua);
      }
      if (lane < 32) *(LAS unsigned short*)(L + L_HM + g * 128 + (16 * c + (lane & 15)) * 2) = (unsigned short)bfbits(bfr(siluf(bfr(g ? ga[1] : ga[0]))) * bfr(g ? ua[1] : ua[0]));
      lds_set(ctl + C_HM + c, 1u);
    } else {
      lds_set(ctl + C_NEXT + c, 160 / 6);
    }
    lds_wait_all<4, 1>(ctl + C_HM, j);
    lds_wait<1>(ctl + C_LANDED, NS_MLP);
#pragma unroll
    for (int k = 0; k < 7; ++k) {
      const int n = min(c + NC * k, DM / 16 - 1);
      v4u bf[2];
      load_bt<2, 32 * 1280, 4 * 1280>(L + (160 * 1024 - RINGB) + (8 * g + ((lane & 15) >> 2)) * 1280 + n * 32 + (lane & 3) * 8, bf);
      const v4f y = mm2<2>(L + L_HM + (lane & 1) * 128 + g * 16, bf);
      yp[k] = pack2(y[0], y[1], tag);
    }
    if (!K_YLATE) put_y();
    lds_set(ctl + C_NEXT + c, NS_MLP + c);
  }
  STAMP(0);
  const int ntot = pl.ntot();
  int s = pl.npro + c;
  auto take = [&](int i, int len) -> const LAS unsigned char* {
    lds_wait<1>(ctl + C_LANDED, s + 1);
    const LAS unsigned char* sp = L + s % NSLOT * SLOTB;
    s += i + NC < len ? NC : len - i + c;
    return sp;
  };
  auto fetch = [&](v4u (&kk)[6], int i, int len) {
    const LAS unsigned char* sp = take(i, len) + r * 384 + j * 16;
#pragma unroll
    for (int u = 0; u < 6; ++u) kk[u] = *(const LAS v4u*)(sp + (u / 3) * 3072 + (u % 3) * 128);
    lds_set(ctl + C_NEXT + c, s);
  };
  v4u e0[6], e1[6];
  fetch(e0, c, pl.len0());
  fetch(e1, c + NC, pl.len0());
  lds_wait<1>(ctl + C_Q, 1);
  STAMP(1);
  LAS unsigned char* pw = L + L_P + c * 512;
#pragma nounroll
  for (int b = 0; b < 2; ++b) {
    const int len = b ? 64 : pl.len0();
    v4u q[3];
#pragma unroll
    for (int u = 0; u < 3; ++u) q[u] = *(const LAS v4u*)(L + L_QB + (b * DH + (j + 8 * u) * 8) * 2);
    float sc[4] = {-3.0e38f, -3.0e38f, -3.0e38f, -3.0e38f};
    auto score = [&](const v4u (&kk)[6], int it) {
      float d0 = 0.f, d1 = 0.f;
#pragma unroll
      for (int u = 0; u < 3; ++u)
#pragma unroll
        for (int e = 0; e < 4; ++e) { d0 = dot2(kk[u][e], q[u][e], d0); d1 = dot2(kk[3 + u][e], q[u][e], d1); }
      d0 += dpp<XOR1>(d0); d1 += dpp<XOR1>(d1); d0 += dpp<XOR2>(d0); d1 += dpp<XOR2>(d1);
      const float d = (j & 4 ? d1 : d0) + dpp<MIR8>(j & 4 ? d0 : d1);
      const float t = bfr(bfr(d) * 0.07216878364870322f) * LOG2E;
#pragma unroll
      for (int k = 0; k < 4; ++k) sc[k] = it == 4 * k + (j & 3) ? t : sc[k];
    };
    int i = c, it = 0;
    if (b == 0) { score(e0, 0); score(e1, 1); i += 2 * NC; it = 2; }
#pragma nounroll
    for (; i < len; i += NC, ++it) {
      v4u kk[6];
      fetch(kk, i, len);
      score(kk, it);
    }
    float M = fmaxf(fmaxf(sc[0], sc[1]), fmaxf(sc[2], sc[3]));
    M = fmaxf(M, dpp<XOR1>(M)); M = fmaxf(M, dpp<XOR2>(M)); M = fmaxf(M, dpp<ROR4>(M)); M = fmaxf(M, dpp<ROR8>(M));
    M = max_rows(M);
    float ls = 0.f;
#pragma unroll
    for (int k = 0; k < 4; ++k) {
      const float pr = exp2f_(sc[k] - M);
      ls += pr;
      *(LAS unsigned short*)(pw + k * 128 + (j & 3) * 32 + (r + 8 * (j >> 2)) * 2) = (unsigned short)bfbits(pr);
    }
    ls = wave_sum(ls);
    *(LAS v2f*)(L + L_MLW + b * 64 + c * 8) = v2f{M, ls};
    lds_set(ctl + C_ML + c, b + 1);
    v4f acc[12];
#pragma unroll
    for (int t = 0; t < 12; ++t) acc[t] = v4f{0.f, 0.f, 0.f, 0.f};
    float f = 0.f;
#pragma nounroll
    for (i = c, it = 0; i < len; i += NC, ++it) {
      if (K_YLATE && b && pl.mlp && i + K_YLATE * NC >= len && i + (K_YLATE - 1) * NC < len) put_y();
      if (i + NC >= len) {
        lds_wait<1>(ctl + C_F, b + 1);
        if (b) lds_wait<1>(ctl + C_RFREE, 1);
        f = *(const LAS float*)(L + L_F + b * 32 + c * 4);
      }
      const LAS unsigned char* sp = take(i, len) + (lane >> 2) * 384 + (lane & 3) * 8;
      const v4s pa = *(const LAS v4s*)(pw + it * 32 + g * 8);
      v4s vf[12];
#pragma unroll
      for (int t = 0; t < 12; ++t) vf[t] = __builtin_amdgcn_ds_read_tr16_b64_v4i16((LAS v4s*)(sp + t * 32));
      lds_set(ctl + C_NEXT + c, s);
#pragma unroll
      for (int t = 0; t < 12; ++t) acc[t] = __builtin_amdgcn_mfma_f32_16x16x16bf16_1k(pa, vf[t], acc[t], 0, 0, 0);
    }
    if (b) STAMP(3); else STAMP(2);
#pragma unroll
    for (int k = 0; k < 3; ++k) {
      const float v = g & 2 ? (g & 1 ? acc[9 + k][0] : acc[6 + k][0]) : (g & 1 ? acc[3 + k][0] : acc[k][0]);
      *(LAS float*)(L + L_REC + c * 768 + (g * 48 + k * 16 + (lane & 15)) * 4) = v * f;
    }
    lds_set(ctl + C_R + c, b + 1);
  }
  STAMP(4);
  if (c == 5) {
    if (K_EARLY0) return;
    lds_wait<0>(ctl + C_PUB, 1);
    combine(p, L, lane, 0);
    return;
  }
  lds_wait<0>(ctl + C_LANDED, ntot + NWO);
  v4u bf[6];
  load_bt<6, 32 * 160, 4 * 160>(L + (ntot + pl.wo_skip()) % NSLOT * SLOTB + ((lane >> 4) * 8 + ((lane & 15) >> 2)) * 160 + c * 32 + (lane & 3) * 8, bf);
  STAMP(5);
  lds_wait_all<2, 0>(ctl + C_O, j);
  STAMP(6);
  const v4f sv = mm2<6>(L + L_OA + (lane & 1) * (DH * 2) + (lane >> 4) * 16, bf);
  if (lane < 16) st8(rw, W_SP + (h * DM + m * 80 + c * 16 + lane) * 8, pack2(sv.x, sv.y, tag));
  lds_set(ctl + C_T3 + c, 1u);
  STAMP(7);
  if (c == 0) FLUSH(16, 8);
  if (c == 4) FLUSH(24, 8);
}

__device__ __forceinline__ void run_pacer(const Params& p, LAS unsigned char* L, int lane, u64 tentry) {
  LAS unsigned* ctl = (LAS unsigned*)(L + L_CTL);
  const int bid = blockIdx.x;
  const __amdgpu_buffer_rsrc_t rw = rsrc(p.ws, W_END);
  auto until = [&](int t) {
    int now = 0;
#pragma nounroll
    for (unsigned n = 0; n < LDS_SPIN; ++n) {
      now = (int)((unsigned)__builtin_amdgcn_s_memrealtime() - (unsigned)tentry);
      if (now >= t) break;
      __builtin_amdgcn_s_sleep(3);
    }
    return now;
  };
  lds_wait<16>(ctl + C_XOK, 1);
  const unsigned tag = (unsigned)rfl((int)ctl[C_TAG]);
  const int h = K_XMAP ? bid >> 3 : bid & 31;
  auto touch = [&](int ph) {
    const GAS unsigned char* a = ph < 4 ? (const GAS unsigned char*)(ph & 1 ? p.v : p.k) + (size_t)((ph >> 1) * NH + h) * SLAB + min(lane << 20, SLAB - 128)
                                        : (const GAS unsigned char*)p.wo + (size_t)h * (DH * DM * 2) + (lane & 1) * (DH * DM * 2 - 128);
    if (K_TOUCH && lane < 4 && (bid >> 5) == 1) { const unsigned v = __builtin_nontemporal_load((const GAS unsigned*)a); asm volatile("" :: "v"(v)); }
  };
  until(K_BASE - K_T1);
  touch(1);
  float tend = K_TEND + 1, base = K_BASE;
  until(K_BASE);
  unsigned had = __hip_atomic_load(ctl + C_LATE, RLX_WG) >> 16;
  lds_set(ctl + C_TEND, (K_TEND + 1) << 12 | had);
#pragma nounroll
  for (int c = 0; c < K_NCP; ++c) {
    const int t = K_CP0 + c * K_CP;
    if (c == K_TC2) touch(2);
    if (c == K_TC3) touch(3);
    if (c == K_TC4) touch(4);
    if (until(t) > t + K_CPD / 2) continue;
    const unsigned lw = __hip_atomic_load(ctl + C_LATE, RLX_WG);
    if (bid < K_NS && lane == 0) st8(rw, W_PC + c * 512 + bid * 8, pack2((float)(lw & 0xffffu), tend + (float)((lw >> 16) - had), tag));
    until(t + K_CPD);
    const v2u g = __builtin_amdgcn_raw_buffer_load_b64(rw, W_PC + c * 512 + lane * 8, 0, 16);
    const bool ok = lane < K_NS && __builtin_amdgcn_perm(g.y, g.x, 0x0c0c0400u) == (tag & 0xffffu) && val(g.x) < 4096.f && val(g.y) > K_TEND && val(g.y) < K_TEND + 4096;
    const float n = (float)__builtin_popcountll(__builtin_amdgcn_ballot_w64(ok));
    if (n < K_NS * 0.75f) continue;
    tend = floorf(wave_sum(ok ? val(g.y) : 0.f) / n + 0.5f);
    had = lw >> 16;
    const float lam = ok ? val(g.x) : 0.f, inv = 1.f / n;
    const float late = fmaxf(wave_sum(lam) * inv - K_DEAD, K_XW * wave_sum(fmaxf(lam - K_L0, 0.f)) * inv - K_XD);
    const float step = fminf(K_STEP, late * fmaxf(c >= K_CL ? K_GL : K_GMIN, K_GAIN * (tend - t) / fmaxf(t - base, K_FLOOR)));
    if (step > K_MIN) { tend = floorf(tend + step); base = t + K_CPD; }
    lds_set(ctl + C_TEND, (unsigned)rfl((int)tend) << 12 | had);
    if (bid == 0) { ((GAS float*)(p.ws + W_ST + (512 << 10)))[c * 2] = late; ((GAS float*)(p.ws + W_ST + (512 << 10)))[c * 2 + 1] = tend - K_TEND; }
  }
}

__global__ void __launch_bounds__(NTHR, 3) fused_decode(Params p) {
  extern __shared__ __attribute__((aligned(16))) unsigned char smem[];
  const u64 tentry = __builtin_amdgcn_s_memrealtime();
  LAS unsigned char* L = (LAS unsigned char*)smem;
  const int tid = threadIdx.x, lane = tid & 63, wave = rfl(tid >> 6);
  if (tid < 80) ((LAS unsigned*)(L + L_CTL))[tid] = tid == C_TEND ? K_TEND << 12 : 0u;
  Early e;
  if (wave == NC + 1) early_loads(p, e, lane);
  int role = wave;
  asm volatile("" : "+s"(role));
  if (role > 0) {
    asm volatile("s_waitcnt lgkmcnt(0)\n\ts_barrier" ::: "memory");
    if (wave <= NC) run_consumer(p, L, lane, wave - 1);
    else if (wave == NC + 1) run_helper(p, L, lane, e);
    else run_pacer(p, L, lane, tentry);
  } else {
    run_loader(p, L, lane, tentry);
  }
}

}

namespace {
__global__ void k_rep(Params p) {
  const u64 now = __builtin_amdgcn_s_memrealtime();
  const GAS u64* st = (const GAS u64*)(p.ws + W_ST);
  const int l = threadIdx.x;
  u64 t0 = ~0ull, mx = 0, sum = 0;
  for (int i = l; i < 256; i += 64) { const u64 d = st[i * 32]; t0 = d < t0 ? d : t0; }
  for (int o = 32; o; o >>= 1) { const u64 b = __shfl_xor(t0, o); t0 = b < t0 ? b : t0; }
  for (int i = l; i < NWGS; i += 64) { const u64 d = st[i * 32 + IDX] - t0; mx = d > mx ? d : mx; sum += d; }
  for (int o = 32; o; o >>= 1) { const u64 b = __shfl_xor(mx, o); mx = b > mx ? b : mx; sum += __shfl_xor(sum, o); }
  u64 v = MODE == 0 ? mx : MODE == 1 ? sum / NWGS : MODE == 2 ? now - t0 : (u64)(((const GAS float*)(p.ws + W_ST + (512 << 10)))[PIDX] + 100.f);
  if (v > 20000) v = 20000;
  while (__builtin_amdgcn_s_memrealtime() - now < 1000 + SCALE * v) __builtin_amdgcn_s_sleep(8);
}
}
extern "C" void kernel_launch(void* const* d_in, const int* in_sizes, int n_in, void* d_out, int out_size, void* d_ws,
                              size_t ws_size, hipStream_t stream) {
  Params p;
  p.x = (const unsigned short*)d_in[0];
  p.k = (const unsigned short*)d_in[1];
  p.v = (const unsigned short*)d_in[2];
  p.wq = (const unsigned short*)d_in[3];
  p.wo = (const unsigned short*)d_in[4];
  p.wr = (const unsigned short*)d_in[6];
  p.wg = (const unsigned short*)d_in[7];
  p.wu = (const unsigned short*)d_in[8];
  p.wd = (const unsigned short*)d_in[9];
  p.out = (unsigned short*)d_out;
  p.ws = (unsigned char*)d_ws;
  (void)hipFuncSetAttribute((const void*)fused_decode, hipFuncAttributeMaxDynamicSharedMemorySize, L_END);
  fused_decode<<<NWG, NTHR, L_END, stream>>>(p);
  k_rep<<<1, 64, 0, stream>>>(p);
}
```

```cpp
#include <hip/hip_runtime.h>
#include <hip/hip_bf16.h>

namespace {

#define GAS __attribute__((address_space(1)))
#define LAS __attribute__((address_space(3)))
#define RLX_AGENT __ATOMIC_RELAXED, __HIP_MEMORY_SCOPE_AGENT
#define RLX_WG __ATOMIC_RELAXED, __HIP_MEMORY_SCOPE_WORKGROUP
typedef unsigned v4u __attribute__((ext_vector_type(4)));
typedef unsigned v2u __attribute__((ext_vector_type(2)));
typedef float v4f __attribute__((ext_vector_type(4)));
typedef float v2f __attribute__((ext_vector_type(2)));
typedef short v4s __attribute__((ext_vector_type(4)));
typedef __bf16 bf16x2 __attribute__((ext_vector_type(2)));
typedef __bf16 bf16x8 __attribute__((ext_vector_type(8)));
typedef unsigned long long u64;

#define K_INF 21
#define K_NSLOT 24
#define K_A1 10
#define K_C1 1
#define K_WMLP 33
#define K_HROT 1
#define K_XMAP 0
#define K_STR 21
#define K_ROT 1
#define K_FEW 2
#define K_SL1 8
#define K_SL2 8
#define K_TEND 5380
#define K_LATE 40
#define K_WB 33
#define K_D1 12
#define K_D2 12
#define K_NCP 14
#define K_CP0 1625
#define K_CP 275
#define K_CPD 125
#define K_L0 60.f
#define K_GMIN 3.f
#define K_STEP 200.f
#define K_KEEP 0
#define K_XW 2.f
#define K_XD 6.f
#define K_BASE 1200
#define K_FLOOR 600.f
#define K_MIN 8.f
#define K_STALL 160
#define K_GAIN 0.6f
#define K_DEAD 50.f
#define K_R0 9
#define K_TOUCH 1
#define K_T1 300
#define K_TC2 3
#define K_TC3 8
#define K_TC4 12
#define K_EARLY0 0
#define K_CL 9
#define K_GL 5.f
#define K_NEND 0
#define K_LATE2 15
#define K_WWO K_INF
#define K_NS 32
#define K_YLATE 2
#define K_QWAIT 22

constexpr int DM = 640, NH = 32, DH = 192, SK = 8192, FF = 1536;
constexpr int NWG = 256, NC = 6, NTHR = (NC + 3) * 64;
constexpr int INF = K_INF;
constexpr int NSLOT = K_NSLOT;
constexpr int SLOTB = 6144, RINGB = NSLOT * SLOTB, NWO = 5, NS_WQ = 7, NS_MLP = 40;
constexpr unsigned KV_BYTES = 2u * NH * SK * DH * 2u;
constexpr int SLAB = SK * DH * 2;
constexpr int NMLP = FF / 64;
constexpr int NFIN = DM / 8;
static_assert(NSLOT == 24, "the MLP images rely on where the ring wraps");
static_assert(INF >= 7 && INF <= 30 && K_WB <= 60, "vmcnt is 6 bits; the last K/V slots are published while wo is requested");

constexpr int L_XP = NSLOT * SLOTB;
constexpr int L_REC = L_XP + 2560;
constexpr int L_QB = L_REC + 4608;
constexpr int L_OA = L_QB + 768;
constexpr int L_HM = L_OA + 768;
constexpr int L_P = L_HM + 256;
constexpr int L_MLW = L_P + 3072;
constexpr int L_F = L_MLW + 128;
constexpr int L_G = L_F + 64;
constexpr int L_CTL = L_G + 768;
constexpr int L_END = L_CTL + 320;
static_assert(L_END <= 160 * 1024 && L_END > 80 * 1024, "one workgroup per CU");
enum { C_NEXT = 0, C_HM = 16, C_ML = 24, C_R = 32, C_T3 = 48, C_O = 56, C_LANDED = 64, C_XOK, C_TAG, C_Q, C_F, C_RFREE, C_G, C_PUB, C_LATE, C_TEND };

constexpr int W_EPOCH = 0;
constexpr int W_ML = 1024;
constexpr int W_OP = W_ML + 2 * NH * 8 * 16;
constexpr int W_SP = W_OP + 2 * NH * 8 * 768;
constexpr int W_YP = W_SP + NH * DM * 8;
constexpr int W_QP = W_YP + NMLP * DM * 8;
constexpr int W_PC = W_QP + NH * 2 * DH * 8;
constexpr int W_END = W_PC + 24 * 512;

constexpr unsigned LDS_SPIN = 1u << 20, GLB_SPIN = 1u << 16;
constexpr float LOG2E = 1.4426950408889634f;

struct Params {
  const unsigned short *x, *k, *v, *wq, *wo, *wr, *wg, *wu, *wd;
  unsigned short* out;
  unsigned char* ws;
};

__device__ __forceinline__ int rfl(int v) { return __builtin_amdgcn_readfirstlane(v); }
__device__ __forceinline__ float bfr(float x) { return (float)(__bf16)x; }
__device__ __forceinline__ unsigned bfbits(float x) { return (unsigned)__builtin_bit_cast(unsigned short, (__bf16)x); }
__device__ __forceinline__ float lo16(unsigned u) { return __builtin_bit_cast(float, u << 16); }
__device__ __forceinline__ float hi16(unsigned u) { return __builtin_bit_cast(float, u & 0xffff0000u); }
__device__ __forceinline__ float asf(unsigned u) { return __builtin_bit_cast(float, u); }
__device__ __forceinline__ unsigned asu(float f) { return __builtin_bit_cast(unsigned, f); }
__device__ __forceinline__ float dot2(unsigned a, unsigned b, float c) {
  return __builtin_amdgcn_fdot2_f32_bf16(__builtin_bit_cast(bf16x2, a), __builtin_bit_cast(bf16x2, b), c, false);
}
template <int CTRL> __device__ __forceinline__ float dpp(float x) {
  return __builtin_bit_cast(float, __builtin_amdgcn_mov_dpp(__builtin_bit_cast(int, x), CTRL, 0xf, 0xf, true));
}
template <int CTRL> __device__ __forceinline__ int dppi(int x) { return __builtin_amdgcn_mov_dpp(x, CTRL, 0xf, 0xf, true); }
constexpr int XOR1 = 0xB1, XOR2 = 0x4E, MIR8 = 0x141, ROR4 = 0x124, ROR8 = 0x128;
__device__ __forceinline__ float fold32(float a, float b) {
  auto s = __builtin_amdgcn_permlane32_swap(asu(a), asu(b), false, false);
  return asf(s[0]) + asf(s[1]);
}
__device__ __forceinline__ float fold16(float a, float b) {
  auto s = __builtin_amdgcn_permlane16_swap(asu(a), asu(b), false, false);
  return asf(s[0]) + asf(s[1]);
}
__device__ __forceinline__ float max_rows(float x) {
  auto s = __builtin_amdgcn_permlane16_swap(asu(x), asu(x), false, false);
  x = fmaxf(asf(s[0]), asf(s[1]));
  auto t = __builtin_amdgcn_permlane32_swap(asu(x), asu(x), false, false);
  return fmaxf(asf(t[0]), asf(t[1]));
}
__device__ __forceinline__ float sum_rows(float x) { x = fold16(x, x); return fold32(x, x); }
__device__ __forceinline__ float wave_sum(float x) {
  x += dpp<XOR1>(x); x += dpp<XOR2>(x); x += dpp<ROR4>(x); x += dpp<ROR8>(x);
  return sum_rows(x);
}
__device__ __forceinline__ float exp2f_(float x) { return __builtin_amdgcn_exp2f(x); }
__device__ __forceinline__ float siluf(float x) { return x / (1.0f + expf(-x)); }
#define CBAR() asm volatile("" ::: "memory")

__device__ __forceinline__ __amdgpu_buffer_rsrc_t rsrc(const void* p, unsigned bytes) {
  return __builtin_amdgcn_make_buffer_rsrc((void*)p, 0, bytes, 0x00020000);
}
__device__ __forceinline__ void lds_set(LAS unsigned* w, unsigned v) {
  CBAR();
  __hip_atomic_store(w, v, RLX_WG);
  CBAR();
}
template <int SLEEP> __device__ __forceinline__ void lds_wait(LAS unsigned* w, unsigned want) {
#pragma nounroll
  for (unsigned n = 0; n < LDS_SPIN; ++n) {
    if ((unsigned)rfl((int)__hip_atomic_load(w, RLX_WG)) >= want) break;
    if (SLEEP) __builtin_amdgcn_s_sleep(SLEEP);
  }
  CBAR();
}
template <int N, int SLEEP> __device__ __forceinline__ void lds_wait_all(LAS unsigned* w, int j, unsigned want = 1u) {
  LAS unsigned* a = w + (j < N ? j : 0);
#pragma nounroll
  for (unsigned n = 0; n < LDS_SPIN; ++n) {
    if (__all(__hip_atomic_load(a, RLX_WG) >= want)) break;
    if (SLEEP) __builtin_amdgcn_s_sleep(SLEEP);
  }
  CBAR();
}
__device__ __forceinline__ unsigned tag_of(unsigned e) { return ((e & 0xffu) | (((e >> 8) ^ e) & 0xffu) << 8) * 0x10001u; }
__device__ __forceinline__ v2u pack2(float a, float b, unsigned tag) {
  return v2u{__builtin_amdgcn_perm(asu(a), tag, 0x07060500u), __builtin_amdgcn_perm(asu(b), tag, 0x07060501u)};
}
__device__ __forceinline__ v4u pack4(float a, float b, float c, float d, unsigned tag) {
  const v2u x = pack2(a, b, tag), y = pack2(c, d, tag);
  return v4u{x.x, x.y, y.x, y.y};
}
__device__ __forceinline__ bool fresh(v4u g, unsigned tag) {
  return (__builtin_amdgcn_perm(g.y, g.x, 0x0c0c0400u) | __builtin_amdgcn_perm(g.w, g.z, 0x04000c0cu)) == tag;
}
__device__ __forceinline__ float val(unsigned w) { return asf(w & 0xffffff00u); }
__device__ __forceinline__ v4u ld16(__amdgpu_buffer_rsrc_t rw, int off) { return __builtin_amdgcn_raw_buffer_load_b128(rw, off, 0, 16); }
__device__ __forceinline__ void st16(__amdgpu_buffer_rsrc_t rw, int off, v4u v) { __builtin_amdgcn_raw_buffer_store_b128(v, rw, off, 0, 16); }
__device__ __forceinline__ void st8(__amdgpu_buffer_rsrc_t rw, int off, v2u v) { __builtin_amdgcn_raw_buffer_store_b64(v, rw, off, 0, 16); }

#define STAMP(i) do { } while (0)
#define STAMPS() do { } while (0)
#define FLUSH(base, n) do { } while (0)

__device__ __forceinline__ void layer_norm(v2u (&xr)[5], LAS unsigned char* L, int lane) {
  v2f x[10];
#pragma unroll
  for (int i = 0; i < 5; ++i) { x[2 * i] = v2f{lo16(xr[i].x), hi16(xr[i].x)}; x[2 * i + 1] = v2f{lo16(xr[i].y), hi16(xr[i].y)}; }
  const float p0 = asf((unsigned)__builtin_amdgcn_readlane((int)asu(x[0].x), 0)), p1 = asf((unsigned)__builtin_amdgcn_readlane((int)asu(x[0].x), 32));
  const float piv = lane < 32 ? p0 : p1;
  v2f s1 = {0.f, 0.f}, s2 = {0.f, 0.f};
#pragma unroll
  for (int i = 0; i < 10; ++i) { const v2f d = x[i] - piv; s1 += d; s2 += d * d; }
  float a = s1.x + s1.y, q = s2.x + s2.y;
  a += dpp<XOR1>(a); q += dpp<XOR1>(q); a += dpp<XOR2>(a); q += dpp<XOR2>(q);
  a += dpp<ROR4>(a); q += dpp<ROR4>(q); a += dpp<ROR8>(a); q += dpp<ROR8>(q);
  a = fold16(a, a) * (1.0f / DM); q = fold16(q, q) * (1.0f / DM);
  const float mb = bfr(piv + a), var = bfr(fmaf(-a, a, q));
  const float den = bfr(__builtin_amdgcn_sqrtf(bfr(var + 1e-5f)));
  const float rcp = __builtin_amdgcn_rcpf(den);
#pragma unroll
  for (int i = 0; i < 5; ++i) {
    unsigned o[2];
#pragma unroll
    for (int e = 0; e < 2; ++e) {
      const v2f t = x[2 * i + e] - mb;
      const v2f d = {bfr(t.x), bfr(t.y)}, q0 = d * rcp;
      const v2f qq = (d - q0 * den) * rcp + q0;
      o[e] = bfbits(qq.x) | (bfbits(qq.y) << 16);
    }
    *(LAS v2u*)(L + L_XP + (lane >> 5) * 1280 + ((lane & 31) + 32 * i) * 8) = v2u{o[0], o[1]};
  }
}

template <int NK, int KSTEP, int HALF>
__device__ __forceinline__ void load_bt(const LAS unsigned char* b, v4u (&f)[NK]) {
#pragma unroll
  for (int ks = 0; ks < NK; ++ks) {
    const v2u lo = __builtin_bit_cast(v2u, __builtin_amdgcn_ds_read_tr16_b64_v4i16((LAS v4s*)(b + ks * KSTEP)));
    const v2u hi = __builtin_bit_cast(v2u, __builtin_amdgcn_ds_read_tr16_b64_v4i16((LAS v4s*)(b + ks * KSTEP + HALF)));
    f[ks] = v4u{lo.x, lo.y, hi.x, hi.y};
  }
}
template <int NK> __device__ __forceinline__ v4f mm2(const LAS unsigned char* a, const v4u (&f)[NK], v4f c = v4f{0.f, 0.f, 0.f, 0.f}) {
  v4u av[NK];
#pragma unroll
  for (int ks = 0; ks < NK; ++ks) av[ks] = *(const LAS v4u*)(a + ks * 64);
#pragma unroll
  for (int ks = 0; ks < NK; ++ks) c = __builtin_amdgcn_mfma_f32_16x16x32_bf16(__builtin_bit_cast(bf16x8, av[ks]), __builtin_bit_cast(bf16x8, f[ks]), c, 0, 0, 0);
  return c;
}

#define DMA(OFF) "s_waitcnt vmcnt(%4)\n\tbuffer_load_dwordx4 %0, %1, %2 offen " OFF "nt lds"
template <int N> __device__ __forceinline__ void dma3(int voff, v4u srd, int soff, unsigned lds) {
  asm volatile("s_mov_b32 m0, %3\n\t" DMA("") "\n\t" DMA("offset:1024 ") "\n\t" DMA("offset:2048 ")
               :: "v"(voff), "s"(srd), "s"(soff), "s"(lds), "i"(N) : "memory");
}
template <int N> __device__ __forceinline__ void dma1(int voff, v4u srd, int soff, unsigned lds) {
  asm volatile("s_mov_b32 m0, %3\n\t" DMA("") :: "v"(voff), "s"(srd), "s"(soff), "s"(lds), "i"(N) : "memory");
}
__device__ __forceinline__ v4u srd_of(const void* ptr, unsigned bytes) {
  const u64 a = (u64)ptr;
  return v4u{(unsigned)a, (unsigned)(a >> 32) & 0xffffu, bytes, 0x00020000u};
}
constexpr int fdiv6(int a) { return a >= 0 ? a / 6 : -((5 - a) / 6); }
constexpr int lag3(int w) { return fdiv6(3 - w); }
constexpr int lag6(int w) { return fdiv6(6 - w); }

constexpr int R0 = K_R0, A1 = K_A1, C1 = K_C1, B1 = 8 * R0 - 6 * A1 - C1;
static_assert(B1 >= A1 && C1 >= 0 && 2 * (B1 + 32 - R0) <= 16 * NC, "member 6 has no prologue; a wave keeps the scores of 16 slots");
struct Plan {
  bool mlp;
  int npro, n1;
  __device__ __forceinline__ Plan(int h, int m) {
    mlp = m == 7 && h < NMLP;
    npro = m < 6 ? NS_WQ : mlp ? NS_MLP : 0;
    n1 = h < NMLP ? (m < 6 ? A1 : m == 6 ? B1 : C1) : R0;
  }
  __device__ __forceinline__ int piece(int i, int b, int h, int m) const {
    if (b == 0 && i < n1 && h < NMLP) return m == 7 ? 6 * A1 + B1 + i : i < A1 ? 7 * i + m : 6 * A1 + i;
    const int rho = b ? i : i - n1 + R0;
    return rho * 8 + ((m + rho * K_ROT) & 7);
  }
  __device__ __forceinline__ int len0() const { return 2 * (n1 + 32 - R0); }
  __device__ __forceinline__ int ntot() const { return npro + 2 * len0() + 128; }
  __device__ __forceinline__ int wo_skip() const { const int r = ntot() % NSLOT; return r + NWO > NSLOT ? NSLOT - r : 0; }
};
__device__ __forceinline__ int piece_off(int pc, int b, int h) { return (b * NH + h) * SLAB + (((pc >> 3) * K_STR + h * K_HROT) & 31) * 98304 + (pc & 7) * 12288; }

struct Loader {
  LAS unsigned* ctl;
  unsigned lbase;
  int s, pos, limit, pub;
  unsigned next_addr;
  u64 stall = 0;
  unsigned t0 = 0;
  int due = 0, pace = 0;
  int now = 0, tend = 0, tnew = K_TEND << 12,
      slast = 0, pmax = 1 << 30, last = 0, shift = 0;
  __device__ __forceinline__ void publish(int n) { pub = max(pub, n); __hip_atomic_store(ctl + C_LANDED, (unsigned)pub, RLX_WG); }
  __device__ __forceinline__ void credit(int last) {
    if (__builtin_expect(last >= limit, 0)) {
#pragma nounroll
      for (unsigned n = 0; n < LDS_SPIN; ++n) {
        int v = (int)__hip_atomic_load((LAS unsigned*)(size_t)next_addr, RLX_WG);
        v = min(v, dppi<XOR1>(v)); v = min(v, dppi<XOR2>(v)); v = min(v, dppi<MIR8>(v));
        limit = rfl(v) + NSLOT;
        if (last < limit) break;
        if (n == 8) { asm volatile("s_waitcnt vmcnt(0)" ::: "memory"); publish(s); }
        __builtin_amdgcn_s_sleep(1);
      }
      CBAR();
    }
  }
  __device__ __forceinline__ unsigned dst() const { return lbase + pos * SLOTB; }
  __device__ __forceinline__ void advance() { ++s; pos = pos + 1 == NSLOT ? 0 : pos + 1; }
  template <int W> __device__ __forceinline__ void rows_w(int voff, v4u srd, int so) {
    int v;
    u64 t;
    asm volatile("ds_read_b32 %0, %1" : "=v"(v) : "v"(next_addr) : "memory");
    dma3<W - 1>(voff, srd, so, dst());
    asm volatile("s_waitcnt lgkmcnt(0)" : "+v"(v));
    tnew = __builtin_amdgcn_readlane(v, 8);
    v = min(v, dppi<XOR1>(v)); v = min(v, dppi<XOR2>(v));
    asm volatile("" : "+v"(v));
    if (lag3(W) == lag6(W)) publish(s + lag3(W));
    dma3<W - 1>(voff, srd, so + 3072, dst() + 3072);
    limit = rfl(min(v, dppi<MIR8>(v))) + NSLOT;
    if (lag3(W) != lag6(W)) publish(s + lag6(W));
    advance();
    dma3<W - 1>(voff, srd, so + 6144, dst());
    if (lag3(W) == lag6(W)) publish(s + lag3(W));
    dma3<W - 1>(voff, srd, so + 9216, dst() + 3072);
    asm volatile("s_memrealtime %0" : "=s"(t));
    if (lag3(W) != lag6(W)) publish(s + lag6(W));
    advance();
    asm volatile("s_waitcnt lgkmcnt(0)" : "+s"(t));
    now = (int)((unsigned)t - t0);
  }
  __device__ __forceinline__ void rows(int voff, v4u srd, int so) {
    credit(s + 1);
    if (__builtin_expect(now - last > K_STALL, 0)) { const int d = now - last - (pace >> 8); shift += d; due += d << 8; stall = last; }
    if (__builtin_expect(tnew != tend, 0)) {
      tend = tnew;
      due = max(due, (now - K_LATE - K_KEEP) << 8);
      pace = min(max((((tend >> 12) + shift - (tend & 0xfff)) * 256 - due) * 2 / (slast - s), 0), pmax);
    }
#pragma nounroll
    for (unsigned n = 0; now < (due >> 8) && n < LDS_SPIN; ++n) now = clock();
    last = now;
    const int late = now - (due >> 8);
    __hip_atomic_store(ctl + C_LATE, (unsigned)(min(late, 0xffff) | min(shift, 0xfff) << 16), RLX_WG);
    due += pace;
    if (late <= (s + K_NEND < slast ? K_LATE : K_LATE2)) rows_w<INF>(voff, srd, so); else rows_w<K_WB>(voff, srd, so);
  }
  __device__ __forceinline__ int clock() const { return (int)((unsigned)__builtin_amdgcn_s_memrealtime() - t0); }
};

template <int NP> __device__ __forceinline__ v2f sum_producers(__amdgpu_buffer_rsrc_t rw, int base, unsigned tag, int lane) {
  const int off = base + (lane >> 2) * (DM * 8) + blockIdx.x * 64 + (lane & 3) * 16;
  const bool two = lane < 4 * (NP - 16);
  v4u a, c;
#pragma nounroll
  for (unsigned n = 0; n < GLB_SPIN; ++n) {
    CBAR();
    a = ld16(rw, off);
    c = ld16(rw, two ? off + 16 * (DM * 8) : off);
    const u64 bad = __builtin_amdgcn_ballot_w64(!(fresh(a, tag) && fresh(c, tag)));
    if (bad == 0) break;
    if (K_SL2 && __builtin_popcountll(bad) > 12) __builtin_amdgcn_s_sleep(K_SL2);
  }
  if (!two) c = v4u{0u, 0u, 0u, 0u};
  float s0 = fold32(val(a.x) + val(c.x), val(a.y) + val(c.y)), s1 = fold32(val(a.z) + val(c.z), val(a.w) + val(c.w));
  s0 = fold16(s0, s0); s1 = fold16(s1, s1);
  s0 += dpp<ROR8>(s0); s1 += dpp<ROR8>(s1);
  return v2f{bfr(s0 + dpp<ROR4>(s0)), bfr(s1 + dpp<ROR4>(s1))};
}

__device__ __forceinline__ void combine(const Params& p, LAS unsigned char* L, int lane, int b) {
  LAS unsigned* ctl = (LAS unsigned*)(L + L_CTL);
  const int h = K_XMAP ? blockIdx.x >> 3 : blockIdx.x & 31, l48 = min(lane, 47);
  const __amdgpu_buffer_rsrc_t rw = rsrc(p.ws, W_END);
  const unsigned tag = (unsigned)rfl((int)ctl[C_TAG]);
  const int off = W_OP + (b * NH + h) * 8 * 768 + l48 * 16;
  v4f g[8];
  v4u mv;
  unsigned miss = 0xffu;
#pragma unroll
  for (int k = 0; k < 8; ++k) g[k] = v4f{0.f, 0.f, 0.f, 0.f};
  if (K_D1) __builtin_amdgcn_s_sleep(K_D1);
#pragma nounroll
  for (unsigned n = 0; n < GLB_SPIN; ++n) {
    v4u t[8];
    CBAR();
    mv = ld16(rw, W_ML + (((K_EARLY0 ? b : 1) * NH + h) * 8 + (lane & 7)) * 16);
#pragma unroll
    for (int k = 0; k < 8; ++k)
      if (miss >> k & 1) t[k] = ld16(rw, off + k * 768);
#pragma unroll
    for (int k = 0; k < 8; ++k)
      if (miss >> k & 1) {
        if (__all(fresh(t[k], tag))) { g[k] = v4f{val(t[k].x), val(t[k].y), val(t[k].z), val(t[k].w)}; miss ^= 1u << k; }
      }
    if (miss == 0u && __all(fresh(mv, tag))) break;
    if (K_SL1 && __builtin_popcount(miss) > K_FEW) __builtin_amdgcn_s_sleep(K_SL1);
  }
  const float Mk = val(b ? mv.z : mv.x);
  float M = Mk;
  M = fmaxf(M, dpp<XOR1>(M)); M = fmaxf(M, dpp<XOR2>(M)); M = fmaxf(M, dpp<MIR8>(M));
  float w = exp2f_(Mk - M), Ls = w * val(b ? mv.w : mv.y);
  Ls += dpp<XOR1>(Ls); Ls += dpp<XOR2>(Ls); Ls += dpp<MIR8>(Ls);
  w *= __builtin_amdgcn_rcpf(Ls);
  v4f o = {0.f, 0.f, 0.f, 0.f};
#pragma unroll
  for (int k = 0; k < 8; ++k) o += asf((unsigned)__builtin_amdgcn_readlane((int)asu(w), k)) * g[k];
  *(LAS v2u*)(L + L_OA + b * (DH * 2) + l48 * 8) = v2u{bfbits(o.x) | (bfbits(o.y) << 16), bfbits(o.z) | (bfbits(o.w) << 16)};
  lds_set(ctl + C_O + b, 1u);
}

__device__ __forceinline__ void run_loader(const Params& p, LAS unsigned char* L, int lane, u64 tentry) {
  LAS unsigned* ctl = (LAS unsigned*)(L + L_CTL);
  const int bid = blockIdx.x, h = K_XMAP ? bid >> 3 : bid & 31, m = K_XMAP ? bid & 7 : bid >> 5;
  const int r = lane >> 3, j = lane & 7;
  __builtin_amdgcn_s_setprio(3);
  STAMPS();
  Loader ld{ctl, (unsigned)(size_t)L, 0, 0, NSLOT, 0, (unsigned)(size_t)(r == 1 ? ctl + C_TEND : ctl + C_NEXT + (j < NC ? j : 0))};
  const int voff = lane * 16;
  if (__builtin_expect(m < 6, 1)) {
    const v4u rq = srd_of(p.wq, DM * NH * DH * 2);
    const int vo = r * (NH * DH * 2) + (j ^ (r & 2)) * 16, so = (m / 3) * 320 * (NH * DH * 2) + h * (DH * 2) + (m % 3) * 128;
#pragma unroll 2
    for (int g = 0; g < 6 * NS_WQ; ++g) dma1<63>(g & 1 ? vo ^ 64 : vo, rq, so + min(g, 39) * (8 * NH * DH * 2), ld.lbase + g * 1024);
    ld.s = ld.pos = NS_WQ;
  }
  asm volatile("s_waitcnt lgkmcnt(0)\n\ts_barrier" ::: "memory");
  STAMP(1);
  const Plan pl(h, m);
  ld.t0 = (unsigned)tentry;
  ld.slast = pl.ntot();
  if (pl.mlp) {
    const v4u rg = srd_of(p.wg, DM * FF * 2), ru = srd_of(p.wu, DM * FF * 2), rd = srd_of(p.wd, FF * DM * 2);
    const int vo = r * (FF * 2) + (j ^ (r & 2)) * 16;
    constexpr int W = K_WMLP;
    const int sd = h * 64 * (DM * 2);
    auto slot = [&](int pc0, int sg) {
#pragma unroll
      for (int q = 0; q < 6; ++q) {
        const int pc = pc0 + q;
        if (pc < 16 || sg >= 0) dma1<W - 1>(q & 1 ? vo ^ 64 : vo, pc & 4 ? ru : rg, (sg < 0 ? 72 : sg) * (8 * FF * 2) + ((pc >> 3) * 4 + (pc & 3)) * (8 * FF * 2) + h * 128, ld.dst() + q * 1024);
        else dma1<W - 1>(voff, rd, sd + (pc - 16) * 1024, ld.dst() + q * 1024);
      }
      ld.publish(ld.s + lag6(W));
      ld.advance();
    };
#pragma nounroll
    for (int t = 0; t < 6; ++t) {
#pragma unroll
      for (int u = 0; u < 4; ++u) slot(6 * u, 12 * t);
    }
#pragma unroll
    for (int u = 0; u < 3; ++u) { ld.credit(ld.s); slot(6 * u, -1); }
#pragma nounroll
    for (int t = 27; t < NS_MLP; ++t) {
      ld.credit(ld.s);
      dma3<W - 1>(voff, rd, sd + (6 * t - 160) * 1024, ld.dst());
      dma3<W - 1>(voff, rd, sd + (6 * t - 157) * 1024, ld.dst() + 3072);
      ld.publish(ld.s + lag6(W));
      ld.advance();
    }
  }
  STAMP(2);
  ld.now = ld.clock();
  ld.last = ld.now;
  ld.due = ld.now << 8;
  ld.tend = K_TEND << 12;
  ld.pace = (K_TEND * 256 - ld.due) * 2 / (ld.slast - ld.s);
  ld.pmax = ld.pace + (ld.pace >> 3);
#pragma nounroll
  for (int ph = 0; ph < 4; ++ph) {
    const v4u rs = srd_of(ph & 1 ? p.v : p.k, KV_BYTES);
    const int b = ph >> 1, np = b ? 32 : pl.len0() / 2;
#pragma nounroll
    for (int i = 0; i < np; ++i) ld.rows(voff, rs, piece_off(pl.piece(i, b, h, m), b, h));
  }
  STAMP(3);
  const v4u ro = srd_of(p.wo, NH * DH * DM * 2);
  int row = lane / 10, ch = lane - row * 10;
  ld.credit(ld.s + pl.wo_skip() + NWO - 1);
  if (pl.wo_skip()) ld.pos = 0;
#pragma nounroll
  for (int t = 0; t < NWO; ++t) {
#pragma unroll
    for (int q = 0; q < 6; ++q) {
      dma1<K_WWO - 1>(row * (DM * 2) + ch * 16, ro, h * DH * (DM * 2) + m * 160, ld.dst() + q * 1024);
      if (q == 2 && lag3(K_WWO) == lag6(K_WWO)) ld.publish(ld.s + lag3(K_WWO));
      if (q == 5 && lag3(K_WWO) != lag6(K_WWO)) ld.publish(ld.s + lag6(K_WWO));
      row += 6; ch += 4;
      if (ch >= 10) { ch -= 10; row += 1; }
    }
    ld.advance();
  }
  if (K_WWO > 48) { asm volatile("s_waitcnt vmcnt(48)" ::: "memory"); ld.publish(ld.s - 8); }
  if (K_WWO > 42) { asm volatile("s_waitcnt vmcnt(42)" ::: "memory"); ld.publish(ld.s - 7); }
  if (K_WWO > 36) { asm volatile("s_waitcnt vmcnt(36)" ::: "memory"); ld.publish(ld.s - 6); }
  if (K_WWO > 30) { asm volatile("s_waitcnt vmcnt(30)" ::: "memory"); ld.publish(ld.s - 5); }
  asm volatile("s_waitcnt vmcnt(0)" ::: "memory");
  ld.publish(ld.s);
  STAMP(4);
  if (bid < NFIN) {
    const v2f y = sum_producers<NMLP>(rsrc(p.ws, W_END), W_YP, (unsigned)rfl((int)ctl[C_TAG]), lane);
    const float sg = *(const LAS float*)(L + L_G + lane * 4);
    const unsigned xn = *(const LAS unsigned*)(L + L_XP + (lane >> 5) * 1280 + bid * 16 + (lane & 3) * 4);
    *(LAS v2f*)(L + L_G + 256 + lane * 8) = v2f{bfr(siluf(bfr(lo16(xn) + bfr(y.x * sg)))), bfr(siluf(bfr(hi16(xn) + bfr(y.y * sg))))};
    lds_set(ctl + C_G, 1u);
  }
  FLUSH(0, 8);
}

struct Early { v2u xr[5], wr[5]; unsigned e0, xo; };
__device__ __forceinline__ void early_loads(const Params& p, Early& e, int lane) {
#pragma unroll
  for (int i = 0; i < 5; ++i) e.xr[i] = ((const GAS v2u*)p.x)[(lane >> 5) * 160 + (lane & 31) + 32 * i];
  e.e0 = __hip_atomic_load((GAS unsigned*)(p.ws + W_EPOCH), RLX_AGENT);
}
__device__ __forceinline__ void run_helper(const Params& p, LAS unsigned char* L, int lane, Early& e) {
  LAS unsigned* ctl = (LAS unsigned*)(L + L_CTL);
  const int bid = blockIdx.x, h = K_XMAP ? bid >> 3 : bid & 31, m = K_XMAP ? bid & 7 : bid >> 5;
  const int j = lane & 7;
  const __amdgpu_buffer_rsrc_t rw = rsrc(p.ws, W_END);
  STAMPS();
  const int fb = lane >> 5, col = min(bid, NFIN - 1) * 8 + 2 * (lane & 3), l48 = min(lane, 47);
#pragma unroll
  for (int i = 0; i < 5; ++i) e.wr[i] = ((const GAS v2u*)p.wr)[(lane & 31) + 32 * i];
  e.xo = ((const GAS unsigned*)p.x)[(fb * DM + col) >> 1];
  __builtin_amdgcn_sched_barrier(0);
  layer_norm(e.xr, L, lane);
  const unsigned epoch = (unsigned)rfl((int)e.e0) + 1u, tag = tag_of(epoch);
  ctl[C_TAG] = tag;
  lds_set(ctl + C_XOK, 1u);
  STAMP(0);
  {
    lds_wait<2>(ctl + C_LANDED, K_QWAIT);
    const int off = W_QP + (h * 2 * DH + 4 * l48) * 8;
    v4u g[4];
#pragma nounroll
    for (unsigned n = 0; n < GLB_SPIN; ++n) {
      CBAR();
#pragma unroll
      for (int i = 0; i < 4; ++i) g[i] = ld16(rw, off + (i & 1) * 16 + (i >> 1) * (DH * 8));
      if (__all(fresh(g[0], tag) && fresh(g[1], tag) && fresh(g[2], tag) && fresh(g[3], tag))) break;
    }
#pragma unroll
    for (int b = 0; b < 2; ++b)
      *(LAS v2u*)(L + L_QB + b * (DH * 2) + l48 * 8) = v2u{bfbits(val(g[0][b]) + val(g[2][b])) | (bfbits(val(g[0][2 + b]) + val(g[2][2 + b])) << 16),
                                                            bfbits(val(g[1][b]) + val(g[3][b])) | (bfbits(val(g[1][2 + b]) + val(g[3][2 + b])) << 16)};
    lds_set(ctl + C_Q, 1u);
  }
  float rs = 0.f;
#pragma unroll
  for (int i = 0; i < 5; ++i) {
    const v2u xx = *(const LAS v2u*)(L + L_XP + fb * 1280 + ((lane & 31) + 32 * i) * 8);
    rs = dot2(e.wr[i].y, xx.y, dot2(e.wr[i].x, xx.x, rs));
  }
  rs += dpp<XOR1>(rs); rs += dpp<XOR2>(rs); rs += dpp<ROR4>(rs); rs += dpp<ROR8>(rs);
  *(LAS float*)(L + L_G + lane * 4) = bfr(1.0f / (1.0f + expf(-bfr(fold16(rs, rs)))));
  v4u po[2], pml;
  v2f st2[2];
#pragma unroll
  for (int b = 0; b < 2; ++b) {
    lds_wait_all<NC, 16>(ctl + C_ML, j, b + 1);
    v2f ml = *(const LAS v2f*)(L + L_MLW + b * 64 + j * 8);
    if (j >= NC) ml = v2f{-3.0e38f, 0.f};
    float M = ml.x;
    M = fmaxf(M, dpp<XOR1>(M)); M = fmaxf(M, dpp<XOR2>(M)); M = fmaxf(M, dpp<MIR8>(M));
    M = val(asu(M));
    const float w = exp2f_(ml.x - M);
    *(LAS float*)(L + L_F + b * 32 + j * 4) = w;
    lds_set(ctl + C_F, b + 1);
    float Ls = w * ml.y;
    Ls += dpp<XOR1>(Ls); Ls += dpp<XOR2>(Ls); Ls += dpp<MIR8>(Ls);
    st2[b] = v2f{M, Ls};
    if (b == 0) {
      lds_wait_all<NC, 16>(ctl + C_R, j, 1);
    } else {
      if (K_EARLY0) combine(p, L, lane, 0);
      pml = pack4(st2[0].x, st2[0].y, st2[1].x, st2[1].y, tag);
      asm volatile("" : "+v"(pml), "+v"(po[0]));
      lds_wait_all<NC, 0>(ctl + C_R, j, 2);
    }
    STAMP(1);
    v4f o = {0.f, 0.f, 0.f, 0.f};
#pragma unroll
    for (int c = 0; c < NC; ++c) o += *(const LAS v4f*)(L + L_REC + c * 768 + l48 * 16);
    po[b] = pack4(o.x, o.y, o.z, o.w, tag);
    if (b == 0) {
      lds_set(ctl + C_RFREE, 1u);
      if (K_EARLY0) {
        if (lane < 48) st16(rw, W_OP + (h * 8 + m) * 768 + lane * 16, po[0]);
        if (lane == 0) st16(rw, W_ML + (h * 8 + m) * 16, pack4(M, Ls, M, Ls, tag));
      }
    }
  }
  if (lane < 48) {
    st16(rw, W_OP + ((NH + h) * 8 + m) * 768 + lane * 16, po[1]);
    if (!K_EARLY0) st16(rw, W_OP + (h * 8 + m) * 768 + lane * 16, po[0]);
  }
  if (lane == 0) st16(rw, W_ML + ((NH + h) * 8 + m) * 16, pml);
  lds_set(ctl + C_PUB, 1u);
  STAMP(2);
  combine(p, L, lane, 1);
  STAMP(4);
  if (bid < NFIN) {
    lds_wait_all<5, 0>(ctl + C_T3, j);
    lds_wait<0>(ctl + C_G, 1);
    const v2f gt = *(const LAS v2f*)(L + L_G + 256 + lane * 8);
    STAMP(5);
    if (K_D2) __builtin_amdgcn_s_sleep(K_D2);
    const v2f sv = sum_producers<NH>(rw, W_SP, tag, lane);
    STAMP(6);
    const unsigned ov = bfbits(lo16(e.xo) + bfr(sv.x * gt.x)) | (bfbits(hi16(e.xo) + bfr(sv.y * gt.y)) << 16);
    if ((lane & 31) < 4) ((GAS unsigned*)p.out)[(fb * DM + col) >> 1] = ov;
    if (bid == 0 && lane == 0) __hip_atomic_store((GAS unsigned*)(p.ws + W_EPOCH), epoch, RLX_AGENT);
    STAMP(7);
  }
  FLUSH(8, bid < NFIN ? 8 : 5);
}

__device__ __forceinline__ void run_consumer(const Params& p, LAS unsigned char* L, int lane, int c) {
  LAS unsigned* ctl = (LAS unsigned*)(L + L_CTL);
  const int bid = blockIdx.x, h = K_XMAP ? bid >> 3 : bid & 31, m = K_XMAP ? bid & 7 : bid >> 5;
  const int r = lane >> 3, j = lane & 7;
  const Plan pl(h, m);
  const __amdgpu_buffer_rsrc_t rw = rsrc(p.ws, W_END);
  STAMPS();
  if (pl.npro == 0 || (m < 6 && c >= 4)) lds_set(ctl + C_NEXT + c, pl.npro + c);
  lds_wait<1>(ctl + C_XOK, 1);
  const unsigned tag = (unsigned)rfl((int)ctl[C_TAG]);
  const int g = lane >> 4;
  const int bq = (lane >> 4) * 1024 + ((lane & 15) >> 2) * 128 + ((2 * c + ((lane & 3) >> 1)) ^ (2 * (r & 3))) * 16 + (lane & 1) * 8;
  v2u yp[7] = {};
  auto put_y = [&]() {
#pragma unroll
    for (int k = 0; k < 7; ++k)
      if (lane < 16 && c + NC * k < DM / 16) st8(rw, W_YP + (h * DM + (c + NC * k) * 16 + lane) * 8, yp[k]);
  };
  if (m < 6) {
    if (c < 4) {
      lds_wait<0>(ctl + C_LANDED, NS_WQ);
      v4u bf[10];
      load_bt<10, 4096, 512>(L + bq, bf);
      lds_set(ctl + C_NEXT + c, NS_WQ + c);
      const v4f qv = mm2<10>(L + L_XP + (lane & 1) * 1280 + (m / 3) * 640 + (lane >> 4) * 16, bf);
      if (lane < 16) st8(rw, W_QP + ((h * 2 + m / 3) * DH + (m % 3) * 64 + 16 * c + lane) * 8, pack2(qv.x, qv.y, tag));
    }
  } else if (pl.mlp) {
    if (c < 4) {
      const LAS unsigned char* ap = L + L_XP + (lane & 1) * 1280 + g * 16;
      v4f ga = {0.f, 0.f, 0.f, 0.f}, ua = {0.f, 0.f, 0.f, 0.f};
#pragma nounroll
      for (int t = 0; t < 20; t += 2) {
        lds_wait<1>(ctl + C_LANDED, (8 * t + 21) / 6);
        v4u gf[2], uf[2];
        load_bt<2, 8192, 512>(L + (t < 18 ? t : t - 18) * 8192 + bq, gf);
        load_bt<2, 8192, 512>(L + (t < 18 ? t : t - 18) * 8192 + 4096 + bq, uf);
        lds_set(ctl + C_NEXT + c, (8 * t + 16) / 6);
        ga = mm2<2>(ap + t * 64, gf, ga);
        ua = mm2<2>(ap + t * 64, uf, ua);
      }
      if (lane < 32) *(LAS unsigned short*)(L + L_HM + g * 128 + (16 * c + (lane & 15)) * 2) = (unsigned short)bfbits(bfr(siluf(bfr(g ? ga[1] : ga[0]))) * bfr(g ? ua[1] : ua[0]));
      lds_set(ctl + C_HM + c, 1u);
    } else {
      lds_set(ctl + C_NEXT + c, 160 / 6);
    }
    lds_wait_all<4, 1>(ctl + C_HM, j);
    lds_wait<1>(ctl + C_LANDED, NS_MLP);
#pragma unroll
    for (int k = 0; k < 7; ++k) {
      const int n = min(c + NC * k, DM / 16 - 1);
      v4u bf[2];
      load_bt<2, 32 * 1280, 4 * 1280>(L + (160 * 1024 - RINGB) + (8 * g + ((lane & 15) >> 2)) * 1280 + n * 32 + (lane & 3) * 8, bf);
      const v4f y = mm2<2>(L + L_HM + (lane & 1) * 128 + g * 16, bf);
      yp[k] = pack2(y[0], y[1], tag);
    }
    if (!K_YLATE) put_y();
    lds_set(ctl + C_NEXT + c, NS_MLP + c);
  }
  STAMP(0);
  const int ntot = pl.ntot();
  int s = pl.npro + c;
  auto take = [&](int i, int len) -> const LAS unsigned char* {
    lds_wait<1>(ctl + C_LANDED, s + 1);
    const LAS unsigned char* sp = L + s % NSLOT * SLOTB;
    s += i + NC < len ? NC : len - i + c;
    return sp;
  };
  auto fetch = [&](v4u (&kk)[6], int i, int len) {
    const LAS unsigned char* sp = take(i, len) + r * 384 + j * 16;
#pragma unroll
    for (int u = 0; u < 6; ++u) kk[u] = *(const LAS v4u*)(sp + (u / 3) * 3072 + (u % 3) * 128);
    lds_set(ctl + C_NEXT + c, s);
  };
  v4u e0[6], e1[6];
  fetch(e0, c, pl.len0());
  fetch(e1, c + NC, pl.len0());
  lds_wait<1>(ctl + C_Q, 1);
  STAMP(1);
  LAS unsigned char* pw = L + L_P + c * 512;
#pragma nounroll
  for (int b = 0; b < 2; ++b) {
    const int len = b ? 64 : pl.len0();
    v4u q[3];
#pragma unroll
    for (int u = 0; u < 3; ++u) q[u] = *(const LAS v4u*)(L + L_QB + (b * DH + (j + 8 * u) * 8) * 2);
    float sc[4] = {-3.0e38f, -3.0e38f, -3.0e38f, -3.0e38f};
    auto score = [&](const v4u (&kk)[6], int it) {
      float d0 = 0.f, d1 = 0.f;
#pragma unroll
      for (int u = 0; u < 3; ++u)
#pragma unroll
        for (int e = 0; e < 4; ++e) { d0 = dot2(kk[u][e], q[u][e], d0); d1 = dot2(kk[3 + u][e], q[u][e], d1); }
      d0 += dpp<XOR1>(d0); d1 += dpp<XOR1>(d1); d0 += dpp<XOR2>(d0); d1 += dpp<XOR2>(d1);
      const float d = (j & 4 ? d1 : d0) + dpp<MIR8>(j & 4 ? d0 : d1);
      const float t = bfr(bfr(d) * 0.07216878364870322f) * LOG2E;
#pragma unroll
      for (int k = 0; k < 4; ++k) sc[k] = it == 4 * k + (j & 3) ? t : sc[k];
    };
    int i = c, it = 0;
    if (b == 0) { score(e0, 0); score(e1, 1); i += 2 * NC; it = 2; }
#pragma nounroll
    for (; i < len; i += NC, ++it) {
      v4u kk[6];
      fetch(kk, i, len);
      score(kk, it);
    }
    float M = fmaxf(fmaxf(sc[0], sc[1]), fmaxf(sc[2], sc[3]));
    M = fmaxf(M, dpp<XOR1>(M)); M = fmaxf(M, dpp<XOR2>(M)); M = fmaxf(M, dpp<ROR4>(M)); M = fmaxf(M, dpp<ROR8>(M));
    M = max_rows(M);
    float ls = 0.f;
#pragma unroll
    for (int k = 0; k < 4; ++k) {
      const float pr = exp2f_(sc[k] - M);
      ls += pr;
      *(LAS unsigned short*)(pw + k * 128 + (j & 3) * 32 + (r + 8 * (j >> 2)) * 2) = (unsigned short)bfbits(pr);
    }
    ls = wave_sum(ls);
    *(LAS v2f*)(L + L_MLW + b * 64 + c * 8) = v2f{M, ls};
    lds_set(ctl + C_ML + c, b + 1);
    v4f acc[12];
#pragma unroll
    for (int t = 0; t < 12; ++t) acc[t] = v4f{0.f, 0.f, 0.f, 0.f};
    float f = 0.f;
#pragma nounroll
    for (i = c, it = 0; i < len; i += NC, ++it) {
      if (K_YLATE && b && pl.mlp && i + K_YLATE * NC >= len && i + (K_YLATE - 1) * NC < len) put_y();
      if (i + NC >= len) {
        lds_wait<1>(ctl + C_F, b + 1);
        if (b) lds_wait<1>(ctl + C_RFREE, 1);
        f = *(const LAS float*)(L + L_F + b * 32 + c * 4);
      }
      const LAS unsigned char* sp = take(i, len) + (lane >> 2) * 384 + (lane & 3) * 8;
      const v4s pa = *(const LAS v4s*)(pw + it * 32 + g * 8);
      v4s vf[12];
#pragma unroll
      for (int t = 0; t < 12; ++t) vf[t] = __builtin_amdgcn_ds_read_tr16_b64_v4i16((LAS v4s*)(sp + t * 32));
      lds_set(ctl + C_NEXT + c, s);
#pragma unroll
      for (int t = 0; t < 12; ++t) acc[t] = __builtin_amdgcn_mfma_f32_16x16x16bf16_1k(pa, vf[t], acc[t], 0, 0, 0);
    }
    if (b) STAMP(3); else STAMP(2);
#pragma unroll
    for (int k = 0; k < 3; ++k) {
      const float v = g & 2 ? (g & 1 ? acc[9 + k][0] : acc[6 + k][0]) : (g & 1 ? acc[3 + k][0] : acc[k][0]);
      *(LAS float*)(L + L_REC + c * 768 + (g * 48 + k * 16 + (lane & 15)) * 4) = v * f;
    }
    lds_set(ctl + C_R + c, b + 1);
  }
  STAMP(4);
  if (c == 5) {
    if (K_EARLY0) return;
    lds_wait<0>(ctl + C_PUB, 1);
    combine(p, L, lane, 0);
    return;
  }
  lds_wait<0>(ctl + C_LANDED, ntot + NWO);
  v4u bf[6];
  load_bt<6, 32 * 160, 4 * 160>(L + (ntot + pl.wo_skip()) % NSLOT * SLOTB + ((lane >> 4) * 8 + ((lane & 15) >> 2)) * 160 + c * 32 + (lane & 3) * 8, bf);
  STAMP(5);
  lds_wait_all<2, 0>(ctl + C_O, j);
  STAMP(6);
  const v4f sv = mm2<6>(L + L_OA + (lane & 1) * (DH * 2) + (lane >> 4) * 16, bf);
  if (lane < 16) st8(rw, W_SP + (h * DM + m * 80 + c * 16 + lane) * 8, pack2(sv.x, sv.y, tag));
  lds_set(ctl + C_T3 + c, 1u);
  STAMP(7);
  if (c == 0) FLUSH(16, 8);
  if (c == 4) FLUSH(24, 8);
}

__device__ __forceinline__ void run_pacer(const Params& p, LAS unsigned char* L, int lane, u64 tentry) {
  LAS unsigned* ctl = (LAS unsigned*)(L + L_CTL);
  const int bid = blockIdx.x;
  const __amdgpu_buffer_rsrc_t rw = rsrc(p.ws, W_END);
  auto until = [&](int t) {
    int now = 0;
#pragma nounroll
    for (unsigned n = 0; n < LDS_SPIN; ++n) {
      now = (int)((unsigned)__builtin_amdgcn_s_memrealtime() - (unsigned)tentry);
      if (now >= t) break;
      __builtin_amdgcn_s_sleep(3);
    }
    return now;
  };
  lds_wait<16>(ctl + C_XOK, 1);
  const unsigned tag = (unsigned)rfl((int)ctl[C_TAG]);
  const int h = K_XMAP ? bid >> 3 : bid & 31;
  auto touch = [&](int ph) {
    const GAS unsigned char* a = ph < 4 ? (const GAS unsigned char*)(ph & 1 ? p.v : p.k) + (size_t)((ph >> 1) * NH + h) * SLAB + min(lane << 20, SLAB - 128)
                                        : (const GAS unsigned char*)p.wo + (size_t)h * (DH * DM * 2) + (lane & 1) * (DH * DM * 2 - 128);
    if (K_TOUCH && lane < 4 && (bid >> 5) == 1) { const unsigned v = __builtin_nontemporal_load((const GAS unsigned*)a); asm volatile("" :: "v"(v)); }
  };
  until(K_BASE - K_T1);
  touch(1);
  float tend = K_TEND + 1, base = K_BASE;
  until(K_BASE);
  unsigned had = __hip_atomic_load(ctl + C_LATE, RLX_WG) >> 16;
  lds_set(ctl + C_TEND, (K_TEND + 1) << 12 | had);
#pragma nounroll
  for (int c = 0; c < K_NCP; ++c) {
    const int t = K_CP0 + c * K_CP;
    if (c == K_TC2) touch(2);
    if (c == K_TC3) touch(3);
    if (c == K_TC4) touch(4);
    if (until(t) > t + K_CPD / 2) continue;
    const unsigned lw = __hip_atomic_load(ctl + C_LATE, RLX_WG);
    if (bid < K_NS && lane == 0) st8(rw, W_PC + c * 512 + bid * 8, pack2((float)(lw & 0xffffu), tend + (float)((lw >> 16) - had), tag));
    until(t + K_CPD);
    const v2u g = __builtin_amdgcn_raw_buffer_load_b64(rw, W_PC + c * 512 + lane * 8, 0, 16);
    const bool ok = lane < K_NS && __builtin_amdgcn_perm(g.y, g.x, 0x0c0c0400u) == (tag & 0xffffu) && val(g.x) < 4096.f && val(g.y) > K_TEND && val(g.y) < K_TEND + 4096;
    const float n = (float)__builtin_popcountll(__builtin_amdgcn_ballot_w64(ok));
    if (n < K_NS * 0.75f) continue;
    tend = floorf(wave_sum(ok ? val(g.y) : 0.f) / n + 0.5f);
    had = lw >> 16;
    const float lam = ok ? val(g.x) : 0.f, inv = 1.f / n;
    const float late = fmaxf(wave_sum(lam) * inv - K_DEAD, K_XW * wave_sum(fmaxf(lam - K_L0, 0.f)) * inv - K_XD);
    const float step = fminf(K_STEP, late * fmaxf(c >= K_CL ? K_GL : K_GMIN, K_GAIN * (tend - t) / fmaxf(t - base, K_FLOOR)));
    if (step > K_MIN) { tend = floorf(tend + step); base = t + K_CPD; }
    lds_set(ctl + C_TEND, (unsigned)rfl((int)tend) << 12 | had);
  }
}

__global__ void __launch_bounds__(NTHR, 3) fused_decode(Params p) {
  extern __shared__ __attribute__((aligned(16))) unsigned char smem[];
  const u64 tentry = __builtin_amdgcn_s_memrealtime();
  LAS unsigned char* L = (LAS unsigned char*)smem;
  const int tid = threadIdx.x, lane = tid & 63, wave = rfl(tid >> 6);
  if (tid < 80) ((LAS unsigned*)(L + L_CTL))[tid] = tid == C_TEND ? K_TEND << 12 : 0u;
  Early e;
  if (wave == NC + 1) early_loads(p, e, lane);
  int role = wave;
  asm volatile("" : "+s"(role));
  if (role > 0) {
    asm volatile("s_waitcnt lgkmcnt(0)\n\ts_barrier" ::: "memory");
    if (wave <= NC) run_consumer(p, L, lane, wave - 1);
    else if (wave == NC + 1) run_helper(p, L, lane, e);
    else run_pacer(p, L, lane, tentry);
  } else {
    run_loader(p, L, lane, tentry);
  }
}

}

extern "C" void kernel_launch(void* const* d_in, const int* in_sizes, int n_in, void* d_out, int out_size, void* d_ws,
                              size_t ws_size, hipStream_t stream) {
  Params p;
  p.x = (const unsigned short*)d_in[0];
  p.k = (const unsigned short*)d_in[1];
  p.v = (const unsigned short*)d_in[2];
  p.wq = (const unsigned short*)d_in[3];
  p.wo = (const unsigned short*)d_in[4];
  p.wr = (const unsigned short*)d_in[6];
  p.wg = (const unsigned short*)d_in[7];
  p.wu = (const unsigned short*)d_in[8];
  p.wd = (const unsigned short*)d_in[9];
  p.out = (unsigned short*)d_out;
  p.ws = (unsigned char*)d_ws;
  (void)hipFuncSetAttribute((const void*)fused_decode, hipFuncAttributeMaxDynamicSharedMemorySize, L_END);
  fused_decode<<<NWG, NTHR, L_END, stream>>>(p);
}
```
